```python
import torch

def _softmax(x, axis=-1):
    return torch.softmax(x, dim=axis)
def _gelu(x):
    return torch.nn.functional.gelu(x, approximate="tanh")
def _silu(x):
    return torch.nn.functional.silu(x)
def _sigmoid(x):
    return torch.sigmoid(x)
def _layernorm(x, axis=-1, eps=1e-5):
    m = x.mean(dim=axis, keepdim=True)
    v = x.var(dim=axis, keepdim=True, unbiased=False)
    return (x - m) / torch.sqrt(v + eps)
def _scatter_add(tgt, idx, src, axis=0):
    return tgt.clone().index_add_(axis, idx, src)
def _segment_sum(src, seg, n):
    out = torch.zeros((n,) + src.shape[1:], dtype=src.dtype)
    return out.index_add_(0, seg, src)
def _pad(x, pw):
    flat = tuple(v for pair in reversed(pw) for v in pair)
    return torch.nn.functional.pad(x, flat)

kernel_name = 'standalone__mha_block_cached__near_ridge'

def setup_inputs():

    _w = (lambda a: (lambda t: t.to(torch.bfloat16) if t.is_floating_point() else t)(torch.as_tensor(a)))
    B, Sq, D = 8, 256, 448
    H, Sk, Dh = 16, 1024, 256
    return {
        "x": _w((lambda *s: torch.randn(*s, dtype=torch.bfloat16) * 0.8)(B, Sq, D)),
        "k_cache": _w((lambda *s: torch.randn(*s, dtype=torch.bfloat16) * 0.8)(B, H, Sk, Dh)),
        "v_cache": _w((lambda *s: torch.randn(*s, dtype=torch.bfloat16) * 0.05)(B, H, Sk, Dh)),
        "wq": _w((lambda *s: torch.randn(*s, dtype=torch.bfloat16) * 0.8)(D, H * Dh) * (D ** -0.5)),
        "wo": _w((lambda *s: torch.randn(*s, dtype=torch.bfloat16) * 0.05)(H * Dh, D) * ((H * Dh) ** -0.5)),
    }

def reference(x, k_cache, v_cache, wq, wo):
    B, Sq, D = x.shape
    H, Dh = 16, 256
    q = (lambda x, *d: x.permute(*d))((x @ wq).reshape(B, Sq, H, Dh), 0, 2, 1, 3)
    scores = torch.einsum("bhqd,bhkd->bhqk", q, k_cache) * (Dh ** -0.5)
    p = _softmax(scores, axis=-1)
    o = torch.einsum("bhqk,bhkd->bhqd", p, v_cache)
    return (lambda x, *d: x.permute(*d))(o, 0, 2, 1, 3).reshape(B, Sq, H * Dh) @ wo
```

```cpp
#include <hip/hip_runtime.h>
#include <hip/hip_bf16.h>
typedef unsigned short u16;
__device__ __forceinline__ float bf2f(u16 x){ return __uint_as_float(((unsigned)x)<<16); }
__device__ __forceinline__ u16 f2bf(float f){ unsigned u=__float_as_uint(f); u += 0x7fffu + ((u>>16)&1u); return (u16)(u>>16); }
constexpr int B=8,SQ=256,D=448,H=16,SK=1024,DH=256;
__global__ void gemm_naive(const u16* A,const u16* Bm,u16* C,int M,int N,int K){
  __shared__ float As[16][17], Bs[16][17];
  int tx=threadIdx.x&15, ty=threadIdx.x>>4;
  int row=blockIdx.y*16+ty, col=blockIdx.x*16+tx;
  float acc=0;
  for(int k0=0;k0<K;k0+=16){
    As[ty][tx]=bf2f(A[(size_t)row*K+k0+tx]);
    Bs[ty][tx]=bf2f(Bm[(size_t)(k0+ty)*N+col]);
    __syncthreads();
    for(int k=0;k<16;k++) acc+=As[ty][k]*Bs[k][tx];
    __syncthreads();
  }
  C[(size_t)row*N+col]=f2bf(acc);
}
__global__ void attn_naive(const u16* Q,const u16* K,const u16* V,u16* O){
  __shared__ float q[DH], p[SK], red[256];
  int bid=blockIdx.x; int qi=bid%SQ; int h=(bid/SQ)%H; int b=bid/(SQ*H);
  int t=threadIdx.x;
  q[t]=bf2f(Q[((size_t)(b*SQ+qi))*(H*DH)+h*DH+t]);
  __syncthreads();
  const u16* Kb=K+((size_t)(b*H+h))*SK*DH; const u16* Vb=V+((size_t)(b*H+h))*SK*DH;
  float m=-1e30f;
  for(int k=t;k<SK;k+=256){ float s=0; for(int d=0;d<DH;d++) s+=q[d]*bf2f(Kb[(size_t)k*DH+d]); s*=0.0625f; p[k]=s; m=fmaxf(m,s);}
  red[t]=m; __syncthreads();
  for(int o=128;o>0;o>>=1){ if(t<o) red[t]=fmaxf(red[t],red[t+o]); __syncthreads(); }
  m=red[0]; __syncthreads();
  float l=0; for(int k=t;k<SK;k+=256){ float e=__expf(p[k]-m); p[k]=e; l+=e; }
  red[t]=l; __syncthreads();
  for(int o=128;o>0;o>>=1){ if(t<o) red[t]+=red[t+o]; __syncthreads(); }
  l=red[0];
  float acc=0; for(int k=0;k<SK;k++) acc+=p[k]*bf2f(Vb[(size_t)k*DH+t]);
  O[((size_t)(b*SQ+qi))*(H*DH)+h*DH+t]=f2bf(acc/l);
}
extern "C" void kernel_launch(void* const* d_in, const int* in_sizes, int n_in,
                              void* d_out, int out_size, void* d_ws, size_t ws_size,
                              hipStream_t stream) {
  const u16* x=(const u16*)d_in[0]; const u16* kc=(const u16*)d_in[1]; const u16* vc=(const u16*)d_in[2];
  const u16* wq=(const u16*)d_in[3]; const u16* wo=(const u16*)d_in[4];
  u16* Q=(u16*)d_ws; u16* O=Q+(size_t)B*SQ*H*DH;
  gemm_naive<<<dim3(H*DH/16,B*SQ/16),256,0,stream>>>(x,wq,Q,B*SQ,H*DH,D);
  attn_naive<<<B*H*SQ,256,0,stream>>>(Q,kc,vc,O);
  gemm_naive<<<dim3(D/16,B*SQ/16),256,0,stream>>>(O,wo,(u16*)d_out,B*SQ,D,H*DH);
}
```

```cpp
#include <hip/hip_runtime.h>
#include <hip/hip_bf16.h>

__global__ __launch_bounds__(256, 1) void mha_fused(const char* __restrict__ x, const char* __restrict__ kc, const char* __restrict__ vc,
                                                    const char* __restrict__ wq, const char* __restrict__ wo, char* __restrict__ ws,
                                                    char* __restrict__ out, char* __restrict__ pad) {
  __shared__ __attribute__((aligned(16))) char lds[163840];
  asm volatile(
    "s_load_dwordx16 s[84:99], %[ka], 0\n"
    "s_getpc_b64 s[72:73]\n"
    "L_pc:\n"
    "s_and_b32 s67, %[blk], -8\n"
    "s_lshl_b32 s67, s67, 9\n"
    "v_lshl_add_u32 v60, v0, 4, s67\n"
    "v_min_u32 v60, L_end-L_pc-16, v60\n"
    "global_load_dwordx4 v[104:107], v60, s[72:73]\n"
    "v_readfirstlane_b32 s69, v0\n"
    "s_mov_b32 s70, %[blk]\n"
    "s_lshr_b32 s79, s70, 3\n"
    "s_and_b32 s78, s79, 1\n"
    "s_lshr_b32 s76, s70, 5\n"
    "s_and_b32 s67, s70, 7\n"
    "s_lshl_b32 s67, s67, 1\n"
    "s_bfe_u32 s77, s70, 0x10004\n"
    "s_or_b32 s77, s77, s67\n"
    "s_lshr_b32 s69, s69, 6\n"
    "s_lshl_b32 s19, s76, 1\n"
    "s_add_u32 s19, s19, s78\n"
    "s_and_b32 s4, s19, 3\n"
    "s_lshl_b32 s4, s4, 1\n"
    "s_lshl_b32 s4, s4, 7\n"
    "s_lshl_b32 s11, s4, 12\n"
    "s_lshl_b32 s62, s69, 12\n"
    "v_mbcnt_lo_u32_b32 v0, -1, 0\n"
    "v_mbcnt_hi_u32_b32 v0, -1, v0\n"
    "v_and_b32 v1, 7, v0\n"
    "v_lshrrev_b32 v2, 4, v0\n"
    "v_xor_b32 v1, v1, v2\n"
    "v_lshlrev_b32 v1, 4, v1\n"
    "v_lshrrev_b32 v2, 3, v0\n"
    "s_mul_i32 s67, s69, 0x7000\n"
    "s_movk_i32 s68, 0x380\n"
    "v_mov_b32 v3, s67\n"
    "v_mad_u32_u24 v3, v2, s68, v3\n"
    "v_add_u32 v26, v3, v1\n"
    "v_xor_b32 v48, 64, v1\n"
    "v_add_u32 v27, v3, v48\n"
    "v_add_u32 v27, 0x1800, v27\n"
    "v_add_u32 v28, 0x3000, v26\n"
    "v_add_u32 v29, 0x3000, v27\n"
    "v_lshrrev_b32 v49, 5, v0\n"
    "v_lshlrev_b32 v49, 6, v49\n"
    "v_and_b32 v50, 3, v0\n"
    "v_lshl_add_u32 v49, v50, 4, v49\n"
    "v_bfe_u32 v50, v0, 2, 3\n"
    "s_lshl_b32 s67, s69, 7\n"
    "v_add_u32 v51, s67, v49\n"
    "v_lshl_add_u32 v30, v50, 13, v51\n"
    "v_xor_b32 v31, 32, v30\n"
    "v_add_u32 v31, 0xfc00, v31\n"
    "v_add_u32 v32, 0x1f800, v30\n"
    "v_add_u32 v33, 0x1f800, v31\n"
    "s_waitcnt lgkmcnt(0)\n"
    "s_lshl_b32 s67, s76, 1\n"
    "s_add_u32 s67, s67, s78\n"
    "s_mul_i32 s67, s67, 0x1c000\n"
    "s_add_u32 s44, s84, s67\n"
    "s_addc_u32 s45, s85, 0\n"
    "s_and_b32 s45, s45, 0xffff\n"
    "s_mov_b32 s46, 0x1c000\n"
    "s_mov_b32 s47, 0x20000\n"
    "s_lshl_b32 s67, s77, 9\n"
    "s_add_u32 s48, s90, s67\n"
    "s_addc_u32 s49, s91, 0\n"
    "s_and_b32 s49, s49, 0xffff\n"
    "s_mov_b32 s50, 0x37e200\n"
    "s_mov_b32 s51, 0x20000\n"
    "s_add_u32 m0, s62, 0x20000\n"
    "s_nop 0\n"
    "buffer_load_dwordx4 v26, s[44:47], s4 offen lds\n"
    "buffer_load_dwordx4 v27, s[44:47], s4 offen offset:1024 lds\n"
    "buffer_load_dwordx4 v28, s[44:47], s4 offen offset:2048 lds\n"
    "buffer_load_dwordx4 v29, s[44:47], s4 offen offset:3072 lds\n"
    "s_add_u32 m0, s62, 0x4000\n"
    "s_nop 0\n"
    "buffer_load_dwordx4 v30, s[48:51], s11 offen lds\n"
    "buffer_load_dwordx4 v31, s[48:51], s11 offen offset:1024 lds\n"
    "buffer_load_dwordx4 v32, s[48:51], s11 offen offset:2048 lds\n"
    "buffer_load_dwordx4 v33, s[48:51], s11 offen offset:3072 lds\n"
    "v_add_u32 v116, 0x40000, v30\n"
    "v_add_u32 v117, 0x40000, v31\n"
    "v_add_u32 v118, 0x40000, v32\n"
    "v_add_u32 v119, 0x40000, v33\n"
    "s_add_u32 m0, s62, 0x18000\n"
    "s_nop 0\n"
    "buffer_load_dwordx4 v116, s[48:51], s11 offen lds\n"
    "buffer_load_dwordx4 v117, s[48:51], s11 offen offset:1024 lds\n"
    "buffer_load_dwordx4 v118, s[48:51], s11 offen offset:2048 lds\n"
    "buffer_load_dwordx4 v119, s[48:51], s11 offen offset:3072 lds\n"
    "s_mul_i32 s67, s19, 37\n"
    "s_lshr_b32 s67, s67, 8\n"
    "s_mul_i32 s67, s67, 7\n"
    "s_sub_u32 s19, s19, s67\n"
    "s_lshl_b32 s19, s19, 7\n"
    "s_add_u32 s5, s4, 0x80\n"
    "s_cmp_eq_u32 s5, 0x380\n"
    "s_cselect_b32 s5, 0, s5\n"
    "s_add_u32 s6, s5, 0x80\n"
    "s_cmp_eq_u32 s6, 0x380\n"
    "s_cselect_b32 s6, 0, s6\n"
    "s_add_u32 s7, s6, 0x80\n"
    "s_cmp_eq_u32 s7, 0x380\n"
    "s_cselect_b32 s7, 0, s7\n"
    "s_lshl_b32 s12, s5, 12\n"
    "s_lshl_b32 s13, s6, 12\n"
    "s_lshl_b32 s14, s7, 12\n"
    "v_and_b32 v52, 15, v0\n"
    "v_lshlrev_b32 v52, 7, v52\n"
    "v_bfe_u32 v53, v0, 1, 3\n"
    "v_lshrrev_b32 v54, 4, v0\n"
    "v_xor_b32 v53, v53, v54\n"
    "v_lshl_add_u32 v4, v53, 4, v52\n"
    "v_xor_b32 v5, 64, v4\n"
    "s_and_b32 s68, s77, 1\n"
    "s_lshl_b32 s68, s68, 11\n"
    "s_add_u32 s67, s62, 0x20000\n"
    "s_or_b32 s67, s67, s68\n"
    "v_add_u32 v6, s67, v4\n"
    "v_add_u32 v7, s67, v5\n"
    "v_xor_b32 v96, 0x800, v6\n"
    "v_xor_b32 v97, 0x800, v7\n"
    "v_subrev_u32 v112, 0x10000, v6\n"
    "v_subrev_u32 v113, 0x10000, v7\n"
    "v_subrev_u32 v98, 0x10000, v96\n"
    "v_subrev_u32 v99, 0x10000, v97\n"
    "v_bfe_u32 v52, v0, 2, 2\n"
    "v_lshlrev_b32 v52, 6, v52\n"
    "v_and_b32 v55, 3, v0\n"
    "v_bfe_u32 v53, v0, 4, 1\n"
    "v_lshlrev_b32 v14, 1, v53\n"
    "v_xor_b32 v14, v14, v55\n"
    "v_lshl_add_u32 v14, v14, 4, v52\n"
    "v_lshl_add_u32 v14, v54, 10, v14\n"
    "v_add_u32 v16, 0x10000, v14\n"
    "s_mov_b64 exec, 0\n"
    "s_cmp_eq_u32 s69, 0\n"
    "s_cbranch_scc1 L_iw0\n"
    "s_cmp_eq_u32 s69, 1\n"
    "s_cbranch_scc1 L_iw1\n"
    "s_cmp_eq_u32 s69, 2\n"
    "s_cbranch_scc1 L_iw2\n"
    "s_branch L_iw3\n"
    "L_iwback:\n"
    "s_mov_b64 exec, -1\n"
    "s_add_u32 m0, s62, 0x24000\n"
    "s_nop 0\n"
    "buffer_load_dwordx4 v26, s[44:47], s5 offen lds\n"
    "buffer_load_dwordx4 v27, s[44:47], s5 offen offset:1024 lds\n"
    "buffer_load_dwordx4 v28, s[44:47], s5 offen offset:2048 lds\n"
    "buffer_load_dwordx4 v29, s[44:47], s5 offen offset:3072 lds\n"
    "s_add_u32 m0, s62, 0x1c000\n"
    "s_nop 0\n"
    "buffer_load_dwordx4 v30, s[48:51], s12 offen lds\n"
    "buffer_load_dwordx4 v31, s[48:51], s12 offen offset:1024 lds\n"
    "buffer_load_dwordx4 v32, s[48:51], s12 offen offset:2048 lds\n"
    "buffer_load_dwordx4 v33, s[48:51], s12 offen offset:3072 lds\n"
    "s_waitcnt vmcnt(12)\n"
    "s_barrier\n"
    "ds_read_b128 v[64:67], v6 offset:0\n"
    "ds_read_b128 v[68:71], v96 offset:0\n"
    "ds_read_b128 v[72:75], v7 offset:0\n"
    "ds_read_b128 v[76:79], v97 offset:0\n"
    "ds_read_b64_tr_b16 a[64:65], v14 offset:16384\n"
    "ds_read_b64_tr_b16 a[66:67], v14 offset:16640\n"
    "ds_read_b64_tr_b16 a[68:69], v14 offset:16392\n"
    "ds_read_b64_tr_b16 a[70:71], v14 offset:16648\n"
    "ds_read_b64_tr_b16 a[72:73], v14 offset:16896\n"
    "ds_read_b64_tr_b16 a[74:75], v14 offset:17152\n"
    "ds_read_b64_tr_b16 a[76:77], v14 offset:16904\n"
    "ds_read_b64_tr_b16 a[78:79], v14 offset:17160\n"
    "ds_read_b64_tr_b16 a[80:81], v14 offset:20480\n"
    "ds_read_b64_tr_b16 a[82:83], v14 offset:20736\n"
    "ds_read_b64_tr_b16 a[84:85], v14 offset:20488\n"
    "ds_read_b64_tr_b16 a[86:87], v14 offset:20744\n"
    "ds_read_b64_tr_b16 a[88:89], v14 offset:20992\n"
    "ds_read_b64_tr_b16 a[90:91], v14 offset:21248\n"
    "ds_read_b64_tr_b16 a[92:93], v14 offset:21000\n"
    "ds_read_b64_tr_b16 a[94:95], v14 offset:21256\n"
    "ds_read_b64_tr_b16 a[96:97], v14 offset:24576\n"
    "ds_read_b64_tr_b16 a[98:99], v14 offset:24832\n"
    "ds_read_b64_tr_b16 a[100:101], v14 offset:24584\n"
    "ds_read_b64_tr_b16 a[102:103], v14 offset:24840\n"
    "ds_read_b64_tr_b16 a[104:105], v14 offset:25088\n"
    "ds_read_b64_tr_b16 a[106:107], v14 offset:25344\n"
    "ds_read_b64_tr_b16 a[108:109], v14 offset:25096\n"
    "ds_read_b64_tr_b16 a[110:111], v14 offset:25352\n"
    "ds_read_b64_tr_b16 a[112:113], v14 offset:28672\n"
    "ds_read_b64_tr_b16 a[114:115], v14 offset:28928\n"
    "ds_read_b64_tr_b16 a[116:117], v14 offset:28680\n"
    "ds_read_b64_tr_b16 a[118:119], v14 offset:28936\n"
    "ds_read_b64_tr_b16 a[120:121], v14 offset:29184\n"
    "ds_read_b64_tr_b16 a[122:123], v14 offset:29440\n"
    "ds_read_b64_tr_b16 a[124:125], v14 offset:29192\n"
    "ds_read_b64_tr_b16 a[126:127], v14 offset:29448\n"
    "L_qs:\n"
    "s_waitcnt vmcnt(8) lgkmcnt(14)\n"
    "s_barrier\n"
    "v_mfma_f32_16x16x32_bf16 v[128:131], a[64:67], v[64:67], 0\n"
    "ds_read_b64_tr_b16 a[128:129], v16 offset:32768\n"
    "s_add_u32 m0, s62, 0x8000\n"
    "v_mfma_f32_16x16x32_bf16 v[136:139], a[64:67], v[68:71], 0\n"
    "buffer_load_dwordx4 v116, s[48:51], s12 offen lds\n"
    "v_mfma_f32_16x16x32_bf16 v[140:143], a[68:71], v[68:71], 0\n"
    "ds_read_b64_tr_b16 a[130:131], v16 offset:33024\n"
    "s_add_u32 s8, s7, 0x80\n"
    "v_mfma_f32_16x16x32_bf16 v[132:135], a[68:71], v[64:67], 0\n"
    "ds_read_b64_tr_b16 a[132:133], v16 offset:32776\n"
    "ds_read_b64_tr_b16 a[134:135], v16 offset:33032\n"
    "v_mfma_f32_16x16x32_bf16 v[144:147], a[72:75], v[64:67], 0\n"
    "s_cmp_eq_u32 s8, 0x380\n"
    "s_cselect_b32 s8, 0, s8\n"
    "v_mfma_f32_16x16x32_bf16 v[152:155], a[72:75], v[68:71], 0\n"
    "ds_read_b64_tr_b16 a[136:137], v16 offset:33280\n"
    "buffer_load_dwordx4 v117, s[48:51], s12 offen offset:1024 lds\n"
    "v_mfma_f32_16x16x32_bf16 v[156:159], a[76:79], v[68:71], 0\n"
    "ds_read_b64_tr_b16 a[138:139], v16 offset:33536\n"
    "s_lshl_b32 s15, s8, 12\n"
    "v_mfma_f32_16x16x32_bf16 v[148:151], a[76:79], v[64:67], 0\n"
    "ds_read_b64_tr_b16 a[140:141], v16 offset:33288\n"
    "s_add_u32 s9, s8, 0x80\n"
    "v_mfma_f32_16x16x32_bf16 v[160:163], a[80:83], v[64:67], 0\n"
    "s_cmp_eq_u32 s9, 0x380\n"
    "s_cselect_b32 s9, 0, s9\n"
    "v_mfma_f32_16x16x32_bf16 v[168:171], a[80:83], v[68:71], 0\n"
    "ds_read_b64_tr_b16 a[142:143], v16 offset:33544\n"
    "s_lshl_b32 s16, s9, 12\n"
    "v_mfma_f32_16x16x32_bf16 v[172:175], a[84:87], v[68:71], 0\n"
    "buffer_load_dwordx4 v118, s[48:51], s12 offen offset:2048 lds\n"
    "v_mfma_f32_16x16x32_bf16 v[164:167], a[84:87], v[64:67], 0\n"
    "ds_read_b64_tr_b16 a[144:145], v16 offset:36864\n"
    "ds_read_b64_tr_b16 a[146:147], v16 offset:37120\n"
    "s_add_u32 s10, s9, 0x80\n"
    "v_mfma_f32_16x16x32_bf16 v[176:179], a[88:91], v[64:67], 0\n"
    "ds_read_b64_tr_b16 a[148:149], v16 offset:36872\n"
    "ds_read_b64_tr_b16 a[150:151], v16 offset:37128\n"
    "v_mfma_f32_16x16x32_bf16 v[184:187], a[88:91], v[68:71], 0\n"
    "ds_read_b64_tr_b16 a[152:153], v16 offset:37376\n"
    "s_cmp_eq_u32 s10, 0x380\n"
    "s_cselect_b32 s10, 0, s10\n"
    "v_mfma_f32_16x16x32_bf16 v[188:191], a[92:95], v[68:71], 0\n"
    "buffer_load_dwordx4 v119, s[48:51], s12 offen offset:3072 lds\n"
    "v_mfma_f32_16x16x32_bf16 v[180:183], a[92:95], v[64:67], 0\n"
    "ds_read_b64_tr_b16 a[154:155], v16 offset:37632\n"
    "s_add_u32 m0, s62, 0x10000\n"
    "v_mfma_f32_16x16x32_bf16 v[192:195], a[96:99], v[64:67], 0\n"
    "ds_read_b64_tr_b16 a[156:157], v16 offset:37384\n"
    "s_lshl_b32 s17, s10, 12\n"
    "v_mfma_f32_16x16x32_bf16 v[200:203], a[96:99], v[68:71], 0\n"
    "ds_read_b64_tr_b16 a[158:159], v16 offset:37640\n"
    "s_lshl_b32 s67, s76, 4\n"
    "s_waitcnt lgkmcnt(15)\n"
    "v_mfma_f32_16x16x32_bf16 v[204:207], a[100:103], v[68:71], 0\n"
    "buffer_load_dwordx4 v26, s[44:47], s6 offen lds\n"
    "v_mfma_f32_16x16x32_bf16 v[196:199], a[100:103], v[64:67], 0\n"
    "ds_read_b64_tr_b16 a[160:161], v16 offset:40960\n"
    "s_add_u32 s67, s67, s77\n"
    "v_mfma_f32_16x16x32_bf16 v[208:211], a[104:107], v[64:67], 0\n"
    "ds_read_b64_tr_b16 a[162:163], v16 offset:41216\n"
    "s_lshl_b32 s67, s67, 19\n"
    "v_mfma_f32_16x16x32_bf16 v[216:219], a[104:107], v[68:71], 0\n"
    "ds_read_b64_tr_b16 a[164:165], v16 offset:40968\n"
    "ds_read_b64_tr_b16 a[166:167], v16 offset:41224\n"
    "v_mfma_f32_16x16x32_bf16 v[220:223], a[108:111], v[68:71], 0\n"
    "ds_read_b64_tr_b16 a[168:169], v16 offset:41472\n"
    "s_add_u32 s36, s86, s67\n"
    "s_addc_u32 s37, s87, 0\n"
    "v_mfma_f32_16x16x32_bf16 v[212:215], a[108:111], v[64:67], 0\n"
    "buffer_load_dwordx4 v27, s[44:47], s6 offen offset:1024 lds\n"
    "v_mfma_f32_16x16x32_bf16 v[224:227], a[112:115], v[64:67], 0\n"
    "ds_read_b64_tr_b16 a[170:171], v16 offset:41728\n"
    "ds_read_b64_tr_b16 a[172:173], v16 offset:41480\n"
    "s_and_b32 s37, s37, 0xffff\n"
    "v_mfma_f32_16x16x32_bf16 v[232:235], a[112:115], v[68:71], 0\n"
    "ds_read_b64_tr_b16 a[174:175], v16 offset:41736\n"
    "ds_read_b64_tr_b16 a[176:177], v16 offset:45056\n"
    "s_mov_b32 s38, 0x80000\n"
    "v_mfma_f32_16x16x32_bf16 v[236:239], a[116:119], v[68:71], 0\n"
    "ds_read_b64_tr_b16 a[178:179], v16 offset:45312\n"
    "s_mov_b32 s39, 0x20000\n"
    "v_mfma_f32_16x16x32_bf16 v[228:231], a[116:119], v[64:67], 0\n"
    "buffer_load_dwordx4 v28, s[44:47], s6 offen offset:2048 lds\n"
    "v_mfma_f32_16x16x32_bf16 v[240:243], a[120:123], v[64:67], 0\n"
    "ds_read_b64_tr_b16 a[180:181], v16 offset:45064\n"
    "s_lshl_b32 s67, s76, 4\n"
    "v_mfma_f32_16x16x32_bf16 v[248:251], a[120:123], v[68:71], 0\n"
    "ds_read_b64_tr_b16 a[182:183], v16 offset:45320\n"
    "ds_read_b64_tr_b16 a[184:185], v16 offset:45568\n"
    "v_mfma_f32_16x16x32_bf16 v[252:255], a[124:127], v[68:71], 0\n"
    "ds_read_b64_tr_b16 a[186:187], v16 offset:45824\n"
    "ds_read_b64_tr_b16 a[188:189], v16 offset:45576\n"
    "v_mfma_f32_16x16x32_bf16 v[244:247], a[124:127], v[64:67], 0\n"
    "ds_read_b64_tr_b16 a[190:191], v16 offset:45832\n"
    "s_waitcnt vmcnt(7) lgkmcnt(6)\n"
    "s_barrier\n"
    "v_mfma_f32_16x16x32_bf16 v[128:131], a[128:131], v[72:75], v[128:131]\n"
    "ds_read_b128 v[80:83], v6 offset:16384\n"
    "s_add_u32 s67, s67, s77\n"
    "v_mfma_f32_16x16x32_bf16 v[136:139], a[128:131], v[76:79], v[136:139]\n"
    "ds_read_b128 v[84:87], v96 offset:16384\n"
    "buffer_load_dwordx4 v29, s[44:47], s6 offen offset:3072 lds\n"
    "v_mfma_f32_16x16x32_bf16 v[140:143], a[132:135], v[76:79], v[140:143]\n"
    "ds_read_b128 v[88:91], v7 offset:16384\n"
    "s_add_u32 m0, s62, 0x14000\n"
    "s_lshl_b32 s67, s67, 19\n"
    "v_mfma_f32_16x16x32_bf16 v[132:135], a[132:135], v[72:75], v[132:135]\n"
    "ds_read_b128 v[92:95], v97 offset:16384\n"
    "s_add_u32 s40, s88, s67\n"
    "s_addc_u32 s41, s89, 0\n"
    "v_mfma_f32_16x16x32_bf16 v[144:147], a[136:139], v[72:75], v[144:147]\n"
    "ds_read_b64_tr_b16 a[64:65], v16 offset:49152\n"
    "s_sub_u32 s40, s40, 0x4000\n"
    "s_subb_u32 s41, s41, 0\n"
    "v_mfma_f32_16x16x32_bf16 v[152:155], a[136:139], v[76:79], v[152:155]\n"
    "ds_read_b64_tr_b16 a[66:67], v16 offset:49408\n"
    "buffer_load_dwordx4 v30, s[48:51], s13 offen lds\n"
    "v_mfma_f32_16x16x32_bf16 v[156:159], a[140:143], v[76:79], v[156:159]\n"
    "ds_read_b64_tr_b16 a[68:69], v16 offset:49160\n"
    "s_and_b32 s41, s41, 0xffff\n"
    "v_mfma_f32_16x16x32_bf16 v[148:151], a[140:143], v[72:75], v[148:151]\n"
    "ds_read_b64_tr_b16 a[70:71], v16 offset:49416\n"
    "s_mov_b32 s42, 0x84000\n"
    "v_mfma_f32_16x16x32_bf16 v[160:163], a[144:147], v[72:75], v[160:163]\n"
    "ds_read_b64_tr_b16 a[72:73], v16 offset:49664\n"
    "ds_read_b64_tr_b16 a[74:75], v16 offset:49920\n"
    "s_mov_b32 s43, 0x20000\n"
    "v_mfma_f32_16x16x32_bf16 v[168:171], a[144:147], v[76:79], v[168:171]\n"
    "ds_read_b64_tr_b16 a[76:77], v16 offset:49672\n"
    "buffer_load_dwordx4 v31, s[48:51], s13 offen offset:1024 lds\n"
    "v_mfma_f32_16x16x32_bf16 v[172:175], a[148:151], v[76:79], v[172:175]\n"
    "ds_read_b64_tr_b16 a[78:79], v16 offset:49928\n"
    "ds_read_b64_tr_b16 a[80:81], v16 offset:53248\n"
    "s_lshl_b32 s67, s69, 7\n"
    "v_mfma_f32_16x16x32_bf16 v[164:167], a[148:151], v[72:75], v[164:167]\n"
    "ds_read_b64_tr_b16 a[82:83], v16 offset:53504\n"
    "v_lshlrev_b32 v3, 9, v2\n"
    "v_mfma_f32_16x16x32_bf16 v[176:179], a[152:155], v[72:75], v[176:179]\n"
    "ds_read_b64_tr_b16 a[84:85], v16 offset:53256\n"
    "v_add_u32 v3, s67, v3\n"
    "v_mfma_f32_16x16x32_bf16 v[184:187], a[152:155], v[76:79], v[184:187]\n"
    "ds_read_b64_tr_b16 a[86:87], v16 offset:53512\n"
    "v_add_u32 v18, v3, v1\n"
    "v_mfma_f32_16x16x32_bf16 v[188:191], a[156:159], v[76:79], v[188:191]\n"
    "ds_read_b64_tr_b16 a[88:89], v16 offset:53760\n"
    "buffer_load_dwordx4 v32, s[48:51], s13 offen offset:2048 lds\n"
    "v_mfma_f32_16x16x32_bf16 v[180:183], a[156:159], v[72:75], v[180:183]\n"
    "ds_read_b64_tr_b16 a[90:91], v16 offset:54016\n"
    "v_add_u32 v19, v3, v48\n"
    "v_mfma_f32_16x16x32_bf16 v[192:195], a[160:163], v[72:75], v[192:195]\n"
    "ds_read_b64_tr_b16 a[92:93], v16 offset:53768\n"
    "v_add_u32 v19, 0xc00, v19\n"
    "v_mfma_f32_16x16x32_bf16 v[200:203], a[160:163], v[76:79], v[200:203]\n"
    "ds_read_b64_tr_b16 a[94:95], v16 offset:54024\n"
    "v_add_u32 v20, 0x1800, v18\n"
    "v_mfma_f32_16x16x32_bf16 v[204:207], a[164:167], v[76:79], v[204:207]\n"
    "ds_read_b64_tr_b16 a[96:97], v16 offset:57344\n"
    "buffer_load_dwordx4 v33, s[48:51], s13 offen offset:3072 lds\n"
    "v_mfma_f32_16x16x32_bf16 v[196:199], a[164:167], v[72:75], v[196:199]\n"
    "ds_read_b64_tr_b16 a[98:99], v16 offset:57600\n"
    "s_add_u32 m0, s62, 0xc000\n"
    "v_mfma_f32_16x16x32_bf16 v[208:211], a[168:171], v[72:75], v[208:211]\n"
    "ds_read_b64_tr_b16 a[100:101], v16 offset:57352\n"
    "ds_read_b64_tr_b16 a[102:103], v16 offset:57608\n"
    "v_add_u32 v21, 0x1800, v19\n"
    "v_mfma_f32_16x16x32_bf16 v[216:219], a[168:171], v[76:79], v[216:219]\n"
    "ds_read_b64_tr_b16 a[104:105], v16 offset:57856\n"
    "v_and_b32 v3, 16, v0\n"
    "v_mfma_f32_16x16x32_bf16 v[220:223], a[172:175], v[76:79], v[220:223]\n"
    "ds_read_b64_tr_b16 a[106:107], v16 offset:58112\n"
    "v_lshlrev_b32 v3, 1, v3\n"
    "v_mfma_f32_16x16x32_bf16 v[212:215], a[172:175], v[72:75], v[212:215]\n"
    "ds_read_b64_tr_b16 a[108:109], v16 offset:57864\n"
    "buffer_load_dwordx4 v116, s[48:51], s13 offen lds\n"
    "v_mfma_f32_16x16x32_bf16 v[224:227], a[176:179], v[72:75], v[224:227]\n"
    "ds_read_b64_tr_b16 a[110:111], v16 offset:58120\n"
    "ds_read_b64_tr_b16 a[112:113], v16 offset:61440\n"
    "v_mfma_f32_16x16x32_bf16 v[232:235], a[176:179], v[76:79], v[232:235]\n"
    "ds_read_b64_tr_b16 a[114:115], v16 offset:61696\n"
    "s_waitcnt lgkmcnt(15)\n"
    "v_mfma_f32_16x16x32_bf16 v[236:239], a[180:183], v[76:79], v[236:239]\n"
    "ds_read_b64_tr_b16 a[116:117], v16 offset:61448\n"
    "v_mfma_f32_16x16x32_bf16 v[228:231], a[180:183], v[72:75], v[228:231]\n"
    "ds_read_b64_tr_b16 a[118:119], v16 offset:61704\n"
    "buffer_load_dwordx4 v117, s[48:51], s13 offen offset:1024 lds\n"
    "v_mfma_f32_16x16x32_bf16 v[240:243], a[184:187], v[72:75], v[240:243]\n"
    "ds_read_b64_tr_b16 a[120:121], v16 offset:61952\n"
    "v_mfma_f32_16x16x32_bf16 v[248:251], a[184:187], v[76:79], v[248:251]\n"
    "ds_read_b64_tr_b16 a[122:123], v16 offset:62208\n"
    "v_mfma_f32_16x16x32_bf16 v[252:255], a[188:191], v[76:79], v[252:255]\n"
    "ds_read_b64_tr_b16 a[124:125], v16 offset:61960\n"
    "v_mfma_f32_16x16x32_bf16 v[244:247], a[188:191], v[72:75], v[244:247]\n"
    "ds_read_b64_tr_b16 a[126:127], v16 offset:62216\n"
    "s_waitcnt vmcnt(10) lgkmcnt(6)\n"
    "s_barrier\n"
    "v_mfma_f32_16x16x32_bf16 v[128:131], a[64:67], v[80:83], v[128:131]\n"
    "ds_read_b64_tr_b16 a[128:129], v14 offset:32768\n"
    "v_xor_b32 v49, v49, v3\n"
    "v_mfma_f32_16x16x32_bf16 v[136:139], a[64:67], v[84:87], v[136:139]\n"
    "ds_read_b64_tr_b16 a[130:131], v14 offset:33024\n"
    "buffer_load_dwordx4 v118, s[48:51], s13 offen offset:2048 lds\n"
    "v_mfma_f32_16x16x32_bf16 v[140:143], a[68:71], v[84:87], v[140:143]\n"
    "ds_read_b64_tr_b16 a[132:133], v14 offset:32776\n"
    "v_xor_b32 v51, v51, v3\n"
    "v_mfma_f32_16x16x32_bf16 v[132:135], a[68:71], v[80:83], v[132:135]\n"
    "ds_read_b64_tr_b16 a[134:135], v14 offset:33032\n"
    "v_lshl_add_u32 v22, v50, 9, v51\n"
    "v_mfma_f32_16x16x32_bf16 v[144:147], a[72:75], v[80:83], v[144:147]\n"
    "ds_read_b64_tr_b16 a[136:137], v14 offset:33280\n"
    "v_add_u32 v23, 0xc00, v22\n"
    "v_mfma_f32_16x16x32_bf16 v[152:155], a[72:75], v[84:87], v[152:155]\n"
    "ds_read_b64_tr_b16 a[138:139], v14 offset:33536\n"
    "buffer_load_dwordx4 v119, s[48:51], s13 offen offset:3072 lds\n"
    "v_mfma_f32_16x16x32_bf16 v[156:159], a[76:79], v[84:87], v[156:159]\n"
    "ds_read_b64_tr_b16 a[140:141], v14 offset:33288\n"
    "s_add_u32 m0, s62, 0x20000\n"
    "v_mfma_f32_16x16x32_bf16 v[148:151], a[76:79], v[80:83], v[148:151]\n"
    "ds_read_b64_tr_b16 a[142:143], v14 offset:33544\n"
    "v_add_u32 v24, 0x1800, v22\n"
    "v_mfma_f32_16x16x32_bf16 v[160:163], a[80:83], v[80:83], v[160:163]\n"
    "ds_read_b64_tr_b16 a[144:145], v14 offset:36864\n"
    "v_add_u32 v25, 0x2400, v22\n"
    "v_mfma_f32_16x16x32_bf16 v[168:171], a[80:83], v[84:87], v[168:171]\n"
    "ds_read_b64_tr_b16 a[146:147], v14 offset:37120\n"
    "buffer_load_dwordx4 v26, s[44:47], s7 offen lds\n"
    "v_mfma_f32_16x16x32_bf16 v[172:175], a[84:87], v[84:87], v[172:175]\n"
    "ds_read_b64_tr_b16 a[148:149], v14 offset:36872\n"
    "v_lshl_add_u32 v52, v55, 3, v52\n"
    "v_mfma_f32_16x16x32_bf16 v[164:167], a[84:87], v[80:83], v[164:167]\n"
    "ds_read_b64_tr_b16 a[150:151], v14 offset:37128\n"
    "v_lshl_add_u32 v52, v53, 5, v52\n"
    "v_mfma_f32_16x16x32_bf16 v[176:179], a[88:91], v[80:83], v[176:179]\n"
    "ds_read_b64_tr_b16 a[152:153], v14 offset:37376\n"
    "v_lshl_add_u32 v52, v53, 8, v52\n"
    "v_mfma_f32_16x16x32_bf16 v[184:187], a[88:91], v[84:87], v[184:187]\n"
    "ds_read_b64_tr_b16 a[154:155], v14 offset:37632\n"
    "v_lshrrev_b32 v54, 5, v0\n"
    "v_mfma_f32_16x16x32_bf16 v[188:191], a[92:95], v[84:87], v[188:191]\n"
    "ds_read_b64_tr_b16 a[156:157], v14 offset:37384\n"
    "buffer_load_dwordx4 v27, s[44:47], s7 offen offset:1024 lds\n"
    "v_mfma_f32_16x16x32_bf16 v[180:183], a[92:95], v[80:83], v[180:183]\n"
    "ds_read_b64_tr_b16 a[158:159], v14 offset:37640\n"
    "v_lshl_add_u32 v8, v54, 10, v52\n"
    "v_mfma_f32_16x16x32_bf16 v[192:195], a[96:99], v[80:83], v[192:195]\n"
    "ds_read_b64_tr_b16 a[160:161], v14 offset:40960\n"
    "v_xor_b32 v9, 32, v8\n"
    "v_mfma_f32_16x16x32_bf16 v[200:203], a[96:99], v[84:87], v[200:203]\n"
    "ds_read_b64_tr_b16 a[162:163], v14 offset:41216\n"
    "v_add_u32 v10, 0x10000, v8\n"
    "v_mfma_f32_16x16x32_bf16 v[204:207], a[100:103], v[84:87], v[204:207]\n"
    "ds_read_b64_tr_b16 a[164:165], v14 offset:40968\n"
    "buffer_load_dwordx4 v28, s[44:47], s7 offen offset:2048 lds\n"
    "v_mfma_f32_16x16x32_bf16 v[196:199], a[100:103], v[80:83], v[196:199]\n"
    "ds_read_b64_tr_b16 a[166:167], v14 offset:41224\n"
    "v_add_u32 v11, 0x10000, v9\n"
    "v_mfma_f32_16x16x32_bf16 v[208:211], a[104:107], v[80:83], v[208:211]\n"
    "ds_read_b64_tr_b16 a[168:169], v14 offset:41472\n"
    "v_add_u32 v12, 0x20000, v8\n"
    "v_mfma_f32_16x16x32_bf16 v[216:219], a[104:107], v[84:87], v[216:219]\n"
    "ds_read_b64_tr_b16 a[170:171], v14 offset:41728\n"
    "v_add_u32 v13, 0x20000, v9\n"
    "v_mfma_f32_16x16x32_bf16 v[220:223], a[108:111], v[84:87], v[220:223]\n"
    "ds_read_b64_tr_b16 a[172:173], v14 offset:41480\n"
    "s_mul_i32 s67, s77, 0x38000\n"
    "v_mfma_f32_16x16x32_bf16 v[212:215], a[108:111], v[80:83], v[212:215]\n"
    "ds_read_b64_tr_b16 a[174:175], v14 offset:41736\n"
    "buffer_load_dwordx4 v29, s[44:47], s7 offen offset:3072 lds\n"
    "v_mfma_f32_16x16x32_bf16 v[224:227], a[112:115], v[80:83], v[224:227]\n"
    "ds_read_b64_tr_b16 a[176:177], v14 offset:45056\n"
    "s_add_u32 m0, s62, 0\n"
    "v_mfma_f32_16x16x32_bf16 v[232:235], a[112:115], v[84:87], v[232:235]\n"
    "ds_read_b64_tr_b16 a[178:179], v14 offset:45312\n"
    "s_waitcnt lgkmcnt(15)\n"
    "v_mfma_f32_16x16x32_bf16 v[236:239], a[116:119], v[84:87], v[236:239]\n"
    "ds_read_b64_tr_b16 a[180:181], v14 offset:45064\n"
    "v_mfma_f32_16x16x32_bf16 v[228:231], a[116:119], v[80:83], v[228:231]\n"
    "ds_read_b64_tr_b16 a[182:183], v14 offset:45320\n"
    "buffer_load_dwordx4 v30, s[48:51], s14 offen lds\n"
    "v_mfma_f32_16x16x32_bf16 v[240:243], a[120:123], v[80:83], v[240:243]\n"
    "ds_read_b64_tr_b16 a[184:185], v14 offset:45568\n"
    "v_mfma_f32_16x16x32_bf16 v[248:251], a[120:123], v[84:87], v[248:251]\n"
    "ds_read_b64_tr_b16 a[186:187], v14 offset:45824\n"
    "v_mfma_f32_16x16x32_bf16 v[252:255], a[124:127], v[84:87], v[252:255]\n"
    "ds_read_b64_tr_b16 a[188:189], v14 offset:45576\n"
    "v_mfma_f32_16x16x32_bf16 v[244:247], a[124:127], v[80:83], v[244:247]\n"
    "ds_read_b64_tr_b16 a[190:191], v14 offset:45832\n"
    "s_waitcnt vmcnt(9) lgkmcnt(6)\n"
    "s_barrier\n"
    "v_mfma_f32_16x16x32_bf16 v[128:131], a[128:131], v[88:91], v[128:131]\n"
    "ds_read_b128 v[64:67], v112 offset:0\n"
    "s_add_u32 s52, s92, s67\n"
    "s_addc_u32 s53, s93, 0\n"
    "v_mfma_f32_16x16x32_bf16 v[136:139], a[128:131], v[92:95], v[136:139]\n"
    "ds_read_b128 v[68:71], v98 offset:0\n"
    "buffer_load_dwordx4 v31, s[48:51], s14 offen offset:1024 lds\n"
    "v_mfma_f32_16x16x32_bf16 v[140:143], a[132:135], v[92:95], v[140:143]\n"
    "ds_read_b128 v[72:75], v113 offset:0\n"
    "ds_read_b128 v[76:79], v99 offset:0\n"
    "s_and_b32 s53, s53, 0xffff\n"
    "v_mfma_f32_16x16x32_bf16 v[132:135], a[132:135], v[88:91], v[132:135]\n"
    "ds_read_b64_tr_b16 a[64:65], v16 offset:16384\n"
    "s_mov_b32 s54, 0x38000\n"
    "v_mfma_f32_16x16x32_bf16 v[144:147], a[136:139], v[88:91], v[144:147]\n"
    "ds_read_b64_tr_b16 a[66:67], v16 offset:16640\n"
    "s_mov_b32 s55, 0x20000\n"
    "v_mfma_f32_16x16x32_bf16 v[152:155], a[136:139], v[92:95], v[152:155]\n"
    "ds_read_b64_tr_b16 a[68:69], v16 offset:16392\n"
    "buffer_load_dwordx4 v32, s[48:51], s14 offen offset:2048 lds\n"
    "v_mfma_f32_16x16x32_bf16 v[156:159], a[140:143], v[92:95], v[156:159]\n"
    "ds_read_b64_tr_b16 a[70:71], v16 offset:16648\n"
    "ds_read_b64_tr_b16 a[72:73], v16 offset:16896\n"
    "s_lshl_b32 s67, s76, 1\n"
    "v_mfma_f32_16x16x32_bf16 v[148:151], a[140:143], v[88:91], v[148:151]\n"
    "ds_read_b64_tr_b16 a[74:75], v16 offset:17152\n"
    "ds_read_b64_tr_b16 a[76:77], v16 offset:16904\n"
    "s_add_u32 s67, s67, s78\n"
    "v_mfma_f32_16x16x32_bf16 v[160:163], a[144:147], v[88:91], v[160:163]\n"
    "ds_read_b64_tr_b16 a[78:79], v16 offset:17160\n"
    "s_lshl_b32 s67, s67, 4\n"
    "v_mfma_f32_16x16x32_bf16 v[168:171], a[144:147], v[92:95], v[168:171]\n"
    "ds_read_b64_tr_b16 a[80:81], v16 offset:20480\n"
    "buffer_load_dwordx4 v33, s[48:51], s14 offen offset:3072 lds\n"
    "v_mfma_f32_16x16x32_bf16 v[172:175], a[148:151], v[92:95], v[172:175]\n"
    "ds_read_b64_tr_b16 a[82:83], v16 offset:20736\n"
    "s_add_u32 m0, s62, 0x4000\n"
    "s_add_u32 s67, s67, s77\n"
    "v_mfma_f32_16x16x32_bf16 v[164:167], a[148:151], v[88:91], v[164:167]\n"
    "ds_read_b64_tr_b16 a[84:85], v16 offset:20488\n"
    "ds_read_b64_tr_b16 a[86:87], v16 offset:20744\n"
    "s_mul_i32 s67, s67, 0x1c000\n"
    "v_mfma_f32_16x16x32_bf16 v[176:179], a[152:155], v[88:91], v[176:179]\n"
    "ds_read_b64_tr_b16 a[88:89], v16 offset:20992\n"
    "s_add_u32 s56, s94, s67\n"
    "s_addc_u32 s57, s95, 0\n"
    "v_mfma_f32_16x16x32_bf16 v[184:187], a[152:155], v[92:95], v[184:187]\n"
    "ds_read_b64_tr_b16 a[90:91], v16 offset:21248\n"
    "s_and_b32 s57, s57, 0xffff\n"
    "v_mfma_f32_16x16x32_bf16 v[188:191], a[156:159], v[92:95], v[188:191]\n"
    "ds_read_b64_tr_b16 a[92:93], v16 offset:21000\n"
    "buffer_load_dwordx4 v116, s[48:51], s14 offen lds\n"
    "v_mfma_f32_16x16x32_bf16 v[180:183], a[156:159], v[88:91], v[180:183]\n"
    "ds_read_b64_tr_b16 a[94:95], v16 offset:21256\n"
    "s_mov_b32 s58, 0x1c000\n"
    "v_mfma_f32_16x16x32_bf16 v[192:195], a[160:163], v[88:91], v[192:195]\n"
    "ds_read_b64_tr_b16 a[96:97], v16 offset:24576\n"
    "s_mov_b32 s59, 0x20000\n"
    "v_mfma_f32_16x16x32_bf16 v[200:203], a[160:163], v[92:95], v[200:203]\n"
    "ds_read_b64_tr_b16 a[98:99], v16 offset:24832\n"
    "s_lshl_b32 s67, s76, 1\n"
    "v_mfma_f32_16x16x32_bf16 v[204:207], a[164:167], v[92:95], v[204:207]\n"
    "ds_read_b64_tr_b16 a[100:101], v16 offset:24584\n"
    "buffer_load_dwordx4 v117, s[48:51], s14 offen offset:1024 lds\n"
    "v_mfma_f32_16x16x32_bf16 v[196:199], a[164:167], v[88:91], v[196:199]\n"
    "ds_read_b64_tr_b16 a[102:103], v16 offset:24840\n"
    "s_add_u32 s67, s67, s78\n"
    "v_mfma_f32_16x16x32_bf16 v[208:211], a[168:171], v[88:91], v[208:211]\n"
    "ds_read_b64_tr_b16 a[104:105], v16 offset:25088\n"
    "s_lshl_b32 s67, s67, 14\n"
    "v_mfma_f32_16x16x32_bf16 v[216:219], a[168:171], v[92:95], v[216:219]\n"
    "ds_read_b64_tr_b16 a[106:107], v16 offset:25344\n"
    "s_add_u32 s67, s67, 0x1e00000\n"
    "v_mfma_f32_16x16x32_bf16 v[220:223], a[172:175], v[92:95], v[220:223]\n"
    "ds_read_b64_tr_b16 a[108:109], v16 offset:25096\n"
    "v_mfma_f32_16x16x32_bf16 v[212:215], a[172:175], v[88:91], v[212:215]\n"
    "ds_read_b64_tr_b16 a[110:111], v16 offset:25352\n"
    "buffer_load_dwordx4 v118, s[48:51], s14 offen offset:2048 lds\n"
    "v_mfma_f32_16x16x32_bf16 v[224:227], a[176:179], v[88:91], v[224:227]\n"
    "ds_read_b64_tr_b16 a[112:113], v16 offset:28672\n"
    "v_mfma_f32_16x16x32_bf16 v[232:235], a[176:179], v[92:95], v[232:235]\n"
    "ds_read_b64_tr_b16 a[114:115], v16 offset:28928\n"
    "s_waitcnt lgkmcnt(15)\n"
    "v_mfma_f32_16x16x32_bf16 v[236:239], a[180:183], v[92:95], v[236:239]\n"
    "ds_read_b64_tr_b16 a[116:117], v16 offset:28680\n"
    "v_mfma_f32_16x16x32_bf16 v[228:231], a[180:183], v[88:91], v[228:231]\n"
    "ds_read_b64_tr_b16 a[118:119], v16 offset:28936\n"
    "buffer_load_dwordx4 v119, s[48:51], s14 offen offset:3072 lds\n"
    "v_mfma_f32_16x16x32_bf16 v[240:243], a[184:187], v[88:91], v[240:243]\n"
    "ds_read_b64_tr_b16 a[120:121], v16 offset:29184\n"
    "v_mfma_f32_16x16x32_bf16 v[248:251], a[184:187], v[92:95], v[248:251]\n"
    "ds_read_b64_tr_b16 a[122:123], v16 offset:29440\n"
    "v_mfma_f32_16x16x32_bf16 v[252:255], a[188:191], v[92:95], v[252:255]\n"
    "ds_read_b64_tr_b16 a[124:125], v16 offset:29192\n"
    "v_mfma_f32_16x16x32_bf16 v[244:247], a[188:191], v[88:91], v[244:247]\n"
    "ds_read_b64_tr_b16 a[126:127], v16 offset:29448\n"
    "s_waitcnt vmcnt(12) lgkmcnt(6)\n"
    "s_barrier\n"
    "L_iw0:\n"
    "s_cbranch_execz L_iw4\n"
    "v_mfma_f32_16x16x32_bf16 v[128:131], a[64:67], v[64:67], v[128:131]\n"
    "ds_read_b64_tr_b16 a[128:129], v14 offset:49152\n"
    "s_add_u32 m0, s62, 0x24000\n"
    "v_mfma_f32_16x16x32_bf16 v[136:139], a[64:67], v[68:71], v[136:139]\n"
    "ds_read_b64_tr_b16 a[130:131], v14 offset:49408\n"
    "buffer_load_dwordx4 v26, s[44:47], s8 offen lds\n"
    "v_mfma_f32_16x16x32_bf16 v[140:143], a[68:71], v[68:71], v[140:143]\n"
    "ds_read_b64_tr_b16 a[132:133], v14 offset:49160\n"
    "s_add_u32 s20, s94, s67\n"
    "s_addc_u32 s21, s95, 0\n"
    "v_mfma_f32_16x16x32_bf16 v[132:135], a[68:71], v[64:67], v[132:135]\n"
    "ds_read_b64_tr_b16 a[134:135], v14 offset:49416\n"
    "s_and_b32 s21, s21, 0xffff\n"
    "v_mfma_f32_16x16x32_bf16 v[144:147], a[72:75], v[64:67], v[144:147]\n"
    "ds_read_b64_tr_b16 a[136:137], v14 offset:49664\n"
    "s_mov_b32 s22, 0x4000\n"
    "v_mfma_f32_16x16x32_bf16 v[152:155], a[72:75], v[68:71], v[152:155]\n"
    "ds_read_b64_tr_b16 a[138:139], v14 offset:49920\n"
    "buffer_load_dwordx4 v27, s[44:47], s8 offen offset:1024 lds\n"
    "v_mfma_f32_16x16x32_bf16 v[156:159], a[76:79], v[68:71], v[156:159]\n"
    "ds_read_b64_tr_b16 a[140:141], v14 offset:49672\n"
    "s_mov_b32 s23, 0x20000\n"
    "v_mfma_f32_16x16x32_bf16 v[148:151], a[76:79], v[64:67], v[148:151]\n"
    "ds_read_b64_tr_b16 a[142:143], v14 offset:49928\n"
    "s_lshl_b32 s67, s76, 1\n"
    "v_mfma_f32_16x16x32_bf16 v[160:163], a[80:83], v[64:67], v[160:163]\n"
    "ds_read_b64_tr_b16 a[144:145], v14 offset:53248\n"
    "s_add_u32 s67, s67, s78\n"
    "v_mfma_f32_16x16x32_bf16 v[168:171], a[80:83], v[68:71], v[168:171]\n"
    "ds_read_b64_tr_b16 a[146:147], v14 offset:53504\n"
    "buffer_load_dwordx4 v28, s[44:47], s8 offen offset:2048 lds\n"
    "v_mfma_f32_16x16x32_bf16 v[172:175], a[84:87], v[68:71], v[172:175]\n"
    "ds_read_b64_tr_b16 a[148:149], v14 offset:53256\n"
    "s_mul_i32 s67, s67, 0x1c0000\n"
    "v_mfma_f32_16x16x32_bf16 v[164:167], a[84:87], v[64:67], v[164:167]\n"
    "ds_read_b64_tr_b16 a[150:151], v14 offset:53512\n"
    "s_add_u32 s24, s94, s67\n"
    "s_addc_u32 s25, s95, 0\n"
    "v_mfma_f32_16x16x32_bf16 v[176:179], a[88:91], v[64:67], v[176:179]\n"
    "ds_read_b64_tr_b16 a[152:153], v14 offset:53760\n"
    "s_and_b32 s25, s25, 0xffff\n"
    "v_mfma_f32_16x16x32_bf16 v[184:187], a[88:91], v[68:71], v[184:187]\n"
    "ds_read_b64_tr_b16 a[154:155], v14 offset:54016\n"
    "s_mov_b32 s26, 0x1c0000\n"
    "v_mfma_f32_16x16x32_bf16 v[188:191], a[92:95], v[68:71], v[188:191]\n"
    "ds_read_b64_tr_b16 a[156:157], v14 offset:53768\n"
    "buffer_load_dwordx4 v29, s[44:47], s8 offen offset:3072 lds\n"
    "v_mfma_f32_16x16x32_bf16 v[180:183], a[92:95], v[64:67], v[180:183]\n"
    "ds_read_b64_tr_b16 a[158:159], v14 offset:54024\n"
    "s_add_u32 m0, s62, 0x1c000\n"
    "s_mov_b32 s27, 0x20000\n"
    "v_mfma_f32_16x16x32_bf16 v[192:195], a[96:99], v[64:67], v[192:195]\n"
    "ds_read_b64_tr_b16 a[160:161], v14 offset:57344\n"
    "s_lshl_b32 s67, s76, 1\n"
    "v_mfma_f32_16x16x32_bf16 v[200:203], a[96:99], v[68:71], v[200:203]\n"
    "ds_read_b64_tr_b16 a[162:163], v14 offset:57600\n"
    "s_add_u32 s67, s67, s78\n"
    "v_mfma_f32_16x16x32_bf16 v[204:207], a[100:103], v[68:71], v[204:207]\n"
    "ds_read_b64_tr_b16 a[164:165], v14 offset:57352\n"
    "buffer_load_dwordx4 v30, s[48:51], s15 offen lds\n"
    "v_mfma_f32_16x16x32_bf16 v[196:199], a[100:103], v[64:67], v[196:199]\n"
    "ds_read_b64_tr_b16 a[166:167], v14 offset:57608\n"
    "s_lshl_b32 s67, s67, 3\n"
    "v_mfma_f32_16x16x32_bf16 v[208:211], a[104:107], v[64:67], v[208:211]\n"
    "ds_read_b64_tr_b16 a[168:169], v14 offset:57856\n"
    "s_and_b32 s68, s77, 1\n"
    "v_mfma_f32_16x16x32_bf16 v[216:219], a[104:107], v[68:71], v[216:219]\n"
    "ds_read_b64_tr_b16 a[170:171], v14 offset:58112\n"
    "s_add_u32 s67, s67, s68\n"
    "v_mfma_f32_16x16x32_bf16 v[220:223], a[108:111], v[68:71], v[220:223]\n"
    "ds_read_b64_tr_b16 a[172:173], v14 offset:57864\n"
    "s_mul_i32 s67, s67, 0x3800\n"
    "v_mfma_f32_16x16x32_bf16 v[212:215], a[108:111], v[64:67], v[212:215]\n"
    "ds_read_b64_tr_b16 a[174:175], v14 offset:58120\n"
    "buffer_load_dwordx4 v31, s[48:51], s15 offen offset:1024 lds\n"
    "v_mfma_f32_16x16x32_bf16 v[224:227], a[112:115], v[64:67], v[224:227]\n"
    "ds_read_b64_tr_b16 a[176:177], v14 offset:61440\n"
    "v_mfma_f32_16x16x32_bf16 v[232:235], a[112:115], v[68:71], v[232:235]\n"
    "ds_read_b64_tr_b16 a[178:179], v14 offset:61696\n"
    "s_waitcnt lgkmcnt(15)\n"
    "v_mfma_f32_16x16x32_bf16 v[236:239], a[116:119], v[68:71], v[236:239]\n"
    "ds_read_b64_tr_b16 a[180:181], v14 offset:61448\n"
    "v_mfma_f32_16x16x32_bf16 v[228:231], a[116:119], v[64:67], v[228:231]\n"
    "ds_read_b64_tr_b16 a[182:183], v14 offset:61704\n"
    "buffer_load_dwordx4 v32, s[48:51], s15 offen offset:2048 lds\n"
    "v_mfma_f32_16x16x32_bf16 v[240:243], a[120:123], v[64:67], v[240:243]\n"
    "ds_read_b64_tr_b16 a[184:185], v14 offset:61952\n"
    "v_mfma_f32_16x16x32_bf16 v[248:251], a[120:123], v[68:71], v[248:251]\n"
    "ds_read_b64_tr_b16 a[186:187], v14 offset:62208\n"
    "v_mfma_f32_16x16x32_bf16 v[252:255], a[124:127], v[68:71], v[252:255]\n"
    "ds_read_b64_tr_b16 a[188:189], v14 offset:61960\n"
    "v_mfma_f32_16x16x32_bf16 v[244:247], a[124:127], v[64:67], v[244:247]\n"
    "ds_read_b64_tr_b16 a[190:191], v14 offset:62216\n"
    "s_waitcnt vmcnt(11) lgkmcnt(6)\n"
    "s_barrier\n"
    "v_mfma_f32_16x16x32_bf16 v[128:131], a[128:131], v[72:75], v[128:131]\n"
    "ds_read_b128 v[80:83], v6 offset:0\n"
    "s_and_b32 s68, s77, 2\n"
    "v_mfma_f32_16x16x32_bf16 v[136:139], a[128:131], v[76:79], v[136:139]\n"
    "ds_read_b128 v[84:87], v96 offset:0\n"
    "buffer_load_dwordx4 v33, s[48:51], s15 offen offset:3072 lds\n"
    "v_mfma_f32_16x16x32_bf16 v[140:143], a[132:135], v[76:79], v[140:143]\n"
    "ds_read_b128 v[88:91], v7 offset:0\n"
    "s_add_u32 m0, s62, 0x8000\n"
    "s_lshl_b32 s68, s68, 5\n"
    "v_mfma_f32_16x16x32_bf16 v[132:135], a[132:135], v[72:75], v[132:135]\n"
    "ds_read_b128 v[92:95], v97 offset:0\n"
    "s_add_u32 s67, s67, s68\n"
    "v_mfma_f32_16x16x32_bf16 v[144:147], a[136:139], v[72:75], v[144:147]\n"
    "ds_read_b64_tr_b16 a[64:65], v14 offset:0\n"
    "s_add_u32 s28, s96, s67\n"
    "s_addc_u32 s29, s97, 0\n"
    "v_mfma_f32_16x16x32_bf16 v[152:155], a[136:139], v[76:79], v[152:155]\n"
    "ds_read_b64_tr_b16 a[66:67], v14 offset:256\n"
    "buffer_load_dwordx4 v116, s[48:51], s15 offen lds\n"
    "v_mfma_f32_16x16x32_bf16 v[156:159], a[140:143], v[76:79], v[156:159]\n"
    "ds_read_b64_tr_b16 a[68:69], v14 offset:8\n"
    "ds_read_b64_tr_b16 a[70:71], v14 offset:264\n"
    "s_and_b32 s29, s29, 0xffff\n"
    "v_mfma_f32_16x16x32_bf16 v[148:151], a[140:143], v[72:75], v[148:151]\n"
    "ds_read_b64_tr_b16 a[72:73], v14 offset:512\n"
    "ds_read_b64_tr_b16 a[74:75], v14 offset:768\n"
    "s_mov_b32 s30, 0x187c0\n"
    "v_mfma_f32_16x16x32_bf16 v[160:163], a[144:147], v[72:75], v[160:163]\n"
    "ds_read_b64_tr_b16 a[76:77], v14 offset:520\n"
    "s_mov_b32 s31, 0x20000\n"
    "v_mfma_f32_16x16x32_bf16 v[168:171], a[144:147], v[76:79], v[168:171]\n"
    "ds_read_b64_tr_b16 a[78:79], v14 offset:776\n"
    "buffer_load_dwordx4 v117, s[48:51], s15 offen offset:1024 lds\n"
    "v_mfma_f32_16x16x32_bf16 v[172:175], a[148:151], v[76:79], v[172:175]\n"
    "ds_read_b64_tr_b16 a[80:81], v14 offset:4096\n"
    "ds_read_b64_tr_b16 a[82:83], v14 offset:4352\n"
    "s_and_b32 s67, s77, 2\n"
    "v_mfma_f32_16x16x32_bf16 v[164:167], a[148:151], v[72:75], v[164:167]\n"
    "ds_read_b64_tr_b16 a[84:85], v14 offset:4104\n"
    "ds_read_b64_tr_b16 a[86:87], v14 offset:4360\n"
    "s_lshl_b32 s67, s67, 5\n"
    "v_mfma_f32_16x16x32_bf16 v[176:179], a[152:155], v[72:75], v[176:179]\n"
    "ds_read_b64_tr_b16 a[88:89], v14 offset:4608\n"
    "v_xor_b32 v49, s67, v49\n"
    "v_mfma_f32_16x16x32_bf16 v[184:187], a[152:155], v[76:79], v[184:187]\n"
    "ds_read_b64_tr_b16 a[90:91], v14 offset:4864\n"
    "s_mul_i32 s67, s69, 0x7000\n"
    "v_mfma_f32_16x16x32_bf16 v[188:191], a[156:159], v[76:79], v[188:191]\n"
    "ds_read_b64_tr_b16 a[92:93], v14 offset:4616\n"
    "buffer_load_dwordx4 v118, s[48:51], s15 offen offset:2048 lds\n"
    "v_mfma_f32_16x16x32_bf16 v[180:183], a[156:159], v[72:75], v[180:183]\n"
    "ds_read_b64_tr_b16 a[94:95], v14 offset:4872\n"
    "s_movk_i32 s68, 0x380\n"
    "v_mfma_f32_16x16x32_bf16 v[192:195], a[160:163], v[72:75], v[192:195]\n"
    "ds_read_b64_tr_b16 a[96:97], v14 offset:8192\n"
    "v_add_u32 v3, s67, v49\n"
    "v_mfma_f32_16x16x32_bf16 v[200:203], a[160:163], v[76:79], v[200:203]\n"
    "ds_read_b64_tr_b16 a[98:99], v14 offset:8448\n"
    "v_mad_u32_u24 v34, v50, s68, v3\n"
    "v_mfma_f32_16x16x32_bf16 v[204:207], a[164:167], v[76:79], v[204:207]\n"
    "ds_read_b64_tr_b16 a[100:101], v14 offset:8200\n"
    "buffer_load_dwordx4 v119, s[48:51], s15 offen offset:3072 lds\n"
    "v_mfma_f32_16x16x32_bf16 v[196:199], a[164:167], v[72:75], v[196:199]\n"
    "ds_read_b64_tr_b16 a[102:103], v14 offset:8456\n"
    "s_add_u32 m0, s62, 0x10000\n"
    "v_add_u32 v35, 0x1800, v34\n"
    "v_mfma_f32_16x16x32_bf16 v[208:211], a[168:171], v[72:75], v[208:211]\n"
    "ds_read_b64_tr_b16 a[104:105], v14 offset:8704\n"
    "v_add_u32 v36, 0x3000, v34\n"
    "v_mfma_f32_16x16x32_bf16 v[216:219], a[168:171], v[76:79], v[216:219]\n"
    "ds_read_b64_tr_b16 a[106:107], v14 offset:8960\n"
    "v_add_u32 v37, 0x4800, v34\n"
    "v_mfma_f32_16x16x32_bf16 v[220:223], a[172:175], v[76:79], v[220:223]\n"
    "ds_read_b64_tr_b16 a[108:109], v14 offset:8712\n"
    "v_mfma_f32_16x16x32_bf16 v[212:215], a[172:175], v[72:75], v[212:215]\n"
    "ds_read_b64_tr_b16 a[110:111], v14 offset:8968\n"
    "buffer_load_dwordx4 v26, s[44:47], s9 offen lds\n"
    "v_mfma_f32_16x16x32_bf16 v[224:227], a[176:179], v[72:75], v[224:227]\n"
    "ds_read_b64_tr_b16 a[112:113], v14 offset:12288\n"
    "v_mfma_f32_16x16x32_bf16 v[232:235], a[176:179], v[76:79], v[232:235]\n"
    "ds_read_b64_tr_b16 a[114:115], v14 offset:12544\n"
    "s_waitcnt lgkmcnt(15)\n"
    "v_mfma_f32_16x16x32_bf16 v[236:239], a[180:183], v[76:79], v[236:239]\n"
    "ds_read_b64_tr_b16 a[116:117], v14 offset:12296\n"
    "v_mfma_f32_16x16x32_bf16 v[228:231], a[180:183], v[72:75], v[228:231]\n"
    "ds_read_b64_tr_b16 a[118:119], v14 offset:12552\n"
    "buffer_load_dwordx4 v27, s[44:47], s9 offen offset:1024 lds\n"
    "v_mfma_f32_16x16x32_bf16 v[240:243], a[184:187], v[72:75], v[240:243]\n"
    "ds_read_b64_tr_b16 a[120:121], v14 offset:12800\n"
    "v_mfma_f32_16x16x32_bf16 v[248:251], a[184:187], v[76:79], v[248:251]\n"
    "ds_read_b64_tr_b16 a[122:123], v14 offset:13056\n"
    "v_mfma_f32_16x16x32_bf16 v[252:255], a[188:191], v[76:79], v[252:255]\n"
    "ds_read_b64_tr_b16 a[124:125], v14 offset:12808\n"
    "v_mfma_f32_16x16x32_bf16 v[244:247], a[188:191], v[72:75], v[244:247]\n"
    "ds_read_b64_tr_b16 a[126:127], v14 offset:13064\n"
    "s_waitcnt vmcnt(14) lgkmcnt(6)\n"
    "s_barrier\n"
    "v_mfma_f32_16x16x32_bf16 v[128:131], a[64:67], v[80:83], v[128:131]\n"
    "ds_read_b64_tr_b16 a[128:129], v14 offset:16384\n"
    "v_lshlrev_b32 v45, 4, v0\n"
    "v_mfma_f32_16x16x32_bf16 v[136:139], a[64:67], v[84:87], v[136:139]\n"
    "ds_read_b64_tr_b16 a[130:131], v14 offset:16640\n"
    "v_add_u32 v45, s62, v45\n"
    "v_mfma_f32_16x16x32_bf16 v[140:143], a[68:71], v[84:87], v[140:143]\n"
    "ds_read_b64_tr_b16 a[132:133], v14 offset:16392\n"
    "s_mov_b32 s65, 0x3db8aa3b\n"
    "v_mfma_f32_16x16x32_bf16 v[132:135], a[68:71], v[80:83], v[132:135]\n"
    "ds_read_b64_tr_b16 a[134:135], v14 offset:16648\n"
    "s_mov_b32 s66, 0x44800000\n"
    "v_mfma_f32_16x16x32_bf16 v[144:147], a[72:75], v[80:83], v[144:147]\n"
    "ds_read_b64_tr_b16 a[136:137], v14 offset:16896\n"
    "buffer_load_dwordx4 v28, s[44:47], s9 offen offset:2048 lds\n"
    "v_mfma_f32_16x16x32_bf16 v[152:155], a[72:75], v[84:87], v[152:155]\n"
    "ds_read_b64_tr_b16 a[138:139], v14 offset:17152\n"
    "v_mov_b32 v40, 0\n"
    "v_mfma_f32_16x16x32_bf16 v[156:159], a[76:79], v[84:87], v[156:159]\n"
    "ds_read_b64_tr_b16 a[140:141], v14 offset:16904\n"
    "v_mov_b32 v41, 0\n"
    "v_mfma_f32_16x16x32_bf16 v[148:151], a[76:79], v[80:83], v[148:151]\n"
    "ds_read_b64_tr_b16 a[142:143], v14 offset:17160\n"
    "v_mov_b32 v120, 0\n"
    "v_mfma_f32_16x16x32_bf16 v[160:163], a[80:83], v[80:83], v[160:163]\n"
    "ds_read_b64_tr_b16 a[144:145], v14 offset:20480\n"
    "buffer_load_dwordx4 v29, s[44:47], s9 offen offset:3072 lds\n"
    "v_mfma_f32_16x16x32_bf16 v[168:171], a[80:83], v[84:87], v[168:171]\n"
    "ds_read_b64_tr_b16 a[146:147], v14 offset:20736\n"
    "s_add_u32 m0, s62, 0x14000\n"
    "v_mfma_f32_16x16x32_bf16 v[172:175], a[84:87], v[84:87], v[172:175]\n"
    "ds_read_b64_tr_b16 a[148:149], v14 offset:20488\n"
    "v_mov_b32 v121, 0\n"
    "v_mfma_f32_16x16x32_bf16 v[164:167], a[84:87], v[80:83], v[164:167]\n"
    "ds_read_b64_tr_b16 a[150:151], v14 offset:20744\n"
    "v_mov_b32 v122, 0\n"
    "v_mfma_f32_16x16x32_bf16 v[176:179], a[88:91], v[80:83], v[176:179]\n"
    "ds_read_b64_tr_b16 a[152:153], v14 offset:20992\n"
    "buffer_load_dwordx4 v30, s[48:51], s16 offen lds\n"
    "v_mfma_f32_16x16x32_bf16 v[184:187], a[88:91], v[84:87], v[184:187]\n"
    "ds_read_b64_tr_b16 a[154:155], v14 offset:21248\n"
    "v_mov_b32 v123, 0\n"
    "v_mfma_f32_16x16x32_bf16 v[188:191], a[92:95], v[84:87], v[188:191]\n"
    "ds_read_b64_tr_b16 a[156:157], v14 offset:21000\n"
    "v_mov_b32 v124, 0\n"
    "v_mfma_f32_16x16x32_bf16 v[180:183], a[92:95], v[80:83], v[180:183]\n"
    "ds_read_b64_tr_b16 a[158:159], v14 offset:21256\n"
    "v_mov_b32 v125, 0\n"
    "v_mfma_f32_16x16x32_bf16 v[192:195], a[96:99], v[80:83], v[192:195]\n"
    "ds_read_b64_tr_b16 a[160:161], v14 offset:24576\n"
    "buffer_load_dwordx4 v31, s[48:51], s16 offen offset:1024 lds\n"
    "v_mfma_f32_16x16x32_bf16 v[200:203], a[96:99], v[84:87], v[200:203]\n"
    "ds_read_b64_tr_b16 a[162:163], v14 offset:24832\n"
    "v_mov_b32 v126, 0\n"
    "v_mfma_f32_16x16x32_bf16 v[204:207], a[100:103], v[84:87], v[204:207]\n"
    "ds_read_b64_tr_b16 a[164:165], v14 offset:24584\n"
    "v_mov_b32 v127, 0\n"
    "v_mfma_f32_16x16x32_bf16 v[196:199], a[100:103], v[80:83], v[196:199]\n"
    "ds_read_b64_tr_b16 a[166:167], v14 offset:24840\n"
    "s_mov_b32 s71, 0x3a800000\n"
    "v_mfma_f32_16x16x32_bf16 v[208:211], a[104:107], v[80:83], v[208:211]\n"
    "ds_read_b64_tr_b16 a[168:169], v14 offset:25088\n"
    "buffer_load_dwordx4 v32, s[48:51], s16 offen offset:2048 lds\n"
    "v_mfma_f32_16x16x32_bf16 v[216:219], a[104:107], v[84:87], v[216:219]\n"
    "ds_read_b64_tr_b16 a[170:171], v14 offset:25344\n"
    "v_mfma_f32_16x16x32_bf16 v[220:223], a[108:111], v[84:87], v[220:223]\n"
    "ds_read_b64_tr_b16 a[172:173], v14 offset:25096\n"
    "v_mfma_f32_16x16x32_bf16 v[212:215], a[108:111], v[80:83], v[212:215]\n"
    "ds_read_b64_tr_b16 a[174:175], v14 offset:25352\n"
    "v_mfma_f32_16x16x32_bf16 v[224:227], a[112:115], v[80:83], v[224:227]\n"
    "ds_read_b64_tr_b16 a[176:177], v14 offset:28672\n"
    "buffer_load_dwordx4 v33, s[48:51], s16 offen offset:3072 lds\n"
    "v_mfma_f32_16x16x32_bf16 v[232:235], a[112:115], v[84:87], v[232:235]\n"
    "ds_read_b64_tr_b16 a[178:179], v14 offset:28928\n"
    "s_add_u32 m0, s62, 0xc000\n"
    "s_waitcnt lgkmcnt(15)\n"
    "v_mfma_f32_16x16x32_bf16 v[236:239], a[116:119], v[84:87], v[236:239]\n"
    "ds_read_b64_tr_b16 a[180:181], v14 offset:28680\n"
    "v_mfma_f32_16x16x32_bf16 v[228:231], a[116:119], v[80:83], v[228:231]\n"
    "ds_read_b64_tr_b16 a[182:183], v14 offset:28936\n"
    "v_mfma_f32_16x16x32_bf16 v[240:243], a[120:123], v[80:83], v[240:243]\n"
    "ds_read_b64_tr_b16 a[184:185], v14 offset:29184\n"
    "buffer_load_dwordx4 v116, s[48:51], s16 offen lds\n"
    "v_mfma_f32_16x16x32_bf16 v[248:251], a[120:123], v[84:87], v[248:251]\n"
    "ds_read_b64_tr_b16 a[186:187], v14 offset:29440\n"
    "v_mfma_f32_16x16x32_bf16 v[252:255], a[124:127], v[84:87], v[252:255]\n"
    "ds_read_b64_tr_b16 a[188:189], v14 offset:29192\n"
    "v_mfma_f32_16x16x32_bf16 v[244:247], a[124:127], v[80:83], v[244:247]\n"
    "ds_read_b64_tr_b16 a[190:191], v14 offset:29448\n"
    "s_waitcnt vmcnt(13) lgkmcnt(6)\n"
    "s_barrier\n"
    "v_mfma_f32_16x16x32_bf16 v[128:131], a[128:131], v[88:91], v[128:131]\n"
    "ds_read_b128 v[64:67], v6 offset:16384\n"
    "v_mfma_f32_16x16x32_bf16 v[136:139], a[128:131], v[92:95], v[136:139]\n"
    "ds_read_b128 v[68:71], v96 offset:16384\n"
    "buffer_load_dwordx4 v117, s[48:51], s16 offen offset:1024 lds\n"
    "v_mfma_f32_16x16x32_bf16 v[140:143], a[132:135], v[92:95], v[140:143]\n"
    "ds_read_b128 v[72:75], v7 offset:16384\n"
    "v_mfma_f32_16x16x32_bf16 v[132:135], a[132:135], v[88:91], v[132:135]\n"
    "ds_read_b128 v[76:79], v97 offset:16384\n"
    "v_mfma_f32_16x16x32_bf16 v[144:147], a[136:139], v[88:91], v[144:147]\n"
    "ds_read_b64_tr_b16 a[64:65], v16 offset:49152\n"
    "ds_read_b64_tr_b16 a[66:67], v16 offset:49408\n"
    "v_mfma_f32_16x16x32_bf16 v[152:155], a[136:139], v[92:95], v[152:155]\n"
    "ds_read_b64_tr_b16 a[68:69], v16 offset:49160\n"
    "buffer_load_dwordx4 v118, s[48:51], s16 offen offset:2048 lds\n"
    "v_mfma_f32_16x16x32_bf16 v[156:159], a[140:143], v[92:95], v[156:159]\n"
    "ds_read_b64_tr_b16 a[70:71], v16 offset:49416\n"
    "ds_read_b64_tr_b16 a[72:73], v16 offset:49664\n"
    "v_mfma_f32_16x16x32_bf16 v[148:151], a[140:143], v[88:91], v[148:151]\n"
    "ds_read_b64_tr_b16 a[74:75], v16 offset:49920\n"
    "v_mfma_f32_16x16x32_bf16 v[160:163], a[144:147], v[88:91], v[160:163]\n"
    "ds_read_b64_tr_b16 a[76:77], v16 offset:49672\n"
    "v_mfma_f32_16x16x32_bf16 v[168:171], a[144:147], v[92:95], v[168:171]\n"
    "ds_read_b64_tr_b16 a[78:79], v16 offset:49928\n"
    "buffer_load_dwordx4 v119, s[48:51], s16 offen offset:3072 lds\n"
    "v_mfma_f32_16x16x32_bf16 v[172:175], a[148:151], v[92:95], v[172:175]\n"
    "ds_read_b64_tr_b16 a[80:81], v16 offset:53248\n"
    "ds_read_b64_tr_b16 a[82:83], v16 offset:53504\n"
    "s_add_u32 m0, s62, 0x20000\n"
    "v_mfma_f32_16x16x32_bf16 v[164:167], a[148:151], v[88:91], v[164:167]\n"
    "ds_read_b64_tr_b16 a[84:85], v16 offset:53256\n"
    "v_mfma_f32_16x16x32_bf16 v[176:179], a[152:155], v[88:91], v[176:179]\n"
    "ds_read_b64_tr_b16 a[86:87], v16 offset:53512\n"
    "v_mfma_f32_16x16x32_bf16 v[184:187], a[152:155], v[92:95], v[184:187]\n"
    "ds_read_b64_tr_b16 a[88:89], v16 offset:53760\n"
    "ds_read_b64_tr_b16 a[90:91], v16 offset:54016\n"
    "v_mfma_f32_16x16x32_bf16 v[188:191], a[156:159], v[92:95], v[188:191]\n"
    "ds_read_b64_tr_b16 a[92:93], v16 offset:53768\n"
    "buffer_load_dwordx4 v26, s[44:47], s10 offen lds\n"
    "v_mfma_f32_16x16x32_bf16 v[180:183], a[156:159], v[88:91], v[180:183]\n"
    "ds_read_b64_tr_b16 a[94:95], v16 offset:54024\n"
    "ds_read_b64_tr_b16 a[96:97], v16 offset:57344\n"
    "v_mfma_f32_16x16x32_bf16 v[192:195], a[160:163], v[88:91], v[192:195]\n"
    "ds_read_b64_tr_b16 a[98:99], v16 offset:57600\n"
    "v_mfma_f32_16x16x32_bf16 v[200:203], a[160:163], v[92:95], v[200:203]\n"
    "ds_read_b64_tr_b16 a[100:101], v16 offset:57352\n"
    "v_mfma_f32_16x16x32_bf16 v[204:207], a[164:167], v[92:95], v[204:207]\n"
    "ds_read_b64_tr_b16 a[102:103], v16 offset:57608\n"
    "buffer_load_dwordx4 v27, s[44:47], s10 offen offset:1024 lds\n"
    "v_mfma_f32_16x16x32_bf16 v[196:199], a[164:167], v[88:91], v[196:199]\n"
    "ds_read_b64_tr_b16 a[104:105], v16 offset:57856\n"
    "v_mfma_f32_16x16x32_bf16 v[208:211], a[168:171], v[88:91], v[208:211]\n"
    "ds_read_b64_tr_b16 a[106:107], v16 offset:58112\n"
    "v_mfma_f32_16x16x32_bf16 v[216:219], a[168:171], v[92:95], v[216:219]\n"
    "ds_read_b64_tr_b16 a[108:109], v16 offset:57864\n"
    "v_mfma_f32_16x16x32_bf16 v[220:223], a[172:175], v[92:95], v[220:223]\n"
    "ds_read_b64_tr_b16 a[110:111], v16 offset:58120\n"
    "v_mfma_f32_16x16x32_bf16 v[212:215], a[172:175], v[88:91], v[212:215]\n"
    "buffer_load_dwordx4 v28, s[44:47], s10 offen offset:2048 lds\n"
    "v_mfma_f32_16x16x32_bf16 v[224:227], a[176:179], v[88:91], v[224:227]\n"
    "ds_read_b64_tr_b16 a[112:113], v16 offset:61440\n"
    "v_mfma_f32_16x16x32_bf16 v[232:235], a[176:179], v[92:95], v[232:235]\n"
    "ds_read_b64_tr_b16 a[114:115], v16 offset:61696\n"
    "s_waitcnt lgkmcnt(15)\n"
    "v_mfma_f32_16x16x32_bf16 v[236:239], a[180:183], v[92:95], v[236:239]\n"
    "ds_read_b64_tr_b16 a[116:117], v16 offset:61448\n"
    "v_mfma_f32_16x16x32_bf16 v[228:231], a[180:183], v[88:91], v[228:231]\n"
    "buffer_load_dwordx4 v29, s[44:47], s10 offen offset:3072 lds\n"
    "v_mfma_f32_16x16x32_bf16 v[240:243], a[184:187], v[88:91], v[240:243]\n"
    "ds_read_b64_tr_b16 a[118:119], v16 offset:61704\n"
    "v_mfma_f32_16x16x32_bf16 v[248:251], a[184:187], v[92:95], v[248:251]\n"
    "ds_read_b64_tr_b16 a[120:121], v16 offset:61952\n"
    "v_mfma_f32_16x16x32_bf16 v[252:255], a[188:191], v[92:95], v[252:255]\n"
    "ds_read_b64_tr_b16 a[122:123], v16 offset:62208\n"
    "ds_read_b64_tr_b16 a[124:125], v16 offset:61960\n"
    "v_mfma_f32_16x16x32_bf16 v[244:247], a[188:191], v[88:91], v[244:247]\n"
    "ds_read_b64_tr_b16 a[126:127], v16 offset:62216\n"
    "s_waitcnt vmcnt(16) lgkmcnt(6)\n"
    "s_barrier\n"
    "v_mfma_f32_16x16x32_bf16 v[128:131], a[64:67], v[64:67], v[128:131]\n"
    "ds_read_b64_tr_b16 a[128:129], v14 offset:32768\n"
    "s_add_u32 m0, s62, 0x18000\n"
    "v_mfma_f32_16x16x32_bf16 v[136:139], a[64:67], v[68:71], v[136:139]\n"
    "ds_read_b64_tr_b16 a[130:131], v14 offset:33024\n"
    "v_mfma_f32_16x16x32_bf16 v[140:143], a[68:71], v[68:71], v[140:143]\n"
    "ds_read_b64_tr_b16 a[132:133], v14 offset:32776\n"
    "buffer_load_dwordx4 v30, s[48:51], s17 offen lds\n"
    "v_mfma_f32_16x16x32_bf16 v[132:135], a[68:71], v[64:67], v[132:135]\n"
    "ds_read_b64_tr_b16 a[134:135], v14 offset:33032\n"
    "v_mfma_f32_16x16x32_bf16 v[144:147], a[72:75], v[64:67], v[144:147]\n"
    "ds_read_b64_tr_b16 a[136:137], v14 offset:33280\n"
    "v_mfma_f32_16x16x32_bf16 v[152:155], a[72:75], v[68:71], v[152:155]\n"
    "ds_read_b64_tr_b16 a[138:139], v14 offset:33536\n"
    "v_mfma_f32_16x16x32_bf16 v[156:159], a[76:79], v[68:71], v[156:159]\n"
    "ds_read_b64_tr_b16 a[140:141], v14 offset:33288\n"
    "v_mfma_f32_16x16x32_bf16 v[148:151], a[76:79], v[64:67], v[148:151]\n"
    "ds_read_b64_tr_b16 a[142:143], v14 offset:33544\n"
    "v_mfma_f32_16x16x32_bf16 v[160:163], a[80:83], v[64:67], v[160:163]\n"
    "ds_read_b64_tr_b16 a[144:145], v14 offset:36864\n"
    "buffer_load_dwordx4 v31, s[48:51], s17 offen offset:1024 lds\n"
    "v_mfma_f32_16x16x32_bf16 v[168:171], a[80:83], v[68:71], v[168:171]\n"
    "ds_read_b64_tr_b16 a[146:147], v14 offset:37120\n"
    "v_mfma_f32_16x16x32_bf16 v[172:175], a[84:87], v[68:71], v[172:175]\n"
    "ds_read_b64_tr_b16 a[148:149], v14 offset:36872\n"
    "v_mfma_f32_16x16x32_bf16 v[164:167], a[84:87], v[64:67], v[164:167]\n"
    "ds_read_b64_tr_b16 a[150:151], v14 offset:37128\n"
    "v_mfma_f32_16x16x32_bf16 v[176:179], a[88:91], v[64:67], v[176:179]\n"
    "ds_read_b64_tr_b16 a[152:153], v14 offset:37376\n"
    "v_mfma_f32_16x16x32_bf16 v[184:187], a[88:91], v[68:71], v[184:187]\n"
    "ds_read_b64_tr_b16 a[154:155], v14 offset:37632\n"
    "v_mfma_f32_16x16x32_bf16 v[188:191], a[92:95], v[68:71], v[188:191]\n"
    "ds_read_b64_tr_b16 a[156:157], v14 offset:37384\n"
    "v_mfma_f32_16x16x32_bf16 v[180:183], a[92:95], v[64:67], v[180:183]\n"
    "ds_read_b64_tr_b16 a[158:159], v14 offset:37640\n"
    "v_mfma_f32_16x16x32_bf16 v[192:195], a[96:99], v[64:67], v[192:195]\n"
    "ds_read_b64_tr_b16 a[160:161], v14 offset:40960\n"
    "v_mfma_f32_16x16x32_bf16 v[200:203], a[96:99], v[68:71], v[200:203]\n"
    "ds_read_b64_tr_b16 a[162:163], v14 offset:41216\n"
    "v_mfma_f32_16x16x32_bf16 v[204:207], a[100:103], v[68:71], v[204:207]\n"
    "ds_read_b64_tr_b16 a[164:165], v14 offset:40968\n"
    "buffer_load_dwordx4 v32, s[48:51], s17 offen offset:2048 lds\n"
    "v_mfma_f32_16x16x32_bf16 v[196:199], a[100:103], v[64:67], v[196:199]\n"
    "ds_read_b64_tr_b16 a[166:167], v14 offset:41224\n"
    "v_mfma_f32_16x16x32_bf16 v[208:211], a[104:107], v[64:67], v[208:211]\n"
    "ds_read_b64_tr_b16 a[168:169], v14 offset:41472\n"
    "v_mfma_f32_16x16x32_bf16 v[216:219], a[104:107], v[68:71], v[216:219]\n"
    "ds_read_b64_tr_b16 a[170:171], v14 offset:41728\n"
    "v_mfma_f32_16x16x32_bf16 v[220:223], a[108:111], v[68:71], v[220:223]\n"
    "ds_read_b64_tr_b16 a[172:173], v14 offset:41480\n"
    "v_mfma_f32_16x16x32_bf16 v[212:215], a[108:111], v[64:67], v[212:215]\n"
    "ds_read_b64_tr_b16 a[174:175], v14 offset:41736\n"
    "v_mfma_f32_16x16x32_bf16 v[224:227], a[112:115], v[64:67], v[224:227]\n"
    "ds_read_b64_tr_b16 a[176:177], v14 offset:45056\n"
    "v_mfma_f32_16x16x32_bf16 v[232:235], a[112:115], v[68:71], v[232:235]\n"
    "ds_read_b64_tr_b16 a[178:179], v14 offset:45312\n"
    "buffer_load_dwordx4 v33, s[48:51], s17 offen offset:3072 lds\n"
    "s_waitcnt lgkmcnt(15)\n"
    "v_mfma_f32_16x16x32_bf16 v[236:239], a[116:119], v[68:71], v[236:239]\n"
    "ds_read_b64_tr_b16 a[180:181], v14 offset:45064\n"
    "v_mfma_f32_16x16x32_bf16 v[228:231], a[116:119], v[64:67], v[228:231]\n"
    "ds_read_b64_tr_b16 a[182:183], v14 offset:45320\n"
    "v_mfma_f32_16x16x32_bf16 v[240:243], a[120:123], v[64:67], v[240:243]\n"
    "ds_read_b64_tr_b16 a[184:185], v14 offset:45568\n"
    "v_mfma_f32_16x16x32_bf16 v[248:251], a[120:123], v[68:71], v[248:251]\n"
    "ds_read_b64_tr_b16 a[186:187], v14 offset:45824\n"
    "v_mfma_f32_16x16x32_bf16 v[252:255], a[124:127], v[68:71], v[252:255]\n"
    "ds_read_b64_tr_b16 a[188:189], v14 offset:45576\n"
    "v_mfma_f32_16x16x32_bf16 v[244:247], a[124:127], v[64:67], v[244:247]\n"
    "ds_read_b64_tr_b16 a[190:191], v14 offset:45832\n"
    "s_waitcnt vmcnt(12) lgkmcnt(6)\n"
    "s_barrier\n"
    "v_mfma_f32_16x16x32_bf16 v[128:131], a[128:131], v[72:75], v[128:131]\n"
    "ds_read_b128 v[80:83], v112 offset:0\n"
    "s_add_u32 m0, s62, 0x1c000\n"
    "v_mfma_f32_16x16x32_bf16 v[136:139], a[128:131], v[76:79], v[136:139]\n"
    "ds_read_b128 v[84:87], v98 offset:0\n"
    "buffer_load_dwordx4 v116, s[48:51], s17 offen lds\n"
    "v_mfma_f32_16x16x32_bf16 v[140:143], a[132:135], v[76:79], v[140:143]\n"
    "ds_read_b128 v[88:91], v113 offset:0\n"
    "v_mfma_f32_16x16x32_bf16 v[132:135], a[132:135], v[72:75], v[132:135]\n"
    "ds_read_b128 v[92:95], v99 offset:0\n"
    "v_mfma_f32_16x16x32_bf16 v[144:147], a[136:139], v[72:75], v[144:147]\n"
    "ds_read_b64_tr_b16 a[64:65], v16 offset:16384\n"
    "ds_read_b64_tr_b16 a[66:67], v16 offset:16640\n"
    "v_mfma_f32_16x16x32_bf16 v[152:155], a[136:139], v[76:79], v[152:155]\n"
    "ds_read_b64_tr_b16 a[68:69], v16 offset:16392\n"
    "buffer_load_dwordx4 v117, s[48:51], s17 offen offset:1024 lds\n"
    "v_mfma_f32_16x16x32_bf16 v[156:159], a[140:143], v[76:79], v[156:159]\n"
    "ds_read_b64_tr_b16 a[70:71], v16 offset:16648\n"
    "v_mfma_f32_16x16x32_bf16 v[148:151], a[140:143], v[72:75], v[148:151]\n"
    "ds_read_b64_tr_b16 a[72:73], v16 offset:16896\n"
    "ds_read_b64_tr_b16 a[74:75], v16 offset:17152\n"
    "v_mfma_f32_16x16x32_bf16 v[160:163], a[144:147], v[72:75], v[160:163]\n"
    "ds_read_b64_tr_b16 a[76:77], v16 offset:16904\n"
    "ds_read_b64_tr_b16 a[78:79], v16 offset:17160\n"
    "v_mfma_f32_16x16x32_bf16 v[168:171], a[144:147], v[76:79], v[168:171]\n"
    "buffer_load_dwordx4 v118, s[48:51], s17 offen offset:2048 lds\n"
    "v_mfma_f32_16x16x32_bf16 v[172:175], a[148:151], v[76:79], v[172:175]\n"
    "ds_read_b64_tr_b16 a[80:81], v16 offset:20480\n"
    "ds_read_b64_tr_b16 a[82:83], v16 offset:20736\n"
    "v_mfma_f32_16x16x32_bf16 v[164:167], a[148:151], v[72:75], v[164:167]\n"
    "ds_read_b64_tr_b16 a[84:85], v16 offset:20488\n"
    "v_mfma_f32_16x16x32_bf16 v[176:179], a[152:155], v[72:75], v[176:179]\n"
    "ds_read_b64_tr_b16 a[86:87], v16 offset:20744\n"
    "v_mfma_f32_16x16x32_bf16 v[184:187], a[152:155], v[76:79], v[184:187]\n"
    "ds_read_b64_tr_b16 a[88:89], v16 offset:20992\n"
    "v_mfma_f32_16x16x32_bf16 v[188:191], a[156:159], v[76:79], v[188:191]\n"
    "ds_read_b64_tr_b16 a[90:91], v16 offset:21248\n"
    "buffer_load_dwordx4 v119, s[48:51], s17 offen offset:3072 lds\n"
    "v_mfma_f32_16x16x32_bf16 v[180:183], a[156:159], v[72:75], v[180:183]\n"
    "ds_read_b64_tr_b16 a[92:93], v16 offset:21000\n"
    "s_add_u32 m0, s62, 0\n"
    "v_mfma_f32_16x16x32_bf16 v[192:195], a[160:163], v[72:75], v[192:195]\n"
    "ds_read_b64_tr_b16 a[94:95], v16 offset:21256\n"
    "ds_read_b64_tr_b16 a[96:97], v16 offset:24576\n"
    "s_mov_b32 s67, 0\n"
    "v_mfma_f32_16x16x32_bf16 v[200:203], a[160:163], v[76:79], v[200:203]\n"
    "ds_read_b64_tr_b16 a[98:99], v16 offset:24832\n"
    "v_mfma_f32_16x16x32_bf16 v[204:207], a[164:167], v[76:79], v[204:207]\n"
    "buffer_load_dwordx4 v18, s[36:39], s67 offen nt lds\n"
    "v_mfma_f32_16x16x32_bf16 v[196:199], a[164:167], v[72:75], v[196:199]\n"
    "ds_read_b64_tr_b16 a[100:101], v16 offset:24584\n"
    "ds_read_b64_tr_b16 a[102:103], v16 offset:24840\n"
    "v_mfma_f32_16x16x32_bf16 v[208:211], a[168:171], v[72:75], v[208:211]\n"
    "ds_read_b64_tr_b16 a[104:105], v16 offset:25088\n"
    "ds_read_b64_tr_b16 a[106:107], v16 offset:25344\n"
    "v_mfma_f32_16x16x32_bf16 v[216:219], a[168:171], v[76:79], v[216:219]\n"
    "ds_read_b64_tr_b16 a[108:109], v16 offset:25096\n"
    "v_mfma_f32_16x16x32_bf16 v[220:223], a[172:175], v[76:79], v[220:223]\n"
    "ds_read_b64_tr_b16 a[110:111], v16 offset:25352\n"
    "v_mfma_f32_16x16x32_bf16 v[212:215], a[172:175], v[72:75], v[212:215]\n"
    "ds_read_b64_tr_b16 a[112:113], v16 offset:28672\n"
    "buffer_load_dwordx4 v19, s[36:39], s67 offen offset:1024 nt lds\n"
    "v_mfma_f32_16x16x32_bf16 v[224:227], a[176:179], v[72:75], v[224:227]\n"
    "ds_read_b64_tr_b16 a[114:115], v16 offset:28928\n"
    "v_mfma_f32_16x16x32_bf16 v[232:235], a[176:179], v[76:79], v[232:235]\n"
    "ds_read_b64_tr_b16 a[116:117], v16 offset:28680\n"
    "s_waitcnt lgkmcnt(15)\n"
    "v_mfma_f32_16x16x32_bf16 v[236:239], a[180:183], v[76:79], v[236:239]\n"
    "ds_read_b64_tr_b16 a[118:119], v16 offset:28936\n"
    "v_mfma_f32_16x16x32_bf16 v[228:231], a[180:183], v[72:75], v[228:231]\n"
    "buffer_load_dwordx4 v20, s[36:39], s67 offen offset:2048 nt lds\n"
    "v_mfma_f32_16x16x32_bf16 v[240:243], a[184:187], v[72:75], v[240:243]\n"
    "ds_read_b64_tr_b16 a[120:121], v16 offset:29184\n"
    "v_mfma_f32_16x16x32_bf16 v[248:251], a[184:187], v[76:79], v[248:251]\n"
    "ds_read_b64_tr_b16 a[122:123], v16 offset:29440\n"
    "v_mfma_f32_16x16x32_bf16 v[252:255], a[188:191], v[76:79], v[252:255]\n"
    "ds_read_b64_tr_b16 a[124:125], v16 offset:29192\n"
    "v_mfma_f32_16x16x32_bf16 v[244:247], a[188:191], v[72:75], v[244:247]\n"
    "ds_read_b64_tr_b16 a[126:127], v16 offset:29448\n"
    "s_waitcnt vmcnt(15) lgkmcnt(6)\n"
    "s_barrier\n"
    "v_mfma_f32_16x16x32_bf16 v[128:131], a[64:67], v[80:83], v[128:131]\n"
    "ds_read_b64_tr_b16 a[128:129], v14 offset:49152\n"
    "ds_read_b64_tr_b16 a[130:131], v14 offset:49408\n"
    "s_lshl_b32 s68, s77, 2\n"
    "v_mfma_f32_16x16x32_bf16 v[136:139], a[64:67], v[84:87], v[136:139]\n"
    "buffer_load_dwordx4 v21, s[36:39], s67 offen offset:3072 nt lds\n"
    "v_mfma_f32_16x16x32_bf16 v[140:143], a[68:71], v[84:87], v[140:143]\n"
    "ds_read_b64_tr_b16 a[132:133], v14 offset:49160\n"
    "s_add_u32 m0, s62, 0x4000\n"
    "v_mfma_f32_16x16x32_bf16 v[132:135], a[68:71], v[80:83], v[132:135]\n"
    "ds_read_b64_tr_b16 a[134:135], v14 offset:49416\n"
    "s_mov_b32 s67, 0x4000\n"
    "v_mfma_f32_16x16x32_bf16 v[144:147], a[72:75], v[80:83], v[144:147]\n"
    "ds_read_b64_tr_b16 a[136:137], v14 offset:49664\n"
    "s_add_u32 s68, s68, s69\n"
    "v_mfma_f32_16x16x32_bf16 v[152:155], a[72:75], v[84:87], v[152:155]\n"
    "buffer_load_dwordx4 v18, s[36:39], s67 offen nt lds\n"
    "v_mfma_f32_16x16x32_bf16 v[156:159], a[76:79], v[84:87], v[156:159]\n"
    "ds_read_b64_tr_b16 a[138:139], v14 offset:49920\n"
    "s_lshl_b32 s18, s68, 2\n"
    "v_mfma_f32_16x16x32_bf16 v[148:151], a[76:79], v[80:83], v[148:151]\n"
    "ds_read_b64_tr_b16 a[140:141], v14 offset:49672\n"
    "v_mov_b32 v27, s18\n"
    "L_iw1:\n"
    "s_cbranch_execz L_iw5\n"
    "v_mfma_f32_16x16x32_bf16 v[160:163], a[80:83], v[80:83], v[160:163]\n"
    "ds_read_b64_tr_b16 a[142:143], v14 offset:49928\n"
    "s_lshl_b32 s18, s68, 7\n"
    "v_mfma_f32_16x16x32_bf16 v[168:171], a[80:83], v[84:87], v[168:171]\n"
    "buffer_load_dwordx4 v19, s[36:39], s67 offen offset:1024 nt lds\n"
    "v_mfma_f32_16x16x32_bf16 v[172:175], a[84:87], v[84:87], v[172:175]\n"
    "ds_read_b64_tr_b16 a[144:145], v14 offset:53248\n"
    "s_add_u32 s18, s18, 0x2000\n"
    "v_mfma_f32_16x16x32_bf16 v[164:167], a[84:87], v[80:83], v[164:167]\n"
    "ds_read_b64_tr_b16 a[146:147], v14 offset:53504\n"
    "v_and_b32 v1, 31, v0\n"
    "v_mfma_f32_16x16x32_bf16 v[176:179], a[88:91], v[80:83], v[176:179]\n"
    "ds_read_b64_tr_b16 a[148:149], v14 offset:53256\n"
    "v_lshlrev_b32 v1, 2, v1\n"
    "v_mfma_f32_16x16x32_bf16 v[184:187], a[88:91], v[84:87], v[184:187]\n"
    "ds_read_b64_tr_b16 a[150:151], v14 offset:53512\n"
    "v_add_u32 v28, s18, v1\n"
    "v_mfma_f32_16x16x32_bf16 v[188:191], a[92:95], v[84:87], v[188:191]\n"
    "buffer_load_dwordx4 v20, s[36:39], s67 offen offset:2048 nt lds\n"
    "v_mfma_f32_16x16x32_bf16 v[180:183], a[92:95], v[80:83], v[180:183]\n"
    "ds_read_b64_tr_b16 a[152:153], v14 offset:53760\n"
    "v_and_b32 v1, 15, v0\n"
    "v_mfma_f32_16x16x32_bf16 v[192:195], a[96:99], v[80:83], v[192:195]\n"
    "ds_read_b64_tr_b16 a[154:155], v14 offset:54016\n"
    "ds_read_b64_tr_b16 a[156:157], v14 offset:53768\n"
    "v_lshlrev_b32 v1, 2, v1\n"
    "v_mfma_f32_16x16x32_bf16 v[200:203], a[96:99], v[84:87], v[200:203]\n"
    "ds_read_b64_tr_b16 a[158:159], v14 offset:54024\n"
    "v_add_u32 v26, s18, v1\n"
    "v_mfma_f32_16x16x32_bf16 v[204:207], a[100:103], v[84:87], v[204:207]\n"
    "buffer_load_dwordx4 v21, s[36:39], s67 offen offset:3072 nt lds\n"
    "v_mfma_f32_16x16x32_bf16 v[196:199], a[100:103], v[80:83], v[196:199]\n"
    "ds_read_b64_tr_b16 a[160:161], v14 offset:57344\n"
    "s_add_u32 m0, s62, 0x10000\n"
    "v_mfma_f32_16x16x32_bf16 v[208:211], a[104:107], v[80:83], v[208:211]\n"
    "ds_read_b64_tr_b16 a[162:163], v14 offset:57600\n"
    "s_mov_b32 s67, 0x4000\n"
    "v_mfma_f32_16x16x32_bf16 v[216:219], a[104:107], v[84:87], v[216:219]\n"
    "ds_read_b64_tr_b16 a[164:165], v14 offset:57352\n"
    "ds_read_b64_tr_b16 a[166:167], v14 offset:57608\n"
    "v_mfma_f32_16x16x32_bf16 v[220:223], a[108:111], v[84:87], v[220:223]\n"
    "ds_read_b64_tr_b16 a[168:169], v14 offset:57856\n"
    "ds_read_b64_tr_b16 a[170:171], v14 offset:58112\n"
    "v_mfma_f32_16x16x32_bf16 v[212:215], a[108:111], v[80:83], v[212:215]\n"
    "buffer_load_dwordx4 v22, s[40:43], s67 offen nt lds\n"
    "v_mfma_f32_16x16x32_bf16 v[224:227], a[112:115], v[80:83], v[224:227]\n"
    "ds_read_b64_tr_b16 a[172:173], v14 offset:57864\n"
    "ds_read_b64_tr_b16 a[174:175], v14 offset:58120\n"
    "v_mfma_f32_16x16x32_bf16 v[232:235], a[112:115], v[84:87], v[232:235]\n"
    "ds_read_b64_tr_b16 a[176:177], v14 offset:61440\n"
    "ds_read_b64_tr_b16 a[178:179], v14 offset:61696\n"
    "s_waitcnt lgkmcnt(15)\n"
    "v_mfma_f32_16x16x32_bf16 v[236:239], a[116:119], v[84:87], v[236:239]\n"
    "ds_read_b64_tr_b16 a[180:181], v14 offset:61448\n"
    "ds_read_b64_tr_b16 a[182:183], v14 offset:61704\n"
    "v_mfma_f32_16x16x32_bf16 v[228:231], a[116:119], v[80:83], v[228:231]\n"
    "buffer_load_dwordx4 v23, s[40:43], s67 offen offset:1024 nt lds\n"
    "v_mfma_f32_16x16x32_bf16 v[240:243], a[120:123], v[80:83], v[240:243]\n"
    "buffer_load_dword v62, v27, s[20:23], 0 offen sc1\n"
    "v_mfma_f32_16x16x32_bf16 v[248:251], a[120:123], v[84:87], v[248:251]\n"
    "ds_read_b64_tr_b16 a[184:185], v14 offset:61952\n"
    "ds_read_b64_tr_b16 a[186:187], v14 offset:62208\n"
    "v_mfma_f32_16x16x32_bf16 v[252:255], a[124:127], v[84:87], v[252:255]\n"
    "ds_read_b64_tr_b16 a[188:189], v14 offset:61960\n"
    "ds_read_b64_tr_b16 a[190:191], v14 offset:62216\n"
    "v_mfma_f32_16x16x32_bf16 v[244:247], a[124:127], v[80:83], v[244:247]\n"
    "s_waitcnt vmcnt(15) lgkmcnt(6)\n"
    "s_barrier\n"
    "v_mfma_f32_16x16x32_bf16 v[128:131], a[128:131], v[88:91], v[128:131]\n"
    "ds_read_b128 v[64:67], v6 offset:0\n"
    "s_mov_b32 s11, s19\n"
    "v_and_b32 v1, 15, v0\n"
    "v_mfma_f32_16x16x32_bf16 v[136:139], a[128:131], v[92:95], v[136:139]\n"
    "ds_read_b128 v[68:71], v96 offset:0\n"
    "buffer_load_dwordx4 v24, s[40:43], s67 offen offset:2048 nt lds\n"
    "v_mfma_f32_16x16x32_bf16 v[140:143], a[132:135], v[92:95], v[140:143]\n"
    "ds_read_b128 v[72:75], v7 offset:0\n"
    "s_add_u32 s12, s11, 0x80\n"
    "v_lshlrev_b32 v1, 2, v1\n"
    "v_mfma_f32_16x16x32_bf16 v[132:135], a[132:135], v[88:91], v[132:135]\n"
    "ds_read_b128 v[76:79], v97 offset:0\n"
    "s_cmp_eq_u32 s12, 0x380\n"
    "s_cselect_b32 s12, 0, s12\n"
    "v_mfma_f32_16x16x32_bf16 v[144:147], a[136:139], v[88:91], v[144:147]\n"
    "ds_read_b64_tr_b16 a[64:65], v16 offset:32768\n"
    "s_add_u32 s13, s12, 0x80\n"
    "v_add_u32 v1, s69, v1\n"
    "v_mfma_f32_16x16x32_bf16 v[152:155], a[136:139], v[92:95], v[152:155]\n"
    "ds_read_b64_tr_b16 a[66:67], v16 offset:33024\n"
    "s_cmp_eq_u32 s13, 0x380\n"
    "s_cselect_b32 s13, 0, s13\n"
    "v_mfma_f32_16x16x32_bf16 v[156:159], a[140:143], v[92:95], v[156:159]\n"
    "ds_read_b64_tr_b16 a[68:69], v16 offset:32776\n"
    "buffer_load_dwordx4 v25, s[40:43], s67 offen offset:3072 nt lds\n"
    "v_mfma_f32_16x16x32_bf16 v[148:151], a[140:143], v[88:91], v[148:151]\n"
    "ds_read_b64_tr_b16 a[70:71], v16 offset:33032\n"
    "s_add_u32 m0, s62, 0x8000\n"
    "v_mfma_f32_16x16x32_bf16 v[160:163], a[144:147], v[88:91], v[160:163]\n"
    "ds_read_b64_tr_b16 a[72:73], v16 offset:33280\n"
    "s_mov_b32 s67, 0x8000\n"
    "s_add_u32 s14, s13, 0x80\n"
    "v_mfma_f32_16x16x32_bf16 v[168:171], a[144:147], v[92:95], v[168:171]\n"
    "ds_read_b64_tr_b16 a[74:75], v16 offset:33536\n"
    "s_cmp_eq_u32 s14, 0x380\n"
    "s_cselect_b32 s14, 0, s14\n"
    "v_mfma_f32_16x16x32_bf16 v[172:175], a[148:151], v[92:95], v[172:175]\n"
    "ds_read_b64_tr_b16 a[76:77], v16 offset:33288\n"
    "ds_read_b64_tr_b16 a[78:79], v16 offset:33544\n"
    "v_lshlrev_b32 v29, 2, v1\n"
    "v_mfma_f32_16x16x32_bf16 v[164:167], a[148:151], v[88:91], v[164:167]\n"
    "ds_read_b64_tr_b16 a[80:81], v16 offset:36864\n"
    "buffer_load_dwordx4 v18, s[36:39], s67 offen nt lds\n"
    "v_mfma_f32_16x16x32_bf16 v[176:179], a[152:155], v[88:91], v[176:179]\n"
    "ds_read_b64_tr_b16 a[82:83], v16 offset:37120\n"
    "ds_read_b64_tr_b16 a[84:85], v16 offset:36872\n"
    "s_lshl_b32 s18, s77, 2\n"
    "v_mfma_f32_16x16x32_bf16 v[184:187], a[152:155], v[92:95], v[184:187]\n"
    "ds_read_b64_tr_b16 a[86:87], v16 offset:37128\n"
    "s_add_u32 s18, s18, 0x2000\n"
    "v_lshlrev_b32 v1, 7, v1\n"
    "v_mfma_f32_16x16x32_bf16 v[188:191], a[156:159], v[92:95], v[188:191]\n"
    "ds_read_b64_tr_b16 a[88:89], v16 offset:37376\n"
    "ds_read_b64_tr_b16 a[90:91], v16 offset:37632\n"
    "v_add_u32 v1, s18, v1\n"
    "v_mfma_f32_16x16x32_bf16 v[180:183], a[156:159], v[88:91], v[180:183]\n"
    "ds_read_b64_tr_b16 a[92:93], v16 offset:37384\n"
    "v_lshrrev_b32 v2, 2, v0\n"
    "v_mfma_f32_16x16x32_bf16 v[192:195], a[160:163], v[88:91], v[192:195]\n"
    "ds_read_b64_tr_b16 a[94:95], v16 offset:37640\n"
    "buffer_load_dwordx4 v19, s[36:39], s67 offen offset:1024 nt lds\n"
    "v_mfma_f32_16x16x32_bf16 v[200:203], a[160:163], v[92:95], v[200:203]\n"
    "ds_read_b64_tr_b16 a[96:97], v16 offset:40960\n"
    "ds_read_b64_tr_b16 a[98:99], v16 offset:41216\n"
    "v_sub_u32 v2, s69, v2\n"
    "v_mfma_f32_16x16x32_bf16 v[204:207], a[164:167], v[92:95], v[204:207]\n"
    "ds_read_b64_tr_b16 a[100:101], v16 offset:40968\n"
    "v_mfma_f32_16x16x32_bf16 v[196:199], a[164:167], v[88:91], v[196:199]\n"
    "ds_read_b64_tr_b16 a[102:103], v16 offset:41224\n"
    "v_mfma_f32_16x16x32_bf16 v[208:211], a[168:171], v[88:91], v[208:211]\n"
    "ds_read_b64_tr_b16 a[104:105], v16 offset:41472\n"
    "v_mfma_f32_16x16x32_bf16 v[216:219], a[168:171], v[92:95], v[216:219]\n"
    "ds_read_b64_tr_b16 a[106:107], v16 offset:41728\n"
    "buffer_load_dwordx4 v20, s[36:39], s67 offen offset:2048 nt lds\n"
    "v_mfma_f32_16x16x32_bf16 v[220:223], a[172:175], v[92:95], v[220:223]\n"
    "ds_read_b64_tr_b16 a[108:109], v16 offset:41480\n"
    "v_mfma_f32_16x16x32_bf16 v[212:215], a[172:175], v[88:91], v[212:215]\n"
    "ds_read_b64_tr_b16 a[110:111], v16 offset:41736\n"
    "v_mfma_f32_16x16x32_bf16 v[224:227], a[176:179], v[88:91], v[224:227]\n"
    "ds_read_b64_tr_b16 a[112:113], v16 offset:45056\n"
    "v_mfma_f32_16x16x32_bf16 v[232:235], a[176:179], v[92:95], v[232:235]\n"
    "ds_read_b64_tr_b16 a[114:115], v16 offset:45312\n"
    "s_waitcnt lgkmcnt(15)\n"
    "v_mfma_f32_16x16x32_bf16 v[236:239], a[180:183], v[92:95], v[236:239]\n"
    "ds_read_b64_tr_b16 a[116:117], v16 offset:45064\n"
    "buffer_load_dwordx4 v21, s[36:39], s67 offen offset:3072 nt lds\n"
    "v_mfma_f32_16x16x32_bf16 v[228:231], a[180:183], v[88:91], v[228:231]\n"
    "ds_read_b64_tr_b16 a[118:119], v16 offset:45320\n"
    "v_mfma_f32_16x16x32_bf16 v[240:243], a[184:187], v[88:91], v[240:243]\n"
    "ds_read_b64_tr_b16 a[120:121], v16 offset:45568\n"
    "v_mfma_f32_16x16x32_bf16 v[248:251], a[184:187], v[92:95], v[248:251]\n"
    "ds_read_b64_tr_b16 a[122:123], v16 offset:45824\n"
    "v_mfma_f32_16x16x32_bf16 v[252:255], a[188:191], v[92:95], v[252:255]\n"
    "ds_read_b64_tr_b16 a[124:125], v16 offset:45576\n"
    "v_mfma_f32_16x16x32_bf16 v[244:247], a[188:191], v[88:91], v[244:247]\n"
    "ds_read_b64_tr_b16 a[126:127], v16 offset:45832\n"
    "s_waitcnt vmcnt(17) lgkmcnt(6)\n"
    "s_barrier\n"
    "v_mfma_f32_16x16x32_bf16 v[128:131], a[64:67], v[64:67], v[128:131]\n"
    "ds_read_b64_tr_b16 a[128:129], v16 offset:49152\n"
    "s_add_u32 m0, s62, 0x14000\n"
    "v_mfma_f32_16x16x32_bf16 v[136:139], a[64:67], v[68:71], v[136:139]\n"
    "ds_read_b64_tr_b16 a[130:131], v16 offset:49408\n"
    "s_mov_b32 s67, 0x8000\n"
    "v_mfma_f32_16x16x32_bf16 v[140:143], a[68:71], v[68:71], v[140:143]\n"
    "buffer_load_dwordx4 v22, s[40:43], s67 offen nt lds\n"
    "v_mfma_f32_16x16x32_bf16 v[132:135], a[68:71], v[64:67], v[132:135]\n"
    "ds_read_b64_tr_b16 a[132:133], v16 offset:49160\n"
    "v_and_b32 v2, 3, v2\n"
    "v_mfma_f32_16x16x32_bf16 v[144:147], a[72:75], v[64:67], v[144:147]\n"
    "ds_read_b64_tr_b16 a[134:135], v16 offset:49416\n"
    "v_lshrrev_b32 v3, 4, v0\n"
    "s_add_u32 s15, s14, 0x80\n"
    "v_mfma_f32_16x16x32_bf16 v[152:155], a[72:75], v[68:71], v[152:155]\n"
    "s_cmp_eq_u32 s15, 0x380\n"
    "s_cselect_b32 s15, 0, s15\n"
    "v_mfma_f32_16x16x32_bf16 v[156:159], a[76:79], v[68:71], v[156:159]\n"
    "ds_read_b64_tr_b16 a[136:137], v16 offset:49664\n"
    "v_lshl_add_u32 v2, v3, 2, v2\n"
    "v_mfma_f32_16x16x32_bf16 v[148:151], a[76:79], v[64:67], v[148:151]\n"
    "ds_read_b64_tr_b16 a[138:139], v16 offset:49920\n"
    "v_mov_b32 v3, 0x7fffff00\n"
    "v_mfma_f32_16x16x32_bf16 v[160:163], a[80:83], v[64:67], v[160:163]\n"
    "ds_read_b64_tr_b16 a[140:141], v16 offset:49672\n"
    "buffer_load_dwordx4 v23, s[40:43], s67 offen offset:1024 nt lds\n"
    "v_mfma_f32_16x16x32_bf16 v[168:171], a[80:83], v[68:71], v[168:171]\n"
    "ds_read_b64_tr_b16 a[142:143], v16 offset:49928\n"
    "s_add_u32 s16, s15, 0x80\n"
    "v_mfma_f32_16x16x32_bf16 v[172:175], a[84:87], v[68:71], v[172:175]\n"
    "s_cmp_eq_u32 s16, 0x380\n"
    "s_cselect_b32 s16, 0, s16\n"
    "v_mfma_f32_16x16x32_bf16 v[164:167], a[84:87], v[64:67], v[164:167]\n"
    "ds_read_b64_tr_b16 a[144:145], v16 offset:53248\n"
    "ds_read_b64_tr_b16 a[146:147], v16 offset:53504\n"
    "s_add_u32 s17, s16, 0x80\n"
    "v_mfma_f32_16x16x32_bf16 v[176:179], a[88:91], v[64:67], v[176:179]\n"
    "s_cmp_eq_u32 s17, 0x380\n"
    "s_cselect_b32 s17, 0, s17\n"
    "v_mfma_f32_16x16x32_bf16 v[184:187], a[88:91], v[68:71], v[184:187]\n"
    "v_cmp_eq_u32 vcc, 0, v2\n"
    "s_nop 1\n"
    "v_cndmask_b32 v30, v3, v1, vcc\n"
    "v_mfma_f32_16x16x32_bf16 v[188:191], a[92:95], v[68:71], v[188:191]\n"
    "ds_read_b64_tr_b16 a[148:149], v16 offset:53256\n"
    "ds_read_b64_tr_b16 a[150:151], v16 offset:53512\n"
    "v_mfma_f32_16x16x32_bf16 v[180:183], a[92:95], v[64:67], v[180:183]\n"
    "v_cmp_eq_u32 vcc, 1, v2\n"
    "s_nop 1\n"
    "v_cndmask_b32 v31, v3, v1, vcc\n"
    "v_mfma_f32_16x16x32_bf16 v[192:195], a[96:99], v[64:67], v[192:195]\n"
    "buffer_load_dwordx4 v24, s[40:43], s67 offen offset:2048 nt lds\n"
    "v_mfma_f32_16x16x32_bf16 v[200:203], a[96:99], v[68:71], v[200:203]\n"
    "ds_read_b64_tr_b16 a[152:153], v16 offset:53760\n"
    "ds_read_b64_tr_b16 a[154:155], v16 offset:54016\n"
    "v_mfma_f32_16x16x32_bf16 v[204:207], a[100:103], v[68:71], v[204:207]\n"
    "ds_read_b64_tr_b16 a[156:157], v16 offset:53768\n"
    "ds_read_b64_tr_b16 a[158:159], v16 offset:54024\n"
    "v_mfma_f32_16x16x32_bf16 v[196:199], a[100:103], v[64:67], v[196:199]\n"
    "ds_read_b64_tr_b16 a[160:161], v16 offset:57344\n"
    "ds_read_b64_tr_b16 a[162:163], v16 offset:57600\n"
    "v_mfma_f32_16x16x32_bf16 v[208:211], a[104:107], v[64:67], v[208:211]\n"
    "ds_read_b64_tr_b16 a[164:165], v16 offset:57352\n"
    "ds_read_b64_tr_b16 a[166:167], v16 offset:57608\n"
    "v_mfma_f32_16x16x32_bf16 v[216:219], a[104:107], v[68:71], v[216:219]\n"
    "v_cmp_eq_u32 vcc, 2, v2\n"
    "s_nop 1\n"
    "v_cndmask_b32 v32, v3, v1, vcc\n"
    "v_mfma_f32_16x16x32_bf16 v[220:223], a[108:111], v[68:71], v[220:223]\n"
    "ds_read_b64_tr_b16 a[168:169], v16 offset:57856\n"
    "ds_read_b64_tr_b16 a[170:171], v16 offset:58112\n"
    "v_mfma_f32_16x16x32_bf16 v[212:215], a[108:111], v[64:67], v[212:215]\n"
    "ds_read_b64_tr_b16 a[172:173], v16 offset:57864\n"
    "ds_read_b64_tr_b16 a[174:175], v16 offset:58120\n"
    "v_mfma_f32_16x16x32_bf16 v[224:227], a[112:115], v[64:67], v[224:227]\n"
    "ds_read_b64_tr_b16 a[176:177], v16 offset:61440\n"
    "buffer_load_dwordx4 v25, s[40:43], s67 offen offset:3072 nt lds\n"
    "v_mfma_f32_16x16x32_bf16 v[232:235], a[112:115], v[68:71], v[232:235]\n"
    "ds_read_b64_tr_b16 a[178:179], v16 offset:61696\n"
    "ds_read_b64_tr_b16 a[180:181], v16 offset:61448\n"
    "s_waitcnt lgkmcnt(15)\n"
    "v_mfma_f32_16x16x32_bf16 v[236:239], a[116:119], v[68:71], v[236:239]\n"
    "ds_read_b64_tr_b16 a[182:183], v16 offset:61704\n"
    "ds_read_b64_tr_b16 a[184:185], v16 offset:61952\n"
    "v_mfma_f32_16x16x32_bf16 v[228:231], a[116:119], v[64:67], v[228:231]\n"
    "ds_read_b64_tr_b16 a[186:187], v16 offset:62208\n"
    "ds_read_b64_tr_b16 a[188:189], v16 offset:61960\n"
    "v_mfma_f32_16x16x32_bf16 v[240:243], a[120:123], v[64:67], v[240:243]\n"
    "v_cmp_eq_u32 vcc, 3, v2\n"
    "s_nop 1\n"
    "v_cndmask_b32 v33, v3, v1, vcc\n"
    "v_mfma_f32_16x16x32_bf16 v[248:251], a[120:123], v[68:71], v[248:251]\n"
    "ds_read_b64_tr_b16 a[190:191], v16 offset:62216\n"
    "s_lshr_b32 s72, s77, 2\n"
    "v_mfma_f32_16x16x32_bf16 v[252:255], a[124:127], v[68:71], v[252:255]\n"
    "s_sub_u32 s72, s69, s72\n"
    "s_and_b32 s72, s72, 3\n"
    "v_mfma_f32_16x16x32_bf16 v[244:247], a[124:127], v[64:67], v[244:247]\n"
    "s_waitcnt vmcnt(17) lgkmcnt(6)\n"
    "s_barrier\n"
    "v_mfma_f32_16x16x32_bf16 v[128:131], a[128:131], v[72:75], v[128:131]\n"
    "ds_read_b128 a[64:67], v4 offset:0\n"
    "s_add_u32 m0, s62, 0xc000\n"
    "v_mfma_f32_16x16x32_bf16 v[136:139], a[128:131], v[76:79], v[136:139]\n"
    "ds_read_b128 a[68:71], v4 offset:2048\n"
    "s_mov_b32 s67, 0xc000\n"
    "v_mfma_f32_16x16x32_bf16 v[140:143], a[132:135], v[76:79], v[140:143]\n"
    "buffer_load_dwordx4 v18, s[36:39], s67 offen nt lds\n"
    "v_mfma_f32_16x16x32_bf16 v[132:135], a[132:135], v[72:75], v[132:135]\n"
    "ds_read_b128 a[72:75], v5 offset:0\n"
    "v_mfma_f32_16x16x32_bf16 v[144:147], a[136:139], v[72:75], v[144:147]\n"
    "ds_read_b128 a[76:79], v5 offset:2048\n"
    "v_mfma_f32_16x16x32_bf16 v[152:155], a[136:139], v[76:79], v[152:155]\n"
    "ds_read_b128 a[80:83], v4 offset:4096\n"
    "v_mfma_f32_16x16x32_bf16 v[156:159], a[140:143], v[76:79], v[156:159]\n"
    "ds_read_b128 a[84:87], v4 offset:6144\n"
    "v_mul_f32 v128, s65, v128\n"
    "v_mul_f32 v129, s65, v129\n"
    "v_mul_f32 v130, s65, v130\n"
    "v_mul_f32 v131, s65, v131\n"
    "v_mul_f32 v132, s65, v132\n"
    "v_mul_f32 v133, s65, v133\n"
    "v_mul_f32 v134, s65, v134\n"
    "v_mfma_f32_16x16x32_bf16 v[148:151], a[140:143], v[72:75], v[148:151]\n"
    "ds_read_b128 a[88:91], v5 offset:4096\n"
    "v_cvt_pk_bf16_f32 v48, v128, v129\n"
    "v_mul_f32 v135, s65, v135\n"
    "v_cvt_pk_bf16_f32 v49, v130, v131\n"
    "v_mfma_f32_16x16x32_bf16 v[160:163], a[144:147], v[72:75], v[160:163]\n"
    "ds_read_b128 a[92:95], v5 offset:6144\n"
    "buffer_load_dwordx4 v19, s[36:39], s67 offen offset:1024 nt lds\n"
    "v_cvt_pk_bf16_f32 v50, v132, v133\n"
    "v_accvgpr_write_b32 a0, v48\n"
    "v_mfma_f32_16x16x32_bf16 v[168:171], a[144:147], v[76:79], v[168:171]\n"
    "v_cvt_pk_bf16_f32 v51, v134, v135\n"
    "v_accvgpr_write_b32 a1, v49\n"
    "v_mul_f32 v136, s65, v136\n"
    "v_accvgpr_write_b32 a2, v50\n"
    "v_mfma_f32_16x16x32_bf16 v[172:175], a[148:151], v[76:79], v[172:175]\n"
    "ds_read_b128 a[96:99], v4 offset:8192\n"
    "v_accvgpr_write_b32 a3, v51\n"
    "v_mul_f32 v137, s65, v137\n"
    "v_mul_f32 v138, s65, v138\n"
    "v_mul_f32 v139, s65, v139\n"
    "v_mul_f32 v140, s65, v140\n"
    "v_mul_f32 v141, s65, v141\n"
    "v_mfma_f32_16x16x32_bf16 v[164:167], a[148:151], v[72:75], v[164:167]\n"
    "ds_read_b128 a[100:103], v4 offset:10240\n"
    "v_cvt_pk_bf16_f32 v52, v136, v137\n"
    "v_mul_f32 v142, s65, v142\n"
    "v_cvt_pk_bf16_f32 v53, v138, v139\n"
    "v_mul_f32 v143, s65, v143\n"
    "v_cvt_pk_bf16_f32 v54, v140, v141\n"
    "v_mul_f32 v144, s65, v144\n"
    "v_mul_f32 v145, s65, v145\n"
    "v_mul_f32 v146, s65, v146\n"
    "v_mfma_f32_16x16x32_bf16 v[176:179], a[152:155], v[72:75], v[176:179]\n"
    "ds_read_b128 a[104:107], v5 offset:8192\n"
    "v_cvt_pk_bf16_f32 v55, v142, v143\n"
    "v_accvgpr_write_b32 a4, v52\n"
    "v_mul_f32 v147, s65, v147\n"
    "v_cvt_pk_bf16_f32 v48, v144, v145\n"
    "v_mfma_f32_16x16x32_bf16 v[184:187], a[152:155], v[76:79], v[184:187]\n"
    "ds_read_b128 a[108:111], v5 offset:10240\n"
    "v_cvt_pk_bf16_f32 v49, v146, v147\n"
    "v_accvgpr_write_b32 a5, v53\n"
    "v_mfma_f32_16x16x32_bf16 v[188:191], a[156:159], v[76:79], v[188:191]\n"
    "v_accvgpr_write_b32 a6, v54\n"
    "v_mul_f32 v148, s65, v148\n"
    "v_accvgpr_write_b32 a7, v55\n"
    "v_mul_f32 v149, s65, v149\n"
    "v_accvgpr_write_b32 a8, v48\n"
    "v_mul_f32 v150, s65, v150\n"
    "v_accvgpr_write_b32 a9, v49\n"
    "v_mul_f32 v151, s65, v151\n"
    "v_mfma_f32_16x16x32_bf16 v[180:183], a[156:159], v[72:75], v[180:183]\n"
    "ds_read_b128 a[112:115], v4 offset:12288\n"
    "v_cvt_pk_bf16_f32 v50, v148, v149\n"
    "v_mul_f32 v152, s65, v152\n"
    "v_cvt_pk_bf16_f32 v51, v150, v151\n"
    "v_mfma_f32_16x16x32_bf16 v[192:195], a[160:163], v[72:75], v[192:195]\n"
    "ds_read_b128 a[116:119], v4 offset:14336\n"
    "buffer_load_dwordx4 v20, s[36:39], s67 offen offset:2048 nt lds\n"
    "v_accvgpr_write_b32 a10, v50\n"
    "v_mul_f32 v153, s65, v153\n"
    "v_mfma_f32_16x16x32_bf16 v[200:203], a[160:163], v[76:79], v[200:203]\n"
    "ds_read_b128 a[120:123], v5 offset:12288\n"
    "v_cvt_pk_bf16_f32 v52, v152, v153\n"
    "v_accvgpr_write_b32 a11, v51\n"
    "v_mul_f32 v154, s65, v154\n"
    "v_mul_f32 v155, s65, v155\n"
    "v_mul_f32 v156, s65, v156\n"
    "v_mfma_f32_16x16x32_bf16 v[204:207], a[164:167], v[76:79], v[204:207]\n"
    "ds_read_b128 a[124:127], v5 offset:14336\n"
    "v_cvt_pk_bf16_f32 v53, v154, v155\n"
    "v_accvgpr_write_b32 a12, v52\n"
    "v_mul_f32 v157, s65, v157\n"
    "v_mul_f32 v158, s65, v158\n"
    "v_mfma_f32_16x16x32_bf16 v[196:199], a[164:167], v[72:75], v[196:199]\n"
    "v_cvt_pk_bf16_f32 v54, v156, v157\n"
    "v_accvgpr_write_b32 a13, v53\n"
    "v_mul_f32 v159, s65, v159\n"
    "v_mul_f32 v160, s65, v160\n"
    "v_mul_f32 v161, s65, v161\n"
    "v_mul_f32 v162, s65, v162\n"
    "v_mul_f32 v163, s65, v163\n"
    "v_mfma_f32_16x16x32_bf16 v[208:211], a[168:171], v[72:75], v[208:211]\n"
    "v_cvt_pk_bf16_f32 v55, v158, v159\n"
    "v_accvgpr_write_b32 a14, v54\n"
    "v_mul_f32 v164, s65, v164\n"
    "v_cvt_pk_bf16_f32 v48, v160, v161\n"
    "v_mul_f32 v165, s65, v165\n"
    "v_mfma_f32_16x16x32_bf16 v[216:219], a[168:171], v[76:79], v[216:219]\n"
    "v_cvt_pk_bf16_f32 v49, v162, v163\n"
    "v_accvgpr_write_b32 a15, v55\n"
    "v_mul_f32 v166, s65, v166\n"
    "v_cvt_pk_bf16_f32 v50, v164, v165\n"
    "v_accvgpr_write_b32 a16, v48\n"
    "v_mul_f32 v167, s65, v167\n"
    "v_mul_f32 v168, s65, v168\n"
    "v_mul_f32 v169, s65, v169\n"
    "v_mul_f32 v170, s65, v170\n"
    "v_mfma_f32_16x16x32_bf16 v[220:223], a[172:175], v[76:79], v[220:223]\n"
    "v_cvt_pk_bf16_f32 v51, v166, v167\n"
    "v_accvgpr_write_b32 a17, v49\n"
    "v_mul_f32 v171, s65, v171\n"
    "v_cvt_pk_bf16_f32 v52, v168, v169\n"
    "v_accvgpr_write_b32 a18, v50\n"
    "v_mfma_f32_16x16x32_bf16 v[212:215], a[172:175], v[72:75], v[212:215]\n"
    "v_cvt_pk_bf16_f32 v53, v170, v171\n"
    "v_accvgpr_write_b32 a19, v51\n"
    "v_mul_f32 v172, s65, v172\n"
    "v_mfma_f32_16x16x32_bf16 v[224:227], a[176:179], v[72:75], v[224:227]\n"
    "buffer_load_dwordx4 v21, s[36:39], s67 offen offset:3072 nt lds\n"
    "v_accvgpr_write_b32 a20, v52\n"
    "v_mul_f32 v173, s65, v173\n"
    "v_mfma_f32_16x16x32_bf16 v[232:235], a[176:179], v[76:79], v[232:235]\n"
    "v_cvt_pk_bf16_f32 v54, v172, v173\n"
    "v_accvgpr_write_b32 a21, v53\n"
    "v_mul_f32 v174, s65, v174\n"
    "v_mul_f32 v175, s65, v175\n"
    "v_mul_f32 v176, s65, v176\n"
    "s_waitcnt lgkmcnt(15)\n"
    "v_mfma_f32_16x16x32_bf16 v[236:239], a[180:183], v[76:79], v[236:239]\n"
    "v_cvt_pk_bf16_f32 v55, v174, v175\n"
    "v_accvgpr_write_b32 a22, v54\n"
    "v_mul_f32 v177, s65, v177\n"
    "v_mul_f32 v178, s65, v178\n"
    "v_mfma_f32_16x16x32_bf16 v[228:231], a[180:183], v[72:75], v[228:231]\n"
    "v_cvt_pk_bf16_f32 v48, v176, v177\n"
    "v_accvgpr_write_b32 a23, v55\n"
    "v_mul_f32 v179, s65, v179\n"
    "v_mul_f32 v180, s65, v180\n"
    "v_mul_f32 v181, s65, v181\n"
    "v_mul_f32 v182, s65, v182\n"
    "v_mfma_f32_16x16x32_bf16 v[240:243], a[184:187], v[72:75], v[240:243]\n"
    "v_cvt_pk_bf16_f32 v49, v178, v179\n"
    "v_accvgpr_write_b32 a24, v48\n"
    "v_mul_f32 v183, s65, v183\n"
    "v_cvt_pk_bf16_f32 v50, v180, v181\n"
    "v_mul_f32 v184, s65, v184\n"
    "v_mul_f32 v185, s65, v185\n"
    "v_mfma_f32_16x16x32_bf16 v[248:251], a[184:187], v[76:79], v[248:251]\n"
    "v_mul_f32 v186, s65, v186\n"
    "v_accvgpr_write_b32 a25, v49\n"
    "v_cvt_pk_bf16_f32 v51, v182, v183\n"
    "v_mul_f32 v187, s65, v187\n"
    "v_accvgpr_write_b32 a26, v50\n"
    "v_cvt_pk_bf16_f32 v52, v184, v185\n"
    "v_mul_f32 v188, s65, v188\n"
    "v_mul_f32 v189, s65, v189\n"
    "v_mul_f32 v190, s65, v190\n"
    "v_mfma_f32_16x16x32_bf16 v[252:255], a[188:191], v[76:79], v[252:255]\n"
    "v_mul_f32 v191, s65, v191\n"
    "v_accvgpr_write_b32 a27, v51\n"
    "v_cvt_pk_bf16_f32 v53, v186, v187\n"
    "v_accvgpr_write_b32 a28, v52\n"
    "v_cvt_pk_bf16_f32 v54, v188, v189\n"
    "v_mfma_f32_16x16x32_bf16 v[244:247], a[188:191], v[72:75], v[244:247]\n"
    "v_cvt_pk_bf16_f32 v55, v190, v191\n"
    "v_accvgpr_write_b32 a29, v53\n"
    "v_accvgpr_write_b32 a30, v54\n"
    "v_accvgpr_write_b32 a31, v55\n"
    "L_qe:\n"
    "s_waitcnt vmcnt(17) lgkmcnt(0)\n"
    "s_barrier\n"
    "v_mfma_f32_16x16x32_bf16 v[64:67], a[64:67], a[0:3], v[120:123]\n"
    "ds_read_b128 a[128:131], v4 offset:16384\n"
    "s_add_u32 m0, s62, 0x18000\n"
    "v_mul_f32 v192, s65, v192\n"
    "v_mul_f32 v193, s65, v193\n"
    "v_mul_f32 v194, s65, v194\n"
    "v_mul_f32 v195, s65, v195\n"
    "v_mul_f32 v196, s65, v196\n"
    "v_mul_f32 v197, s65, v197\n"
    "v_mul_f32 v198, s65, v198\n"
    "v_mul_f32 v199, s65, v199\n"
    "v_mfma_f32_16x16x32_bf16 v[68:71], a[68:71], a[0:3], v[120:123]\n"
    "ds_read_b128 a[132:135], v4 offset:18432\n"
    "s_mov_b32 s67, 0xc000\n"
    "v_cvt_pk_bf16_f32 v48, v192, v193\n"
    "v_mul_f32 v200, s65, v200\n"
    "v_cvt_pk_bf16_f32 v49, v194, v195\n"
    "v_mul_f32 v201, s65, v201\n"
    "v_cvt_pk_bf16_f32 v50, v196, v197\n"
    "v_mul_f32 v202, s65, v202\n"
    "v_mfma_f32_16x16x32_bf16 v[76:79], a[68:71], a[4:7], v[124:127]\n"
    "ds_read_b128 a[136:139], v5 offset:16384\n"
    "buffer_load_dwordx4 v22, s[40:43], s67 offen nt lds\n"
    "v_cvt_pk_bf16_f32 v51, v198, v199\n"
    "v_accvgpr_write_b32 a32, v48\n"
    "v_mul_f32 v203, s65, v203\n"
    "v_mfma_f32_16x16x32_bf16 v[72:75], a[64:67], a[4:7], v[124:127]\n"
    "ds_read_b128 a[140:143], v5 offset:18432\n"
    "v_cvt_pk_bf16_f32 v52, v200, v201\n"
    "v_accvgpr_write_b32 a33, v49\n"
    "v_mul_f32 v204, s65, v204\n"
    "v_cvt_pk_bf16_f32 v53, v202, v203\n"
    "v_mfma_f32_16x16x32_bf16 v[64:67], a[72:75], a[8:11], v[64:67]\n"
    "v_accvgpr_write_b32 a34, v50\n"
    "v_mul_f32 v205, s65, v205\n"
    "v_accvgpr_write_b32 a35, v51\n"
    "v_mul_f32 v206, s65, v206\n"
    "v_accvgpr_write_b32 a36, v52\n"
    "v_mul_f32 v207, s65, v207\n"
    "v_mfma_f32_16x16x32_bf16 v[68:71], a[76:79], a[8:11], v[68:71]\n"
    "ds_read_b128 a[144:147], v4 offset:20480\n"
    "v_cvt_pk_bf16_f32 v54, v204, v205\n"
    "v_accvgpr_write_b32 a37, v53\n"
    "v_mfma_f32_16x16x32_bf16 v[76:79], a[76:79], a[12:15], v[76:79]\n"
    "ds_read_b128 a[148:151], v4 offset:22528\n"
    "buffer_load_dwordx4 v23, s[40:43], s67 offen offset:1024 nt lds\n"
    "v_cvt_pk_bf16_f32 v55, v206, v207\n"
    "v_accvgpr_write_b32 a38, v54\n"
    "v_mul_f32 v208, s65, v208\n"
    "v_mul_f32 v209, s65, v209\n"
    "v_mfma_f32_16x16x32_bf16 v[72:75], a[72:75], a[12:15], v[72:75]\n"
    "ds_read_b128 a[152:155], v5 offset:20480\n"
    "v_cvt_pk_bf16_f32 v48, v208, v209\n"
    "v_accvgpr_write_b32 a39, v55\n"
    "v_mul_f32 v210, s65, v210\n"
    "v_mul_f32 v211, s65, v211\n"
    "v_mul_f32 v212, s65, v212\n"
    "v_mul_f32 v213, s65, v213\n"
    "v_mul_f32 v214, s65, v214\n"
    "v_mfma_f32_16x16x32_bf16 v[64:67], a[80:83], a[16:19], v[64:67]\n"
    "ds_read_b128 a[156:159], v5 offset:22528\n"
    "v_cvt_pk_bf16_f32 v49, v210, v211\n"
    "v_accvgpr_write_b32 a40, v48\n"
    "v_mul_f32 v215, s65, v215\n"
    "v_cvt_pk_bf16_f32 v50, v212, v213\n"
    "v_mul_f32 v216, s65, v216\n"
    "v_mul_f32 v217, s65, v217\n"
    "v_mul_f32 v218, s65, v218\n"
    "v_mul_f32 v219, s65, v219\n"
    "v_mul_f32 v220, s65, v220\n"
    "v_mfma_f32_16x16x32_bf16 v[68:71], a[84:87], a[16:19], v[68:71]\n"
    "v_cvt_pk_bf16_f32 v51, v214, v215\n"
    "v_accvgpr_write_b32 a41, v49\n"
    "v_mul_f32 v221, s65, v221\n"
    "v_mfma_f32_16x16x32_bf16 v[76:79], a[84:87], a[20:23], v[76:79]\n"
    "ds_read_b128 a[160:163], v4 offset:24576\n"
    "buffer_load_dwordx4 v24, s[40:43], s67 offen offset:2048 nt lds\n"
    "v_mfma_f32_16x16x32_bf16 v[72:75], a[80:83], a[20:23], v[72:75]\n"
    "ds_read_b128 a[164:167], v4 offset:26624\n"
    "v_cvt_pk_bf16_f32 v52, v216, v217\n"
    "v_accvgpr_write_b32 a42, v50\n"
    "v_mul_f32 v222, s65, v222\n"
    "v_cvt_pk_bf16_f32 v53, v218, v219\n"
    "v_accvgpr_write_b32 a43, v51\n"
    "v_mul_f32 v223, s65, v223\n"
    "v_cvt_pk_bf16_f32 v54, v220, v221\n"
    "v_mul_f32 v224, s65, v224\n"
    "v_mfma_f32_16x16x32_bf16 v[64:67], a[88:91], a[24:27], v[64:67]\n"
    "ds_read_b128 a[168:171], v5 offset:24576\n"
    "v_cvt_pk_bf16_f32 v55, v222, v223\n"
    "v_accvgpr_write_b32 a44, v52\n"
    "v_mul_f32 v225, s65, v225\n"
    "v_accvgpr_write_b32 a45, v53\n"
    "v_mul_f32 v226, s65, v226\n"
    "v_mfma_f32_16x16x32_bf16 v[68:71], a[92:95], a[24:27], v[68:71]\n"
    "ds_read_b128 a[172:175], v5 offset:26624\n"
    "v_cvt_pk_bf16_f32 v48, v224, v225\n"
    "v_accvgpr_write_b32 a46, v54\n"
    "v_mul_f32 v227, s65, v227\n"
    "v_accvgpr_write_b32 a47, v55\n"
    "v_mul_f32 v228, s65, v228\n"
    "v_mul_f32 v229, s65, v229\n"
    "v_mfma_f32_16x16x32_bf16 v[76:79], a[92:95], a[28:31], v[76:79]\n"
    "buffer_load_dwordx4 v25, s[40:43], s67 offen offset:3072 nt lds\n"
    "v_cvt_pk_bf16_f32 v49, v226, v227\n"
    "v_accvgpr_write_b32 a48, v48\n"
    "v_mul_f32 v230, s65, v230\n"
    "v_cvt_pk_bf16_f32 v50, v228, v229\n"
    "v_mfma_f32_16x16x32_bf16 v[72:75], a[88:91], a[28:31], v[72:75]\n"
    "ds_read_b128 a[176:179], v4 offset:28672\n"
    "s_add_u32 m0, s62, 0\n"
    "v_accvgpr_write_b32 a49, v49\n"
    "v_mul_f32 v231, s65, v231\n"
    "v_accvgpr_write_b32 a50, v50\n"
    "v_mul_f32 v232, s65, v232\n"
    "v_mul_f32 v233, s65, v233\n"
    "v_mfma_f32_16x16x32_bf16 v[64:67], a[96:99], a[32:35], v[64:67]\n"
    "ds_read_b128 a[180:183], v4 offset:30720\n"
    "s_mov_b32 s67, 0x10000\n"
    "v_cvt_pk_bf16_f32 v51, v230, v231\n"
    "v_mul_f32 v234, s65, v234\n"
    "v_mfma_f32_16x16x32_bf16 v[68:71], a[100:103], a[32:35], v[68:71]\n"
    "ds_read_b128 a[184:187], v5 offset:28672\n"
    "v_cvt_pk_bf16_f32 v52, v232, v233\n"
    "v_accvgpr_write_b32 a51, v51\n"
    "v_mul_f32 v235, s65, v235\n"
    "v_mfma_f32_16x16x32_bf16 v[76:79], a[100:103], a[36:39], v[76:79]\n"
    "ds_read_b128 a[188:191], v5 offset:30720\n"
    "buffer_load_dwordx4 v18, s[36:39], s67 offen nt lds\n"
    "v_cvt_pk_bf16_f32 v53, v234, v235\n"
    "v_accvgpr_write_b32 a52, v52\n"
    "v_mfma_f32_16x16x32_bf16 v[72:75], a[96:99], a[36:39], v[72:75]\n"
    "v_accvgpr_write_b32 a53, v53\n"
    "v_mul_f32 v236, s65, v236\n"
    "v_mul_f32 v237, s65, v237\n"
    "v_mul_f32 v238, s65, v238\n"
    "v_mul_f32 v239, s65, v239\n"
    "v_mul_f32 v240, s65, v240\n"
    "v_mul_f32 v241, s65, v241\n"
    "v_mul_f32 v242, s65, v242\n"
    "v_mfma_f32_16x16x32_bf16 v[64:67], a[104:107], a[40:43], v[64:67]\n"
    "v_cvt_pk_bf16_f32 v54, v236, v237\n"
    "v_mul_f32 v243, s65, v243\n"
    "v_cvt_pk_bf16_f32 v55, v238, v239\n"
    "v_mul_f32 v244, s65, v244\n"
    "v_cvt_pk_bf16_f32 v48, v240, v241\n"
    "v_mul_f32 v245, s65, v245\n"
    "v_mul_f32 v246, s65, v246\n"
    "v_mul_f32 v247, s65, v247\n"
    "L_iw2:\n"
    "s_cbranch_execz L_iw6\n"
    "v_mul_f32 v248, s65, v248\n"
    "v_mul_f32 v249, s65, v249\n"
    "v_mul_f32 v250, s65, v250\n"
    "v_mfma_f32_16x16x32_bf16 v[68:71], a[108:111], a[40:43], v[68:71]\n"
    "v_cvt_pk_bf16_f32 v49, v242, v243\n"
    "v_accvgpr_write_b32 a54, v54\n"
    "v_mul_f32 v251, s65, v251\n"
    "v_cvt_pk_bf16_f32 v50, v244, v245\n"
    "v_accvgpr_write_b32 a55, v55\n"
    "v_mul_f32 v252, s65, v252\n"
    "v_mfma_f32_16x16x32_bf16 v[76:79], a[108:111], a[44:47], v[76:79]\n"
    "v_cvt_pk_bf16_f32 v51, v246, v247\n"
    "v_accvgpr_write_b32 a56, v48\n"
    "v_mul_f32 v253, s65, v253\n"
    "v_cvt_pk_bf16_f32 v52, v248, v249\n"
    "v_accvgpr_write_b32 a57, v49\n"
    "v_mfma_f32_16x16x32_bf16 v[72:75], a[104:107], a[44:47], v[72:75]\n"
    "buffer_load_dwordx4 v19, s[36:39], s67 offen offset:1024 nt lds\n"
    "v_cvt_pk_bf16_f32 v53, v250, v251\n"
    "v_accvgpr_write_b32 a58, v50\n"
    "v_mul_f32 v254, s65, v254\n"
    "v_mfma_f32_16x16x32_bf16 v[64:67], a[112:115], a[48:51], v[64:67]\n"
    "v_cvt_pk_bf16_f32 v54, v252, v253\n"
    "v_accvgpr_write_b32 a59, v51\n"
    "v_mul_f32 v255, s65, v255\n"
    "v_mfma_f32_16x16x32_bf16 v[68:71], a[116:119], a[48:51], v[68:71]\n"
    "v_cvt_pk_bf16_f32 v55, v254, v255\n"
    "v_accvgpr_write_b32 a60, v52\n"
    "v_accvgpr_write_b32 a61, v53\n"
    "v_accvgpr_write_b32 a62, v54\n"
    "v_mfma_f32_16x16x32_bf16 v[76:79], a[116:119], a[52:55], v[76:79]\n"
    "v_accvgpr_write_b32 a63, v55\n"
    "v_mfma_f32_16x16x32_bf16 v[72:75], a[112:115], a[52:55], v[72:75]\n"
    "buffer_load_dwordx4 v20, s[36:39], s67 offen offset:2048 nt lds\n"
    "v_mfma_f32_16x16x32_bf16 v[64:67], a[120:123], a[56:59], v[64:67]\n"
    "v_mfma_f32_16x16x32_bf16 v[68:71], a[124:127], a[56:59], v[68:71]\n"
    "v_mfma_f32_16x16x32_bf16 v[76:79], a[124:127], a[60:63], v[76:79]\n"
    "v_mfma_f32_16x16x32_bf16 v[72:75], a[120:123], a[60:63], v[72:75]\n"
    "s_waitcnt vmcnt(19) lgkmcnt(0)\n"
    "s_barrier\n"
    "v_mfma_f32_16x16x32_bf16 v[80:83], a[128:131], a[0:3], v[120:123]\n"
    "ds_read_b64_tr_b16 a[64:65], v10 offset:0\n"
    "ds_read_b64_tr_b16 a[66:67], v10 offset:2048\n"
    "v_mfma_f32_16x16x32_bf16 v[84:87], a[132:135], a[0:3], v[120:123]\n"
    "ds_read_b64_tr_b16 a[68:69], v11 offset:0\n"
    "ds_read_b64_tr_b16 a[70:71], v11 offset:2048\n"
    "buffer_load_dwordx4 v21, s[36:39], s67 offen offset:3072 nt lds\n"
    "v_mfma_f32_16x16x32_bf16 v[92:95], a[132:135], a[4:7], v[124:127]\n"
    "ds_read_b64_tr_b16 a[72:73], v10 offset:512\n"
    "ds_read_b64_tr_b16 a[74:75], v10 offset:2560\n"
    "v_mfma_f32_16x16x32_bf16 v[88:91], a[128:131], a[4:7], v[124:127]\n"
    "ds_read_b64_tr_b16 a[76:77], v11 offset:512\n"
    "v_exp_f32 v48, v64\n"
    "v_mfma_f32_16x16x32_bf16 v[80:83], a[136:139], a[8:11], v[80:83]\n"
    "ds_read_b64_tr_b16 a[78:79], v11 offset:2560\n"
    "ds_read_b64_tr_b16 a[80:81], v10 offset:4096\n"
    "v_exp_f32 v49, v65\n"
    "v_mfma_f32_16x16x32_bf16 v[84:87], a[140:143], a[8:11], v[84:87]\n"
    "v_exp_f32 v50, v66\n"
    "v_add_f32 v42, v48, v49\n"
    "v_cvt_pk_bf16_f32 v96, v48, v49\n"
    "v_mfma_f32_16x16x32_bf16 v[92:95], a[140:143], a[12:15], v[92:95]\n"
    "ds_read_b64_tr_b16 a[82:83], v10 offset:6144\n"
    "v_exp_f32 v51, v67\n"
    "v_add_f32 v42, v42, v50\n"
    "v_mfma_f32_16x16x32_bf16 v[88:91], a[136:139], a[12:15], v[88:91]\n"
    "ds_read_b64_tr_b16 a[84:85], v11 offset:4096\n"
    "v_exp_f32 v52, v68\n"
    "v_mfma_f32_16x16x32_bf16 v[80:83], a[144:147], a[16:19], v[80:83]\n"
    "ds_read_b64_tr_b16 a[86:87], v11 offset:6144\n"
    "v_exp_f32 v53, v69\n"
    "v_cvt_pk_bf16_f32 v97, v50, v51\n"
    "v_mfma_f32_16x16x32_bf16 v[84:87], a[148:151], a[16:19], v[84:87]\n"
    "v_exp_f32 v54, v70\n"
    "v_add_f32 v42, v42, v51\n"
    "v_cvt_pk_bf16_f32 v98, v52, v53\n"
    "v_mfma_f32_16x16x32_bf16 v[92:95], a[148:151], a[20:23], v[92:95]\n"
    "ds_read_b64_tr_b16 a[88:89], v10 offset:4608\n"
    "v_exp_f32 v55, v71\n"
    "v_add_f32 v42, v42, v52\n"
    "v_mfma_f32_16x16x32_bf16 v[88:91], a[144:147], a[20:23], v[88:91]\n"
    "ds_read_b64_tr_b16 a[90:91], v10 offset:6656\n"
    "v_cvt_pk_bf16_f32 v99, v54, v55\n"
    "v_add_f32 v42, v42, v53\n"
    "v_mfma_f32_16x16x32_bf16 v[80:83], a[152:155], a[24:27], v[80:83]\n"
    "ds_read_b64_tr_b16 a[92:93], v11 offset:4608\n"
    "ds_read_b64_tr_b16 a[94:95], v11 offset:6656\n"
    "v_add_f32 v42, v42, v54\n"
    "v_add_f32 v42, v42, v55\n"
    "v_mfma_f32_16x16x32_bf16 v[84:87], a[156:159], a[24:27], v[84:87]\n"
    "ds_read_b64_tr_b16 a[96:97], v10 offset:8192\n"
    "ds_read_b64_tr_b16 a[98:99], v10 offset:10240\n"
    "v_exp_f32 v48, v72\n"
    "v_mfma_f32_16x16x32_bf16 v[92:95], a[156:159], a[28:31], v[92:95]\n"
    "ds_read_b64_tr_b16 a[100:101], v11 offset:8192\n"
    "ds_read_b64_tr_b16 a[102:103], v11 offset:10240\n"
    "v_exp_f32 v49, v73\n"
    "v_mfma_f32_16x16x32_bf16 v[88:91], a[152:155], a[28:31], v[88:91]\n"
    "v_exp_f32 v50, v74\n"
    "v_add_f32 v43, v48, v49\n"
    "v_mfma_f32_16x16x32_bf16 v[80:83], a[160:163], a[32:35], v[80:83]\n"
    "ds_read_b64_tr_b16 a[104:105], v10 offset:8704\n"
    "v_exp_f32 v51, v75\n"
    "v_cvt_pk_bf16_f32 v100, v48, v49\n"
    "v_mfma_f32_16x16x32_bf16 v[84:87], a[164:167], a[32:35], v[84:87]\n"
    "ds_read_b64_tr_b16 a[106:107], v10 offset:10752\n"
    "v_exp_f32 v52, v76\n"
    "v_cvt_pk_bf16_f32 v101, v50, v51\n"
    "v_mfma_f32_16x16x32_bf16 v[92:95], a[164:167], a[36:39], v[92:95]\n"
    "ds_read_b64_tr_b16 a[108:109], v11 offset:8704\n"
    "v_exp_f32 v53, v77\n"
    "v_add_f32 v43, v43, v50\n"
    "v_mfma_f32_16x16x32_bf16 v[88:91], a[160:163], a[36:39], v[88:91]\n"
    "ds_read_b64_tr_b16 a[110:111], v11 offset:10752\n"
    "v_exp_f32 v54, v78\n"
    "v_mfma_f32_16x16x32_bf16 v[80:83], a[168:171], a[40:43], v[80:83]\n"
    "v_exp_f32 v55, v79\n"
    "v_add_f32 v43, v43, v51\n"
    "v_cvt_pk_bf16_f32 v102, v52, v53\n"
    "v_mfma_f32_16x16x32_bf16 v[84:87], a[172:175], a[40:43], v[84:87]\n"
    "ds_read_b64_tr_b16 a[112:113], v10 offset:12288\n"
    "v_add_f32 v43, v43, v52\n"
    "v_cvt_pk_bf16_f32 v103, v54, v55\n"
    "v_add_f32 v43, v43, v53\n"
    "v_mfma_f32_16x16x32_bf16 v[92:95], a[172:175], a[44:47], v[92:95]\n"
    "ds_read_b64_tr_b16 a[114:115], v10 offset:14336\n"
    "ds_read_b64_tr_b16 a[116:117], v11 offset:12288\n"
    "v_add_f32 v43, v43, v54\n"
    "v_add_f32 v43, v43, v55\n"
    "v_mfma_f32_16x16x32_bf16 v[88:91], a[168:171], a[44:47], v[88:91]\n"
    "ds_read_b64_tr_b16 a[118:119], v11 offset:14336\n"
    "v_add_f32 v44, v42, v43\n"
    "v_min_f32 v56, v42, v43\n"
    "v_mfma_f32_16x16x32_bf16 v[80:83], a[176:179], a[48:51], v[80:83]\n"
    "ds_read_b64_tr_b16 a[120:121], v10 offset:12800\n"
    "ds_read_b64_tr_b16 a[122:123], v10 offset:14848\n"
    "v_cmp_nle_f32 s[98:99], s71, v56\n"
    "v_cmp_nge_f32 vcc, s66, v44\n"
    "v_mfma_f32_16x16x32_bf16 v[84:87], a[180:183], a[48:51], v[84:87]\n"
    "ds_read_b64_tr_b16 a[124:125], v11 offset:12800\n"
    "ds_read_b64_tr_b16 a[126:127], v11 offset:14848\n"
    "v_mfma_f32_16x16x32_bf16 v[92:95], a[180:183], a[52:55], v[92:95]\n"
    "v_mfma_f32_16x16x32_bf16 v[88:91], a[176:179], a[52:55], v[88:91]\n"
    "v_mfma_f32_16x16x32_bf16 v[80:83], a[184:187], a[56:59], v[80:83]\n"
    "v_mfma_f32_16x16x32_bf16 v[84:87], a[188:191], a[56:59], v[84:87]\n"
    "v_mfma_f32_16x16x32_bf16 v[92:95], a[188:191], a[60:63], v[92:95]\n"
    "v_mfma_f32_16x16x32_bf16 v[88:91], a[184:187], a[60:63], v[88:91]\n"
    "s_nop 1\n"
    "s_or_b64 vcc, vcc, s[98:99]\n"
    "s_cbranch_scc1 L_first\n"
    "L_firstret:\n"
    "s_waitcnt vmcnt(12) lgkmcnt(0)\n"
    "s_barrier\n"
    "s_mov_b32 s63, 0x10000\n"
    "v_mfma_f32_16x16x32_bf16 v[128:131], a[64:67], v[96:99], 0\n"
    "ds_read_b128 a[128:131], v4 offset:32768\n"
    "buffer_store_dword v62, v28, s[20:23], 0 offen sc1\n"
    "v_mfma_f32_16x16x32_bf16 v[136:139], a[64:67], v[100:103], 0\n"
    "ds_read_b128 a[132:135], v4 offset:34816\n"
    "s_add_u32 m0, s62, 0x1c000\n"
    "v_mfma_f32_16x16x32_bf16 v[140:143], a[68:71], v[100:103], 0\n"
    "buffer_load_dwordx4 v22, s[40:43], s63 offen nt lds\n"
    "v_mfma_f32_16x16x32_bf16 v[132:135], a[68:71], v[96:99], 0\n"
    "v_exp_f32 v48, v80\n"
    "v_mfma_f32_16x16x32_bf16 v[144:147], a[72:75], v[96:99], 0\n"
    "v_exp_f32 v49, v81\n"
    "v_mfma_f32_16x16x32_bf16 v[152:155], a[72:75], v[100:103], 0\n"
    "v_exp_f32 v50, v82\n"
    "v_mfma_f32_16x16x32_bf16 v[156:159], a[76:79], v[100:103], 0\n"
    "v_exp_f32 v51, v83\n"
    "v_mfma_f32_16x16x32_bf16 v[148:151], a[76:79], v[96:99], 0\n"
    "v_exp_f32 v52, v84\n"
    "v_mfma_f32_16x16x32_bf16 v[160:163], a[80:83], v[96:99], 0\n"
    "ds_read_b128 a[136:139], v5 offset:32768\n"
    "buffer_load_dwordx4 v23, s[40:43], s63 offen offset:1024 nt lds\n"
    "v_mfma_f32_16x16x32_bf16 v[168:171], a[80:83], v[100:103], 0\n"
    "v_exp_f32 v53, v85\n"
    "v_mfma_f32_16x16x32_bf16 v[172:175], a[84:87], v[100:103], 0\n"
    "v_exp_f32 v54, v86\n"
    "v_mfma_f32_16x16x32_bf16 v[164:167], a[84:87], v[96:99], 0\n"
    "v_exp_f32 v55, v87\n"
    "v_mfma_f32_16x16x32_bf16 v[176:179], a[88:91], v[96:99], 0\n"
    "ds_read_b128 a[140:143], v5 offset:34816\n"
    "v_add_f32 v40, v40, v42\n"
    "v_mfma_f32_16x16x32_bf16 v[184:187], a[88:91], v[100:103], 0\n"
    "ds_read_b128 a[144:147], v4 offset:36864\n"
    "v_add_f32 v41, v41, v43\n"
    "v_mfma_f32_16x16x32_bf16 v[188:191], a[92:95], v[100:103], 0\n"
    "ds_read_b128 a[148:151], v4 offset:38912\n"
    "v_add_f32 v42, v48, v49\n"
    "v_mfma_f32_16x16x32_bf16 v[180:183], a[92:95], v[96:99], 0\n"
    "ds_read_b128 a[152:155], v5 offset:36864\n"
    "v_add_f32 v42, v42, v50\n"
    "v_mfma_f32_16x16x32_bf16 v[192:195], a[96:99], v[96:99], 0\n"
    "ds_read_b128 a[156:159], v5 offset:38912\n"
    "buffer_load_dwordx4 v24, s[40:43], s63 offen offset:2048 nt lds\n"
    "v_mfma_f32_16x16x32_bf16 v[200:203], a[96:99], v[100:103], 0\n"
    "ds_read_b128 a[160:163], v4 offset:40960\n"
    "v_add_f32 v42, v42, v51\n"
    "v_mfma_f32_16x16x32_bf16 v[204:207], a[100:103], v[100:103], 0\n"
    "ds_read_b128 a[164:167], v4 offset:43008\n"
    "v_add_f32 v42, v42, v52\n"
    "v_mfma_f32_16x16x32_bf16 v[196:199], a[100:103], v[96:99], 0\n"
    "ds_read_b128 a[168:171], v5 offset:40960\n"
    "v_add_f32 v42, v42, v53\n"
    "v_mfma_f32_16x16x32_bf16 v[208:211], a[104:107], v[96:99], 0\n"
    "ds_read_b128 a[172:175], v5 offset:43008\n"
    "v_add_f32 v42, v42, v54\n"
    "v_mfma_f32_16x16x32_bf16 v[216:219], a[104:107], v[100:103], 0\n"
    "ds_read_b128 a[176:179], v4 offset:45056\n"
    "v_add_f32 v42, v42, v55\n"
    "v_mfma_f32_16x16x32_bf16 v[220:223], a[108:111], v[100:103], 0\n"
    "ds_read_b128 a[180:183], v4 offset:47104\n"
    "v_cvt_pk_bf16_f32 v104, v48, v49\n"
    "v_mfma_f32_16x16x32_bf16 v[212:215], a[108:111], v[96:99], 0\n"
    "ds_read_b128 a[184:187], v5 offset:45056\n"
    "v_cvt_pk_bf16_f32 v105, v50, v51\n"
    "v_mfma_f32_16x16x32_bf16 v[224:227], a[112:115], v[96:99], 0\n"
    "ds_read_b128 a[188:191], v5 offset:47104\n"
    "buffer_load_dwordx4 v25, s[40:43], s63 offen offset:3072 nt lds\n"
    "v_mfma_f32_16x16x32_bf16 v[232:235], a[112:115], v[100:103], 0\n"
    "ds_read_b64_tr_b16 a[64:65], v10 offset:16384\n"
    "v_cvt_pk_bf16_f32 v106, v52, v53\n"
    "v_mfma_f32_16x16x32_bf16 v[236:239], a[116:119], v[100:103], 0\n"
    "ds_read_b64_tr_b16 a[66:67], v10 offset:18432\n"
    "v_cvt_pk_bf16_f32 v107, v54, v55\n"
    "v_mfma_f32_16x16x32_bf16 v[228:231], a[116:119], v[96:99], 0\n"
    "ds_read_b64_tr_b16 a[68:69], v11 offset:16384\n"
    "ds_read_b64_tr_b16 a[70:71], v11 offset:18432\n"
    "v_mfma_f32_16x16x32_bf16 v[240:243], a[120:123], v[96:99], 0\n"
    "ds_read_b64_tr_b16 a[72:73], v10 offset:16896\n"
    "v_mfma_f32_16x16x32_bf16 v[248:251], a[120:123], v[100:103], 0\n"
    "ds_read_b64_tr_b16 a[74:75], v10 offset:18944\n"
    "v_mfma_f32_16x16x32_bf16 v[252:255], a[124:127], v[100:103], 0\n"
    "ds_read_b64_tr_b16 a[76:77], v11 offset:16896\n"
    "v_mfma_f32_16x16x32_bf16 v[244:247], a[124:127], v[96:99], 0\n"
    "ds_read_b64_tr_b16 a[78:79], v11 offset:18944\n"
    "s_waitcnt lgkmcnt(8)\n"
    "v_mfma_f32_16x16x32_bf16 v[64:67], a[128:131], a[0:3], v[120:123]\n"
    "ds_read_b64_tr_b16 a[80:81], v10 offset:20480\n"
    "ds_read_b64_tr_b16 a[82:83], v10 offset:22528\n"
    "s_add_u32 m0, s62, 0x4000\n"
    "v_mfma_f32_16x16x32_bf16 v[68:71], a[132:135], a[0:3], v[120:123]\n"
    "ds_read_b64_tr_b16 a[84:85], v11 offset:20480\n"
    "s_add_u32 s63, s63, 0x4000\n"
    "v_mfma_f32_16x16x32_bf16 v[76:79], a[132:135], a[4:7], v[124:127]\n"
    "buffer_load_dwordx4 v18, s[36:39], s63 offen nt lds\n"
    "v_mfma_f32_16x16x32_bf16 v[72:75], a[128:131], a[4:7], v[124:127]\n"
    "v_exp_f32 v48, v88\n"
    "v_mfma_f32_16x16x32_bf16 v[64:67], a[136:139], a[8:11], v[64:67]\n"
    "v_exp_f32 v49, v89\n"
    "v_mfma_f32_16x16x32_bf16 v[68:71], a[140:143], a[8:11], v[68:71]\n"
    "v_exp_f32 v50, v90\n"
    "v_mfma_f32_16x16x32_bf16 v[76:79], a[140:143], a[12:15], v[76:79]\n"
    "v_exp_f32 v51, v91\n"
    "v_mfma_f32_16x16x32_bf16 v[72:75], a[136:139], a[12:15], v[72:75]\n"
    "ds_read_b64_tr_b16 a[86:87], v11 offset:22528\n"
    "v_add_f32 v43, v48, v49\n"
    "v_mfma_f32_16x16x32_bf16 v[64:67], a[144:147], a[16:19], v[64:67]\n"
    "ds_read_b64_tr_b16 a[88:89], v10 offset:20992\n"
    "buffer_load_dwordx4 v19, s[36:39], s63 offen offset:1024 nt lds\n"
    "v_mfma_f32_16x16x32_bf16 v[68:71], a[148:151], a[16:19], v[68:71]\n"
    "v_exp_f32 v52, v92\n"
    "v_mfma_f32_16x16x32_bf16 v[76:79], a[148:151], a[20:23], v[76:79]\n"
    "ds_read_b64_tr_b16 a[90:91], v10 offset:23040\n"
    "v_add_f32 v43, v43, v50\n"
    "v_mfma_f32_16x16x32_bf16 v[72:75], a[144:147], a[20:23], v[72:75]\n"
    "v_exp_f32 v53, v93\n"
    "v_mfma_f32_16x16x32_bf16 v[64:67], a[152:155], a[24:27], v[64:67]\n"
    "ds_read_b64_tr_b16 a[92:93], v11 offset:20992\n"
    "v_add_f32 v43, v43, v51\n"
    "v_mfma_f32_16x16x32_bf16 v[68:71], a[156:159], a[24:27], v[68:71]\n"
    "ds_read_b64_tr_b16 a[94:95], v11 offset:23040\n"
    "v_add_f32 v43, v43, v52\n"
    "v_mfma_f32_16x16x32_bf16 v[76:79], a[156:159], a[28:31], v[76:79]\n"
    "ds_read_b64_tr_b16 a[96:97], v10 offset:24576\n"
    "v_add_f32 v43, v43, v53\n"
    "v_mfma_f32_16x16x32_bf16 v[72:75], a[152:155], a[28:31], v[72:75]\n"
    "v_exp_f32 v54, v94\n"
    "v_mfma_f32_16x16x32_bf16 v[64:67], a[160:163], a[32:35], v[64:67]\n"
    "ds_read_b64_tr_b16 a[98:99], v10 offset:26624\n"
    "buffer_load_dwordx4 v20, s[36:39], s63 offen offset:2048 nt lds\n"
    "v_mfma_f32_16x16x32_bf16 v[68:71], a[164:167], a[32:35], v[68:71]\n"
    "ds_read_b64_tr_b16 a[100:101], v11 offset:24576\n"
    "v_add_f32 v43, v43, v54\n"
    "v_mfma_f32_16x16x32_bf16 v[76:79], a[164:167], a[36:39], v[76:79]\n"
    "v_exp_f32 v55, v95\n"
    "v_mfma_f32_16x16x32_bf16 v[72:75], a[160:163], a[36:39], v[72:75]\n"
    "ds_read_b64_tr_b16 a[102:103], v11 offset:26624\n"
    "v_add_f32 v43, v43, v55\n"
    "v_mfma_f32_16x16x32_bf16 v[64:67], a[168:171], a[40:43], v[64:67]\n"
    "ds_read_b64_tr_b16 a[104:105], v10 offset:25088\n"
    "v_add_f32 v44, v42, v43\n"
    "v_mfma_f32_16x16x32_bf16 v[68:71], a[172:175], a[40:43], v[68:71]\n"
    "ds_read_b64_tr_b16 a[106:107], v10 offset:27136\n"
    "v_cmp_nge_f32 vcc, s66, v44\n"
    "v_mfma_f32_16x16x32_bf16 v[76:79], a[172:175], a[44:47], v[76:79]\n"
    "ds_read_b64_tr_b16 a[108:109], v11 offset:25088\n"
    "v_cvt_pk_bf16_f32 v108, v48, v49\n"
    "v_mfma_f32_16x16x32_bf16 v[72:75], a[168:171], a[44:47], v[72:75]\n"
    "ds_read_b64_tr_b16 a[110:111], v11 offset:27136\n"
    "v_cvt_pk_bf16_f32 v109, v50, v51\n"
    "v_mfma_f32_16x16x32_bf16 v[64:67], a[176:179], a[48:51], v[64:67]\n"
    "ds_read_b64_tr_b16 a[112:113], v10 offset:28672\n"
    "v_cvt_pk_bf16_f32 v110, v52, v53\n"
    "v_mfma_f32_16x16x32_bf16 v[68:71], a[180:183], a[48:51], v[68:71]\n"
    "ds_read_b64_tr_b16 a[114:115], v10 offset:30720\n"
    "buffer_load_dwordx4 v21, s[36:39], s63 offen offset:3072 nt lds\n"
    "v_mfma_f32_16x16x32_bf16 v[76:79], a[180:183], a[52:55], v[76:79]\n"
    "ds_read_b64_tr_b16 a[116:117], v11 offset:28672\n"
    "v_cvt_pk_bf16_f32 v111, v54, v55\n"
    "v_mfma_f32_16x16x32_bf16 v[72:75], a[176:179], a[52:55], v[72:75]\n"
    "ds_read_b64_tr_b16 a[118:119], v11 offset:30720\n"
    "ds_read_b64_tr_b16 a[120:121], v10 offset:29184\n"
    "v_mfma_f32_16x16x32_bf16 v[64:67], a[184:187], a[56:59], v[64:67]\n"
    "ds_read_b64_tr_b16 a[122:123], v10 offset:31232\n"
    "ds_read_b64_tr_b16 a[124:125], v11 offset:29184\n"
    "v_mfma_f32_16x16x32_bf16 v[68:71], a[188:191], a[56:59], v[68:71]\n"
    "ds_read_b64_tr_b16 a[126:127], v11 offset:31232\n"
    "v_mfma_f32_16x16x32_bf16 v[76:79], a[188:191], a[60:63], v[76:79]\n"
    "v_mfma_f32_16x16x32_bf16 v[72:75], a[184:187], a[60:63], v[72:75]\n"
    "s_cbranch_vccnz L_stub0\n"
    "L_ret0:\n"
    "s_waitcnt vmcnt(13) lgkmcnt(6)\n"
    "s_barrier\n"
    "v_mfma_f32_16x16x32_bf16 v[128:131], a[64:67], v[104:107], v[128:131]\n"
    "ds_read_b128 a[128:131], v4 offset:49152\n"
    "s_add_u32 m0, s62, 0x10000\n"
    "v_mfma_f32_16x16x32_bf16 v[136:139], a[64:67], v[108:111], v[136:139]\n"
    "ds_read_b128 a[132:135], v4 offset:51200\n"
    "buffer_load_dwordx4 v22, s[40:43], s63 offen nt lds\n"
    "v_mfma_f32_16x16x32_bf16 v[140:143], a[68:71], v[108:111], v[140:143]\n"
    "ds_read_b128 a[136:139], v5 offset:49152\n"
    "v_add_f32 v40, v40, v42\n"
    "v_mfma_f32_16x16x32_bf16 v[132:135], a[68:71], v[104:107], v[132:135]\n"
    "v_exp_f32 v48, v64\n"
    "v_mfma_f32_16x16x32_bf16 v[144:147], a[72:75], v[104:107], v[144:147]\n"
    "v_exp_f32 v49, v65\n"
    "v_mfma_f32_16x16x32_bf16 v[152:155], a[72:75], v[108:111], v[152:155]\n"
    "ds_read_b128 a[140:143], v5 offset:51200\n"
    "v_cvt_pk_bf16_f32 v96, v48, v49\n"
    "v_mfma_f32_16x16x32_bf16 v[156:159], a[76:79], v[108:111], v[156:159]\n"
    "ds_read_b128 a[144:147], v4 offset:53248\n"
    "v_exp_f32 v50, v66\n"
    "v_mfma_f32_16x16x32_bf16 v[148:151], a[76:79], v[104:107], v[148:151]\n"
    "v_exp_f32 v51, v67\n"
    "v_mfma_f32_16x16x32_bf16 v[160:163], a[80:83], v[104:107], v[160:163]\n"
    "ds_read_b128 a[148:151], v4 offset:55296\n"
    "v_cvt_pk_bf16_f32 v97, v50, v51\n"
    "v_mfma_f32_16x16x32_bf16 v[168:171], a[80:83], v[108:111], v[168:171]\n"
    "v_exp_f32 v52, v68\n"
    "v_mfma_f32_16x16x32_bf16 v[172:175], a[84:87], v[108:111], v[172:175]\n"
    "buffer_load_dwordx4 v23, s[40:43], s63 offen offset:1024 nt lds\n"
    "v_mfma_f32_16x16x32_bf16 v[164:167], a[84:87], v[104:107], v[164:167]\n"
    "ds_read_b128 a[152:155], v5 offset:53248\n"
    "v_add_f32 v41, v41, v43\n"
    "v_mfma_f32_16x16x32_bf16 v[176:179], a[88:91], v[104:107], v[176:179]\n"
    "ds_read_b128 a[156:159], v5 offset:55296\n"
    "v_add_f32 v42, v48, v49\n"
    "v_mfma_f32_16x16x32_bf16 v[184:187], a[88:91], v[108:111], v[184:187]\n"
    "v_exp_f32 v53, v69\n"
    "v_mfma_f32_16x16x32_bf16 v[188:191], a[92:95], v[108:111], v[188:191]\n"
    "ds_read_b128 a[160:163], v4 offset:57344\n"
    "v_cvt_pk_bf16_f32 v98, v52, v53\n"
    "v_mfma_f32_16x16x32_bf16 v[180:183], a[92:95], v[104:107], v[180:183]\n"
    "ds_read_b128 a[164:167], v4 offset:59392\n"
    "v_add_f32 v42, v42, v50\n"
    "v_mfma_f32_16x16x32_bf16 v[192:195], a[96:99], v[104:107], v[192:195]\n"
    "ds_read_b128 a[168:171], v5 offset:57344\n"
    "v_add_f32 v42, v42, v51\n"
    "v_mfma_f32_16x16x32_bf16 v[200:203], a[96:99], v[108:111], v[200:203]\n"
    "buffer_load_dwordx4 v24, s[40:43], s63 offen offset:2048 nt lds\n"
    "v_mfma_f32_16x16x32_bf16 v[204:207], a[100:103], v[108:111], v[204:207]\n"
    "v_exp_f32 v54, v70\n"
    "v_mfma_f32_16x16x32_bf16 v[196:199], a[100:103], v[104:107], v[196:199]\n"
    "ds_read_b128 a[172:175], v5 offset:59392\n"
    "v_add_f32 v42, v42, v52\n"
    "v_mfma_f32_16x16x32_bf16 v[208:211], a[104:107], v[104:107], v[208:211]\n"
    "ds_read_b128 a[176:179], v4 offset:61440\n"
    "v_add_f32 v42, v42, v53\n"
    "v_mfma_f32_16x16x32_bf16 v[216:219], a[104:107], v[108:111], v[216:219]\n"
    "v_exp_f32 v55, v71\n"
    "v_mfma_f32_16x16x32_bf16 v[220:223], a[108:111], v[108:111], v[220:223]\n"
    "ds_read_b128 a[180:183], v4 offset:63488\n"
    "v_cvt_pk_bf16_f32 v99, v54, v55\n"
    "v_mfma_f32_16x16x32_bf16 v[212:215], a[108:111], v[104:107], v[212:215]\n"
    "ds_read_b128 a[184:187], v5 offset:61440\n"
    "v_add_f32 v42, v42, v54\n"
    "v_mfma_f32_16x16x32_bf16 v[224:227], a[112:115], v[104:107], v[224:227]\n"
    "ds_read_b128 a[188:191], v5 offset:63488\n"
    "v_add_f32 v42, v42, v55\n"
    "v_mfma_f32_16x16x32_bf16 v[232:235], a[112:115], v[108:111], v[232:235]\n"
    "ds_read_b64_tr_b16 a[64:65], v10 offset:32768\n"
    "s_waitcnt lgkmcnt(15)\n"
    "v_mfma_f32_16x16x32_bf16 v[236:239], a[116:119], v[108:111], v[236:239]\n"
    "ds_read_b64_tr_b16 a[66:67], v10 offset:34816\n"
    "v_mfma_f32_16x16x32_bf16 v[228:231], a[116:119], v[104:107], v[228:231]\n"
    "ds_read_b64_tr_b16 a[68:69], v11 offset:32768\n"
    "v_mfma_f32_16x16x32_bf16 v[240:243], a[120:123], v[104:107], v[240:243]\n"
    "buffer_load_dwordx4 v25, s[40:43], s63 offen offset:3072 nt lds\n"
    "v_mfma_f32_16x16x32_bf16 v[248:251], a[120:123], v[108:111], v[248:251]\n"
    "ds_read_b64_tr_b16 a[70:71], v11 offset:34816\n"
    "ds_read_b64_tr_b16 a[72:73], v10 offset:33280\n"
    "v_mfma_f32_16x16x32_bf16 v[252:255], a[124:127], v[108:111], v[252:255]\n"
    "ds_read_b64_tr_b16 a[74:75], v10 offset:35328\n"
    "ds_read_b64_tr_b16 a[76:77], v11 offset:33280\n"
    "v_mfma_f32_16x16x32_bf16 v[244:247], a[124:127], v[104:107], v[244:247]\n"
    "ds_read_b64_tr_b16 a[78:79], v11 offset:35328\n"
    "s_waitcnt lgkmcnt(8)\n"
    "v_mfma_f32_16x16x32_bf16 v[80:83], a[128:131], a[0:3], v[120:123]\n"
    "ds_read_b64_tr_b16 a[80:81], v10 offset:36864\n"
    "ds_read_b64_tr_b16 a[82:83], v10 offset:38912\n"
    "s_add_u32 m0, s62, 0x8000\n"
    "v_mfma_f32_16x16x32_bf16 v[84:87], a[132:135], a[0:3], v[120:123]\n"
    "ds_read_b64_tr_b16 a[84:85], v11 offset:36864\n"
    "s_add_u32 s63, s63, 0x4000\n"
    "v_mfma_f32_16x16x32_bf16 v[92:95], a[132:135], a[4:7], v[124:127]\n"
    "buffer_load_dwordx4 v18, s[36:39], s63 offen nt lds\n"
    "v_mfma_f32_16x16x32_bf16 v[88:91], a[128:131], a[4:7], v[124:127]\n"
    "v_exp_f32 v48, v72\n"
    "v_mfma_f32_16x16x32_bf16 v[80:83], a[136:139], a[8:11], v[80:83]\n"
    "v_exp_f32 v49, v73\n"
    "v_mfma_f32_16x16x32_bf16 v[84:87], a[140:143], a[8:11], v[84:87]\n"
    "v_exp_f32 v50, v74\n"
    "v_mfma_f32_16x16x32_bf16 v[92:95], a[140:143], a[12:15], v[92:95]\n"
    "v_exp_f32 v51, v75\n"
    "v_mfma_f32_16x16x32_bf16 v[88:91], a[136:139], a[12:15], v[88:91]\n"
    "ds_read_b64_tr_b16 a[86:87], v11 offset:38912\n"
    "v_add_f32 v43, v48, v49\n"
    "v_mfma_f32_16x16x32_bf16 v[80:83], a[144:147], a[16:19], v[80:83]\n"
    "ds_read_b64_tr_b16 a[88:89], v10 offset:37376\n"
    "buffer_load_dwordx4 v19, s[36:39], s63 offen offset:1024 nt lds\n"
    "v_mfma_f32_16x16x32_bf16 v[84:87], a[148:151], a[16:19], v[84:87]\n"
    "v_exp_f32 v52, v76\n"
    "v_mfma_f32_16x16x32_bf16 v[92:95], a[148:151], a[20:23], v[92:95]\n"
    "ds_read_b64_tr_b16 a[90:91], v10 offset:39424\n"
    "v_add_f32 v43, v43, v50\n"
    "v_mfma_f32_16x16x32_bf16 v[88:91], a[144:147], a[20:23], v[88:91]\n"
    "v_exp_f32 v53, v77\n"
    "v_mfma_f32_16x16x32_bf16 v[80:83], a[152:155], a[24:27], v[80:83]\n"
    "ds_read_b64_tr_b16 a[92:93], v11 offset:37376\n"
    "v_add_f32 v43, v43, v51\n"
    "v_mfma_f32_16x16x32_bf16 v[84:87], a[156:159], a[24:27], v[84:87]\n"
    "ds_read_b64_tr_b16 a[94:95], v11 offset:39424\n"
    "v_add_f32 v43, v43, v52\n"
    "v_mfma_f32_16x16x32_bf16 v[92:95], a[156:159], a[28:31], v[92:95]\n"
    "ds_read_b64_tr_b16 a[96:97], v10 offset:40960\n"
    "v_add_f32 v43, v43, v53\n"
    "v_mfma_f32_16x16x32_bf16 v[88:91], a[152:155], a[28:31], v[88:91]\n"
    "v_exp_f32 v54, v78\n"
    "v_mfma_f32_16x16x32_bf16 v[80:83], a[160:163], a[32:35], v[80:83]\n"
    "ds_read_b64_tr_b16 a[98:99], v10 offset:43008\n"
    "buffer_load_dwordx4 v20, s[36:39], s63 offen offset:2048 nt lds\n"
    "v_mfma_f32_16x16x32_bf16 v[84:87], a[164:167], a[32:35], v[84:87]\n"
    "ds_read_b64_tr_b16 a[100:101], v11 offset:40960\n"
    "v_add_f32 v43, v43, v54\n"
    "v_mfma_f32_16x16x32_bf16 v[92:95], a[164:167], a[36:39], v[92:95]\n"
    "v_exp_f32 v55, v79\n"
    "v_mfma_f32_16x16x32_bf16 v[88:91], a[160:163], a[36:39], v[88:91]\n"
    "ds_read_b64_tr_b16 a[102:103], v11 offset:43008\n"
    "v_add_f32 v43, v43, v55\n"
    "v_mfma_f32_16x16x32_bf16 v[80:83], a[168:171], a[40:43], v[80:83]\n"
    "ds_read_b64_tr_b16 a[104:105], v10 offset:41472\n"
    "v_add_f32 v44, v42, v43\n"
    "v_mfma_f32_16x16x32_bf16 v[84:87], a[172:175], a[40:43], v[84:87]\n"
    "ds_read_b64_tr_b16 a[106:107], v10 offset:43520\n"
    "v_cmp_nge_f32 vcc, s66, v44\n"
    "v_mfma_f32_16x16x32_bf16 v[92:95], a[172:175], a[44:47], v[92:95]\n"
    "ds_read_b64_tr_b16 a[108:109], v11 offset:41472\n"
    "v_cvt_pk_bf16_f32 v100, v48, v49\n"
    "v_mfma_f32_16x16x32_bf16 v[88:91], a[168:171], a[44:47], v[88:91]\n"
    "ds_read_b64_tr_b16 a[110:111], v11 offset:43520\n"
    "v_cvt_pk_bf16_f32 v101, v50, v51\n"
    "v_mfma_f32_16x16x32_bf16 v[80:83], a[176:179], a[48:51], v[80:83]\n"
    "ds_read_b64_tr_b16 a[112:113], v10 offset:45056\n"
    "v_cvt_pk_bf16_f32 v102, v52, v53\n"
    "v_mfma_f32_16x16x32_bf16 v[84:87], a[180:183], a[48:51], v[84:87]\n"
    "ds_read_b64_tr_b16 a[114:115], v10 offset:47104\n"
    "buffer_load_dwordx4 v21, s[36:39], s63 offen offset:3072 nt lds\n"
    "v_mfma_f32_16x16x32_bf16 v[92:95], a[180:183], a[52:55], v[92:95]\n"
    "ds_read_b64_tr_b16 a[116:117], v11 offset:45056\n"
    "v_cvt_pk_bf16_f32 v103, v54, v55\n"
    "v_mfma_f32_16x16x32_bf16 v[88:91], a[176:179], a[52:55], v[88:91]\n"
    "ds_read_b64_tr_b16 a[118:119], v11 offset:47104\n"
    "ds_read_b64_tr_b16 a[120:121], v10 offset:45568\n"
    "v_mfma_f32_16x16x32_bf16 v[80:83], a[184:187], a[56:59], v[80:83]\n"
    "ds_read_b64_tr_b16 a[122:123], v10 offset:47616\n"
    "ds_read_b64_tr_b16 a[124:125], v11 offset:45568\n"
    "v_mfma_f32_16x16x32_bf16 v[84:87], a[188:191], a[56:59], v[84:87]\n"
    "ds_read_b64_tr_b16 a[126:127], v11 offset:47616\n"
    "v_mfma_f32_16x16x32_bf16 v[92:95], a[188:191], a[60:63], v[92:95]\n"
    "v_mfma_f32_16x16x32_bf16 v[88:91], a[184:187], a[60:63], v[88:91]\n"
    "s_cbranch_vccnz L_stub1\n"
    "L_ret1:\n"
    "s_waitcnt vmcnt(12) lgkmcnt(6)\n"
    "s_barrier\n"
    "v_mfma_f32_16x16x32_bf16 v[128:131], a[64:67], v[96:99], v[128:131]\n"
    "ds_read_b128 a[128:131], v4 offset:0\n"
    "s_add_u32 m0, s62, 0x14000\n"
    "v_mfma_f32_16x16x32_bf16 v[136:139], a[64:67], v[100:103], v[136:139]\n"
    "ds_read_b128 a[132:135], v4 offset:2048\n"
    "buffer_load_dwordx4 v22, s[40:43], s63 offen nt lds\n"
    "v_mfma_f32_16x16x32_bf16 v[140:143], a[68:71], v[100:103], v[140:143]\n"
    "ds_read_b128 a[136:139], v5 offset:0\n"
    "v_add_f32 v40, v40, v42\n"
    "v_mfma_f32_16x16x32_bf16 v[132:135], a[68:71], v[96:99], v[132:135]\n"
    "v_exp_f32 v48, v80\n"
    "v_mfma_f32_16x16x32_bf16 v[144:147], a[72:75], v[96:99], v[144:147]\n"
    "v_exp_f32 v49, v81\n"
    "v_mfma_f32_16x16x32_bf16 v[152:155], a[72:75], v[100:103], v[152:155]\n"
    "ds_read_b128 a[140:143], v5 offset:2048\n"
    "v_cvt_pk_bf16_f32 v104, v48, v49\n"
    "v_mfma_f32_16x16x32_bf16 v[156:159], a[76:79], v[100:103], v[156:159]\n"
    "ds_read_b128 a[144:147], v4 offset:4096\n"
    "v_exp_f32 v50, v82\n"
    "v_mfma_f32_16x16x32_bf16 v[148:151], a[76:79], v[96:99], v[148:151]\n"
    "v_exp_f32 v51, v83\n"
    "v_mfma_f32_16x16x32_bf16 v[160:163], a[80:83], v[96:99], v[160:163]\n"
    "ds_read_b128 a[148:151], v4 offset:6144\n"
    "v_cvt_pk_bf16_f32 v105, v50, v51\n"
    "v_mfma_f32_16x16x32_bf16 v[168:171], a[80:83], v[100:103], v[168:171]\n"
    "v_exp_f32 v52, v84\n"
    "v_mfma_f32_16x16x32_bf16 v[172:175], a[84:87], v[100:103], v[172:175]\n"
    "buffer_load_dwordx4 v23, s[40:43], s63 offen offset:1024 nt lds\n"
    "v_mfma_f32_16x16x32_bf16 v[164:167], a[84:87], v[96:99], v[164:167]\n"
    "ds_read_b128 a[152:155], v5 offset:4096\n"
    "v_add_f32 v41, v41, v43\n"
    "v_mfma_f32_16x16x32_bf16 v[176:179], a[88:91], v[96:99], v[176:179]\n"
    "ds_read_b128 a[156:159], v5 offset:6144\n"
    "v_add_f32 v42, v48, v49\n"
    "v_mfma_f32_16x16x32_bf16 v[184:187], a[88:91], v[100:103], v[184:187]\n"
    "v_exp_f32 v53, v85\n"
    "v_mfma_f32_16x16x32_bf16 v[188:191], a[92:95], v[100:103], v[188:191]\n"
    "ds_read_b128 a[160:163], v4 offset:8192\n"
    "v_cvt_pk_bf16_f32 v106, v52, v53\n"
    "v_mfma_f32_16x16x32_bf16 v[180:183], a[92:95], v[96:99], v[180:183]\n"
    "ds_read_b128 a[164:167], v4 offset:10240\n"
    "v_add_f32 v42, v42, v50\n"
    "v_mfma_f32_16x16x32_bf16 v[192:195], a[96:99], v[96:99], v[192:195]\n"
    "ds_read_b128 a[168:171], v5 offset:8192\n"
    "v_add_f32 v42, v42, v51\n"
    "v_mfma_f32_16x16x32_bf16 v[200:203], a[96:99], v[100:103], v[200:203]\n"
    "buffer_load_dwordx4 v24, s[40:43], s63 offen offset:2048 nt lds\n"
    "v_mfma_f32_16x16x32_bf16 v[204:207], a[100:103], v[100:103], v[204:207]\n"
    "v_exp_f32 v54, v86\n"
    "v_mfma_f32_16x16x32_bf16 v[196:199], a[100:103], v[96:99], v[196:199]\n"
    "ds_read_b128 a[172:175], v5 offset:10240\n"
    "v_add_f32 v42, v42, v52\n"
    "v_mfma_f32_16x16x32_bf16 v[208:211], a[104:107], v[96:99], v[208:211]\n"
    "ds_read_b128 a[176:179], v4 offset:12288\n"
    "v_add_f32 v42, v42, v53\n"
    "v_mfma_f32_16x16x32_bf16 v[216:219], a[104:107], v[100:103], v[216:219]\n"
    "v_exp_f32 v55, v87\n"
    "v_mfma_f32_16x16x32_bf16 v[220:223], a[108:111], v[100:103], v[220:223]\n"
    "ds_read_b128 a[180:183], v4 offset:14336\n"
    "v_cvt_pk_bf16_f32 v107, v54, v55\n"
    "v_mfma_f32_16x16x32_bf16 v[212:215], a[108:111], v[96:99], v[212:215]\n"
    "ds_read_b128 a[184:187], v5 offset:12288\n"
    "v_add_f32 v42, v42, v54\n"
    "v_mfma_f32_16x16x32_bf16 v[224:227], a[112:115], v[96:99], v[224:227]\n"
    "ds_read_b128 a[188:191], v5 offset:14336\n"
    "v_add_f32 v42, v42, v55\n"
    "v_mfma_f32_16x16x32_bf16 v[232:235], a[112:115], v[100:103], v[232:235]\n"
    "ds_read_b64_tr_b16 a[64:65], v10 offset:49152\n"
    "s_waitcnt lgkmcnt(15)\n"
    "v_mfma_f32_16x16x32_bf16 v[236:239], a[116:119], v[100:103], v[236:239]\n"
    "ds_read_b64_tr_b16 a[66:67], v10 offset:51200\n"
    "v_mfma_f32_16x16x32_bf16 v[228:231], a[116:119], v[96:99], v[228:231]\n"
    "ds_read_b64_tr_b16 a[68:69], v11 offset:49152\n"
    "v_mfma_f32_16x16x32_bf16 v[240:243], a[120:123], v[96:99], v[240:243]\n"
    "buffer_load_dwordx4 v25, s[40:43], s63 offen offset:3072 nt lds\n"
    "v_mfma_f32_16x16x32_bf16 v[248:251], a[120:123], v[100:103], v[248:251]\n"
    "ds_read_b64_tr_b16 a[70:71], v11 offset:51200\n"
    "ds_read_b64_tr_b16 a[72:73], v10 offset:49664\n"
    "v_mfma_f32_16x16x32_bf16 v[252:255], a[124:127], v[100:103], v[252:255]\n"
    "ds_read_b64_tr_b16 a[74:75], v10 offset:51712\n"
    "ds_read_b64_tr_b16 a[76:77], v11 offset:49664\n"
    "v_mfma_f32_16x16x32_bf16 v[244:247], a[124:127], v[96:99], v[244:247]\n"
    "ds_read_b64_tr_b16 a[78:79], v11 offset:51712\n"
    "s_waitcnt lgkmcnt(8)\n"
    "v_add_u32 v62, 1, v62\n"
    "s_waitcnt vmcnt(20)\n"
    "buffer_store_dword v62, v27, s[20:23], 0 offen sc1\n"
    "s_mov_b32 s64, 6\n"
    "L_iw3:\n"
    "s_cbranch_execz L_iw7\n"
    ".p2align 6\n"
    "L_loop:\n"
    "v_mfma_f32_16x16x32_bf16 v[64:67], a[128:131], a[0:3], v[120:123]\n"
    "ds_read_b64_tr_b16 a[80:81], v10 offset:53248\n"
    "ds_read_b64_tr_b16 a[82:83], v10 offset:55296\n"
    "s_add_u32 m0, s62, 0xc000\n"
    "v_mfma_f32_16x16x32_bf16 v[68:71], a[132:135], a[0:3], v[120:123]\n"
    "ds_read_b64_tr_b16 a[84:85], v11 offset:53248\n"
    "s_add_u32 s63, s63, 0x4000\n"
    "v_mfma_f32_16x16x32_bf16 v[76:79], a[132:135], a[4:7], v[124:127]\n"
    "buffer_load_dwordx4 v18, s[36:39], s63 offen nt lds\n"
    "v_mfma_f32_16x16x32_bf16 v[72:75], a[128:131], a[4:7], v[124:127]\n"
    "v_exp_f32 v48, v88\n"
    "v_mfma_f32_16x16x32_bf16 v[64:67], a[136:139], a[8:11], v[64:67]\n"
    "v_exp_f32 v49, v89\n"
    "v_mfma_f32_16x16x32_bf16 v[68:71], a[140:143], a[8:11], v[68:71]\n"
    "v_exp_f32 v50, v90\n"
    "v_mfma_f32_16x16x32_bf16 v[76:79], a[140:143], a[12:15], v[76:79]\n"
    "v_exp_f32 v51, v91\n"
    "v_mfma_f32_16x16x32_bf16 v[72:75], a[136:139], a[12:15], v[72:75]\n"
    "ds_read_b64_tr_b16 a[86:87], v11 offset:55296\n"
    "v_add_f32 v43, v48, v49\n"
    "v_mfma_f32_16x16x32_bf16 v[64:67], a[144:147], a[16:19], v[64:67]\n"
    "ds_read_b64_tr_b16 a[88:89], v10 offset:53760\n"
    "buffer_load_dwordx4 v19, s[36:39], s63 offen offset:1024 nt lds\n"
    "v_mfma_f32_16x16x32_bf16 v[68:71], a[148:151], a[16:19], v[68:71]\n"
    "v_exp_f32 v52, v92\n"
    "v_mfma_f32_16x16x32_bf16 v[76:79], a[148:151], a[20:23], v[76:79]\n"
    "ds_read_b64_tr_b16 a[90:91], v10 offset:55808\n"
    "v_add_f32 v43, v43, v50\n"
    "v_mfma_f32_16x16x32_bf16 v[72:75], a[144:147], a[20:23], v[72:75]\n"
    "v_exp_f32 v53, v93\n"
    "v_mfma_f32_16x16x32_bf16 v[64:67], a[152:155], a[24:27], v[64:67]\n"
    "ds_read_b64_tr_b16 a[92:93], v11 offset:53760\n"
    "v_add_f32 v43, v43, v51\n"
    "v_mfma_f32_16x16x32_bf16 v[68:71], a[156:159], a[24:27], v[68:71]\n"
    "ds_read_b64_tr_b16 a[94:95], v11 offset:55808\n"
    "v_add_f32 v43, v43, v52\n"
    "v_mfma_f32_16x16x32_bf16 v[76:79], a[156:159], a[28:31], v[76:79]\n"
    "ds_read_b64_tr_b16 a[96:97], v10 offset:57344\n"
    "v_add_f32 v43, v43, v53\n"
    "v_mfma_f32_16x16x32_bf16 v[72:75], a[152:155], a[28:31], v[72:75]\n"
    "v_exp_f32 v54, v94\n"
    "v_mfma_f32_16x16x32_bf16 v[64:67], a[160:163], a[32:35], v[64:67]\n"
    "ds_read_b64_tr_b16 a[98:99], v10 offset:59392\n"
    "buffer_load_dwordx4 v20, s[36:39], s63 offen offset:2048 nt lds\n"
    "v_mfma_f32_16x16x32_bf16 v[68:71], a[164:167], a[32:35], v[68:71]\n"
    "ds_read_b64_tr_b16 a[100:101], v11 offset:57344\n"
    "v_add_f32 v43, v43, v54\n"
    "v_mfma_f32_16x16x32_bf16 v[76:79], a[164:167], a[36:39], v[76:79]\n"
    "v_exp_f32 v55, v95\n"
    "v_mfma_f32_16x16x32_bf16 v[72:75], a[160:163], a[36:39], v[72:75]\n"
    "ds_read_b64_tr_b16 a[102:103], v11 offset:59392\n"
    "v_add_f32 v43, v43, v55\n"
    "v_mfma_f32_16x16x32_bf16 v[64:67], a[168:171], a[40:43], v[64:67]\n"
    "ds_read_b64_tr_b16 a[104:105], v10 offset:57856\n"
    "v_add_f32 v44, v42, v43\n"
    "v_mfma_f32_16x16x32_bf16 v[68:71], a[172:175], a[40:43], v[68:71]\n"
    "ds_read_b64_tr_b16 a[106:107], v10 offset:59904\n"
    "v_cmp_nge_f32 vcc, s66, v44\n"
    "v_mfma_f32_16x16x32_bf16 v[76:79], a[172:175], a[44:47], v[76:79]\n"
    "ds_read_b64_tr_b16 a[108:109], v11 offset:57856\n"
    "v_cvt_pk_bf16_f32 v108, v48, v49\n"
    "v_mfma_f32_16x16x32_bf16 v[72:75], a[168:171], a[44:47], v[72:75]\n"
    "ds_read_b64_tr_b16 a[110:111], v11 offset:59904\n"
    "v_cvt_pk_bf16_f32 v109, v50, v51\n"
    "v_mfma_f32_16x16x32_bf16 v[64:67], a[176:179], a[48:51], v[64:67]\n"
    "ds_read_b64_tr_b16 a[112:113], v10 offset:61440\n"
    "v_cvt_pk_bf16_f32 v110, v52, v53\n"
    "v_mfma_f32_16x16x32_bf16 v[68:71], a[180:183], a[48:51], v[68:71]\n"
    "ds_read_b64_tr_b16 a[114:115], v10 offset:63488\n"
    "buffer_load_dwordx4 v21, s[36:39], s63 offen offset:3072 nt lds\n"
    "v_mfma_f32_16x16x32_bf16 v[76:79], a[180:183], a[52:55], v[76:79]\n"
    "ds_read_b64_tr_b16 a[116:117], v11 offset:61440\n"
    "v_cvt_pk_bf16_f32 v111, v54, v55\n"
    "v_mfma_f32_16x16x32_bf16 v[72:75], a[176:179], a[52:55], v[72:75]\n"
    "ds_read_b64_tr_b16 a[118:119], v11 offset:63488\n"
    "ds_read_b64_tr_b16 a[120:121], v10 offset:61952\n"
    "v_mfma_f32_16x16x32_bf16 v[64:67], a[184:187], a[56:59], v[64:67]\n"
    "ds_read_b64_tr_b16 a[122:123], v10 offset:64000\n"
    "ds_read_b64_tr_b16 a[124:125], v11 offset:61952\n"
    "v_mfma_f32_16x16x32_bf16 v[68:71], a[188:191], a[56:59], v[68:71]\n"
    "ds_read_b64_tr_b16 a[126:127], v11 offset:64000\n"
    "v_mfma_f32_16x16x32_bf16 v[76:79], a[188:191], a[60:63], v[76:79]\n"
    "v_mfma_f32_16x16x32_bf16 v[72:75], a[184:187], a[60:63], v[72:75]\n"
    "s_cbranch_vccnz L_stub2\n"
    "L_ret2:\n"
    "s_waitcnt vmcnt(12) lgkmcnt(6)\n"
    "s_barrier\n"
    "v_mfma_f32_16x16x32_bf16 v[128:131], a[64:67], v[104:107], v[128:131]\n"
    "ds_read_b128 a[128:131], v4 offset:16384\n"
    "s_add_u32 m0, s62, 0x18000\n"
    "v_mfma_f32_16x16x32_bf16 v[136:139], a[64:67], v[108:111], v[136:139]\n"
    "ds_read_b128 a[132:135], v4 offset:18432\n"
    "buffer_load_dwordx4 v22, s[40:43], s63 offen nt lds\n"
    "v_mfma_f32_16x16x32_bf16 v[140:143], a[68:71], v[108:111], v[140:143]\n"
    "ds_read_b128 a[136:139], v5 offset:16384\n"
    "v_add_f32 v40, v40, v42\n"
    "v_mfma_f32_16x16x32_bf16 v[132:135], a[68:71], v[104:107], v[132:135]\n"
    "v_exp_f32 v48, v64\n"
    "v_mfma_f32_16x16x32_bf16 v[144:147], a[72:75], v[104:107], v[144:147]\n"
    "v_exp_f32 v49, v65\n"
    "v_mfma_f32_16x16x32_bf16 v[152:155], a[72:75], v[108:111], v[152:155]\n"
    "ds_read_b128 a[140:143], v5 offset:18432\n"
    "v_cvt_pk_bf16_f32 v96, v48, v49\n"
    "v_mfma_f32_16x16x32_bf16 v[156:159], a[76:79], v[108:111], v[156:159]\n"
    "ds_read_b128 a[144:147], v4 offset:20480\n"
    "v_exp_f32 v50, v66\n"
    "v_mfma_f32_16x16x32_bf16 v[148:151], a[76:79], v[104:107], v[148:151]\n"
    "v_exp_f32 v51, v67\n"
    "v_mfma_f32_16x16x32_bf16 v[160:163], a[80:83], v[104:107], v[160:163]\n"
    "ds_read_b128 a[148:151], v4 offset:22528\n"
    "v_cvt_pk_bf16_f32 v97, v50, v51\n"
    "v_mfma_f32_16x16x32_bf16 v[168:171], a[80:83], v[108:111], v[168:171]\n"
    "v_exp_f32 v52, v68\n"
    "v_mfma_f32_16x16x32_bf16 v[172:175], a[84:87], v[108:111], v[172:175]\n"
    "buffer_load_dwordx4 v23, s[40:43], s63 offen offset:1024 nt lds\n"
    "v_mfma_f32_16x16x32_bf16 v[164:167], a[84:87], v[104:107], v[164:167]\n"
    "ds_read_b128 a[152:155], v5 offset:20480\n"
    "v_add_f32 v41, v41, v43\n"
    "v_mfma_f32_16x16x32_bf16 v[176:179], a[88:91], v[104:107], v[176:179]\n"
    "ds_read_b128 a[156:159], v5 offset:22528\n"
    "v_add_f32 v42, v48, v49\n"
    "v_mfma_f32_16x16x32_bf16 v[184:187], a[88:91], v[108:111], v[184:187]\n"
    "v_exp_f32 v53, v69\n"
    "v_mfma_f32_16x16x32_bf16 v[188:191], a[92:95], v[108:111], v[188:191]\n"
    "ds_read_b128 a[160:163], v4 offset:24576\n"
    "v_cvt_pk_bf16_f32 v98, v52, v53\n"
    "v_mfma_f32_16x16x32_bf16 v[180:183], a[92:95], v[104:107], v[180:183]\n"
    "ds_read_b128 a[164:167], v4 offset:26624\n"
    "v_add_f32 v42, v42, v50\n"
    "v_mfma_f32_16x16x32_bf16 v[192:195], a[96:99], v[104:107], v[192:195]\n"
    "ds_read_b128 a[168:171], v5 offset:24576\n"
    "v_add_f32 v42, v42, v51\n"
    "v_mfma_f32_16x16x32_bf16 v[200:203], a[96:99], v[108:111], v[200:203]\n"
    "buffer_load_dwordx4 v24, s[40:43], s63 offen offset:2048 nt lds\n"
    "v_mfma_f32_16x16x32_bf16 v[204:207], a[100:103], v[108:111], v[204:207]\n"
    "v_exp_f32 v54, v70\n"
    "v_mfma_f32_16x16x32_bf16 v[196:199], a[100:103], v[104:107], v[196:199]\n"
    "ds_read_b128 a[172:175], v5 offset:26624\n"
    "v_add_f32 v42, v42, v52\n"
    "v_mfma_f32_16x16x32_bf16 v[208:211], a[104:107], v[104:107], v[208:211]\n"
    "ds_read_b128 a[176:179], v4 offset:28672\n"
    "v_add_f32 v42, v42, v53\n"
    "v_mfma_f32_16x16x32_bf16 v[216:219], a[104:107], v[108:111], v[216:219]\n"
    "v_exp_f32 v55, v71\n"
    "v_mfma_f32_16x16x32_bf16 v[220:223], a[108:111], v[108:111], v[220:223]\n"
    "ds_read_b128 a[180:183], v4 offset:30720\n"
    "v_cvt_pk_bf16_f32 v99, v54, v55\n"
    "v_mfma_f32_16x16x32_bf16 v[212:215], a[108:111], v[104:107], v[212:215]\n"
    "ds_read_b128 a[184:187], v5 offset:28672\n"
    "v_add_f32 v42, v42, v54\n"
    "v_mfma_f32_16x16x32_bf16 v[224:227], a[112:115], v[104:107], v[224:227]\n"
    "ds_read_b128 a[188:191], v5 offset:30720\n"
    "v_add_f32 v42, v42, v55\n"
    "v_mfma_f32_16x16x32_bf16 v[232:235], a[112:115], v[108:111], v[232:235]\n"
    "ds_read_b64_tr_b16 a[64:65], v10 offset:0\n"
    "s_waitcnt lgkmcnt(15)\n"
    "v_mfma_f32_16x16x32_bf16 v[236:239], a[116:119], v[108:111], v[236:239]\n"
    "ds_read_b64_tr_b16 a[66:67], v10 offset:2048\n"
    "v_mfma_f32_16x16x32_bf16 v[228:231], a[116:119], v[104:107], v[228:231]\n"
    "ds_read_b64_tr_b16 a[68:69], v11 offset:0\n"
    "v_mfma_f32_16x16x32_bf16 v[240:243], a[120:123], v[104:107], v[240:243]\n"
    "buffer_load_dwordx4 v25, s[40:43], s63 offen offset:3072 nt lds\n"
    "v_mfma_f32_16x16x32_bf16 v[248:251], a[120:123], v[108:111], v[248:251]\n"
    "ds_read_b64_tr_b16 a[70:71], v11 offset:2048\n"
    "ds_read_b64_tr_b16 a[72:73], v10 offset:512\n"
    "v_mfma_f32_16x16x32_bf16 v[252:255], a[124:127], v[108:111], v[252:255]\n"
    "ds_read_b64_tr_b16 a[74:75], v10 offset:2560\n"
    "ds_read_b64_tr_b16 a[76:77], v11 offset:512\n"
    "v_mfma_f32_16x16x32_bf16 v[244:247], a[124:127], v[104:107], v[244:247]\n"
    "ds_read_b64_tr_b16 a[78:79], v11 offset:2560\n"
    "s_waitcnt lgkmcnt(8)\n"
    "v_mfma_f32_16x16x32_bf16 v[80:83], a[128:131], a[0:3], v[120:123]\n"
    "ds_read_b64_tr_b16 a[80:81], v10 offset:4096\n"
    "ds_read_b64_tr_b16 a[82:83], v10 offset:6144\n"
    "s_add_u32 m0, s62, 0\n"
    "v_mfma_f32_16x16x32_bf16 v[84:87], a[132:135], a[0:3], v[120:123]\n"
    "ds_read_b64_tr_b16 a[84:85], v11 offset:4096\n"
    "s_add_u32 s63, s63, 0x4000\n"
    "v_mfma_f32_16x16x32_bf16 v[92:95], a[132:135], a[4:7], v[124:127]\n"
    "buffer_load_dwordx4 v18, s[36:39], s63 offen nt lds\n"
    "v_mfma_f32_16x16x32_bf16 v[88:91], a[128:131], a[4:7], v[124:127]\n"
    "v_exp_f32 v48, v72\n"
    "v_mfma_f32_16x16x32_bf16 v[80:83], a[136:139], a[8:11], v[80:83]\n"
    "v_exp_f32 v49, v73\n"
    "v_mfma_f32_16x16x32_bf16 v[84:87], a[140:143], a[8:11], v[84:87]\n"
    "v_exp_f32 v50, v74\n"
    "v_mfma_f32_16x16x32_bf16 v[92:95], a[140:143], a[12:15], v[92:95]\n"
    "v_exp_f32 v51, v75\n"
    "v_mfma_f32_16x16x32_bf16 v[88:91], a[136:139], a[12:15], v[88:91]\n"
    "ds_read_b64_tr_b16 a[86:87], v11 offset:6144\n"
    "v_add_f32 v43, v48, v49\n"
    "v_mfma_f32_16x16x32_bf16 v[80:83], a[144:147], a[16:19], v[80:83]\n"
    "ds_read_b64_tr_b16 a[88:89], v10 offset:4608\n"
    "buffer_load_dwordx4 v19, s[36:39], s63 offen offset:1024 nt lds\n"
    "v_mfma_f32_16x16x32_bf16 v[84:87], a[148:151], a[16:19], v[84:87]\n"
    "v_exp_f32 v52, v76\n"
    "v_mfma_f32_16x16x32_bf16 v[92:95], a[148:151], a[20:23], v[92:95]\n"
    "ds_read_b64_tr_b16 a[90:91], v10 offset:6656\n"
    "v_add_f32 v43, v43, v50\n"
    "v_mfma_f32_16x16x32_bf16 v[88:91], a[144:147], a[20:23], v[88:91]\n"
    "v_exp_f32 v53, v77\n"
    "v_mfma_f32_16x16x32_bf16 v[80:83], a[152:155], a[24:27], v[80:83]\n"
    "ds_read_b64_tr_b16 a[92:93], v11 offset:4608\n"
    "v_add_f32 v43, v43, v51\n"
    "v_mfma_f32_16x16x32_bf16 v[84:87], a[156:159], a[24:27], v[84:87]\n"
    "ds_read_b64_tr_b16 a[94:95], v11 offset:6656\n"
    "v_add_f32 v43, v43, v52\n"
    "v_mfma_f32_16x16x32_bf16 v[92:95], a[156:159], a[28:31], v[92:95]\n"
    "ds_read_b64_tr_b16 a[96:97], v10 offset:8192\n"
    "v_add_f32 v43, v43, v53\n"
    "v_mfma_f32_16x16x32_bf16 v[88:91], a[152:155], a[28:31], v[88:91]\n"
    "v_exp_f32 v54, v78\n"
    "v_mfma_f32_16x16x32_bf16 v[80:83], a[160:163], a[32:35], v[80:83]\n"
    "ds_read_b64_tr_b16 a[98:99], v10 offset:10240\n"
    "buffer_load_dwordx4 v20, s[36:39], s63 offen offset:2048 nt lds\n"
    "v_mfma_f32_16x16x32_bf16 v[84:87], a[164:167], a[32:35], v[84:87]\n"
    "ds_read_b64_tr_b16 a[100:101], v11 offset:8192\n"
    "v_add_f32 v43, v43, v54\n"
    "v_mfma_f32_16x16x32_bf16 v[92:95], a[164:167], a[36:39], v[92:95]\n"
    "v_exp_f32 v55, v79\n"
    "v_mfma_f32_16x16x32_bf16 v[88:91], a[160:163], a[36:39], v[88:91]\n"
    "ds_read_b64_tr_b16 a[102:103], v11 offset:10240\n"
    "v_add_f32 v43, v43, v55\n"
    "v_mfma_f32_16x16x32_bf16 v[80:83], a[168:171], a[40:43], v[80:83]\n"
    "ds_read_b64_tr_b16 a[104:105], v10 offset:8704\n"
    "v_add_f32 v44, v42, v43\n"
    "v_mfma_f32_16x16x32_bf16 v[84:87], a[172:175], a[40:43], v[84:87]\n"
    "ds_read_b64_tr_b16 a[106:107], v10 offset:10752\n"
    "v_cmp_nge_f32 vcc, s66, v44\n"
    "v_mfma_f32_16x16x32_bf16 v[92:95], a[172:175], a[44:47], v[92:95]\n"
    "ds_read_b64_tr_b16 a[108:109], v11 offset:8704\n"
    "v_cvt_pk_bf16_f32 v100, v48, v49\n"
    "v_mfma_f32_16x16x32_bf16 v[88:91], a[168:171], a[44:47], v[88:91]\n"
    "ds_read_b64_tr_b16 a[110:111], v11 offset:10752\n"
    "v_cvt_pk_bf16_f32 v101, v50, v51\n"
    "v_mfma_f32_16x16x32_bf16 v[80:83], a[176:179], a[48:51], v[80:83]\n"
    "ds_read_b64_tr_b16 a[112:113], v10 offset:12288\n"
    "v_cvt_pk_bf16_f32 v102, v52, v53\n"
    "v_mfma_f32_16x16x32_bf16 v[84:87], a[180:183], a[48:51], v[84:87]\n"
    "ds_read_b64_tr_b16 a[114:115], v10 offset:14336\n"
    "buffer_load_dwordx4 v21, s[36:39], s63 offen offset:3072 nt lds\n"
    "v_mfma_f32_16x16x32_bf16 v[92:95], a[180:183], a[52:55], v[92:95]\n"
    "ds_read_b64_tr_b16 a[116:117], v11 offset:12288\n"
    "v_cvt_pk_bf16_f32 v103, v54, v55\n"
    "v_mfma_f32_16x16x32_bf16 v[88:91], a[176:179], a[52:55], v[88:91]\n"
    "ds_read_b64_tr_b16 a[118:119], v11 offset:14336\n"
    "ds_read_b64_tr_b16 a[120:121], v10 offset:12800\n"
    "v_mfma_f32_16x16x32_bf16 v[80:83], a[184:187], a[56:59], v[80:83]\n"
    "ds_read_b64_tr_b16 a[122:123], v10 offset:14848\n"
    "ds_read_b64_tr_b16 a[124:125], v11 offset:12800\n"
    "v_mfma_f32_16x16x32_bf16 v[84:87], a[188:191], a[56:59], v[84:87]\n"
    "ds_read_b64_tr_b16 a[126:127], v11 offset:14848\n"
    "v_mfma_f32_16x16x32_bf16 v[92:95], a[188:191], a[60:63], v[92:95]\n"
    "v_mfma_f32_16x16x32_bf16 v[88:91], a[184:187], a[60:63], v[88:91]\n"
    "s_cbranch_vccnz L_stub3\n"
    "L_ret3:\n"
    "s_waitcnt vmcnt(12) lgkmcnt(6)\n"
    "s_barrier\n"
    "v_mfma_f32_16x16x32_bf16 v[128:131], a[64:67], v[96:99], v[128:131]\n"
    "ds_read_b128 a[128:131], v4 offset:32768\n"
    "s_add_u32 m0, s62, 0x1c000\n"
    "v_mfma_f32_16x16x32_bf16 v[136:139], a[64:67], v[100:103], v[136:139]\n"
    "ds_read_b128 a[132:135], v4 offset:34816\n"
    "buffer_load_dwordx4 v22, s[40:43], s63 offen nt lds\n"
    "v_mfma_f32_16x16x32_bf16 v[140:143], a[68:71], v[100:103], v[140:143]\n"
    "ds_read_b128 a[136:139], v5 offset:32768\n"
    "v_add_f32 v40, v40, v42\n"
    "v_mfma_f32_16x16x32_bf16 v[132:135], a[68:71], v[96:99], v[132:135]\n"
    "v_exp_f32 v48, v80\n"
    "v_mfma_f32_16x16x32_bf16 v[144:147], a[72:75], v[96:99], v[144:147]\n"
    "v_exp_f32 v49, v81\n"
    "v_mfma_f32_16x16x32_bf16 v[152:155], a[72:75], v[100:103], v[152:155]\n"
    "ds_read_b128 a[140:143], v5 offset:34816\n"
    "v_cvt_pk_bf16_f32 v104, v48, v49\n"
    "v_mfma_f32_16x16x32_bf16 v[156:159], a[76:79], v[100:103], v[156:159]\n"
    "ds_read_b128 a[144:147], v4 offset:36864\n"
    "v_exp_f32 v50, v82\n"
    "v_mfma_f32_16x16x32_bf16 v[148:151], a[76:79], v[96:99], v[148:151]\n"
    "v_exp_f32 v51, v83\n"
    "v_mfma_f32_16x16x32_bf16 v[160:163], a[80:83], v[96:99], v[160:163]\n"
    "ds_read_b128 a[148:151], v4 offset:38912\n"
    "v_cvt_pk_bf16_f32 v105, v50, v51\n"
    "v_mfma_f32_16x16x32_bf16 v[168:171], a[80:83], v[100:103], v[168:171]\n"
    "v_exp_f32 v52, v84\n"
    "v_mfma_f32_16x16x32_bf16 v[172:175], a[84:87], v[100:103], v[172:175]\n"
    "buffer_load_dwordx4 v23, s[40:43], s63 offen offset:1024 nt lds\n"
    "v_mfma_f32_16x16x32_bf16 v[164:167], a[84:87], v[96:99], v[164:167]\n"
    "ds_read_b128 a[152:155], v5 offset:36864\n"
    "v_add_f32 v41, v41, v43\n"
    "v_mfma_f32_16x16x32_bf16 v[176:179], a[88:91], v[96:99], v[176:179]\n"
    "ds_read_b128 a[156:159], v5 offset:38912\n"
    "v_add_f32 v42, v48, v49\n"
    "v_mfma_f32_16x16x32_bf16 v[184:187], a[88:91], v[100:103], v[184:187]\n"
    "v_exp_f32 v53, v85\n"
    "v_mfma_f32_16x16x32_bf16 v[188:191], a[92:95], v[100:103], v[188:191]\n"
    "ds_read_b128 a[160:163], v4 offset:40960\n"
    "v_cvt_pk_bf16_f32 v106, v52, v53\n"
    "v_mfma_f32_16x16x32_bf16 v[180:183], a[92:95], v[96:99], v[180:183]\n"
    "ds_read_b128 a[164:167], v4 offset:43008\n"
    "v_add_f32 v42, v42, v50\n"
    "v_mfma_f32_16x16x32_bf16 v[192:195], a[96:99], v[96:99], v[192:195]\n"
    "ds_read_b128 a[168:171], v5 offset:40960\n"
    "v_add_f32 v42, v42, v51\n"
    "v_mfma_f32_16x16x32_bf16 v[200:203], a[96:99], v[100:103], v[200:203]\n"
    "buffer_load_dwordx4 v24, s[40:43], s63 offen offset:2048 nt lds\n"
    "v_mfma_f32_16x16x32_bf16 v[204:207], a[100:103], v[100:103], v[204:207]\n"
    "v_exp_f32 v54, v86\n"
    "v_mfma_f32_16x16x32_bf16 v[196:199], a[100:103], v[96:99], v[196:199]\n"
    "ds_read_b128 a[172:175], v5 offset:43008\n"
    "v_add_f32 v42, v42, v52\n"
    "v_mfma_f32_16x16x32_bf16 v[208:211], a[104:107], v[96:99], v[208:211]\n"
    "ds_read_b128 a[176:179], v4 offset:45056\n"
    "v_add_f32 v42, v42, v53\n"
    "v_mfma_f32_16x16x32_bf16 v[216:219], a[104:107], v[100:103], v[216:219]\n"
    "v_exp_f32 v55, v87\n"
    "v_mfma_f32_16x16x32_bf16 v[220:223], a[108:111], v[100:103], v[220:223]\n"
    "ds_read_b128 a[180:183], v4 offset:47104\n"
    "v_cvt_pk_bf16_f32 v107, v54, v55\n"
    "v_mfma_f32_16x16x32_bf16 v[212:215], a[108:111], v[96:99], v[212:215]\n"
    "ds_read_b128 a[184:187], v5 offset:45056\n"
    "v_add_f32 v42, v42, v54\n"
    "v_mfma_f32_16x16x32_bf16 v[224:227], a[112:115], v[96:99], v[224:227]\n"
    "ds_read_b128 a[188:191], v5 offset:47104\n"
    "v_add_f32 v42, v42, v55\n"
    "v_mfma_f32_16x16x32_bf16 v[232:235], a[112:115], v[100:103], v[232:235]\n"
    "ds_read_b64_tr_b16 a[64:65], v10 offset:16384\n"
    "s_waitcnt lgkmcnt(15)\n"
    "v_mfma_f32_16x16x32_bf16 v[236:239], a[116:119], v[100:103], v[236:239]\n"
    "ds_read_b64_tr_b16 a[66:67], v10 offset:18432\n"
    "v_mfma_f32_16x16x32_bf16 v[228:231], a[116:119], v[96:99], v[228:231]\n"
    "ds_read_b64_tr_b16 a[68:69], v11 offset:16384\n"
    "v_mfma_f32_16x16x32_bf16 v[240:243], a[120:123], v[96:99], v[240:243]\n"
    "buffer_load_dwordx4 v25, s[40:43], s63 offen offset:3072 nt lds\n"
    "v_mfma_f32_16x16x32_bf16 v[248:251], a[120:123], v[100:103], v[248:251]\n"
    "ds_read_b64_tr_b16 a[70:71], v11 offset:18432\n"
    "ds_read_b64_tr_b16 a[72:73], v10 offset:16896\n"
    "v_mfma_f32_16x16x32_bf16 v[252:255], a[124:127], v[100:103], v[252:255]\n"
    "ds_read_b64_tr_b16 a[74:75], v10 offset:18944\n"
    "ds_read_b64_tr_b16 a[76:77], v11 offset:16896\n"
    "v_mfma_f32_16x16x32_bf16 v[244:247], a[124:127], v[96:99], v[244:247]\n"
    "ds_read_b64_tr_b16 a[78:79], v11 offset:18944\n"
    "s_waitcnt lgkmcnt(8)\n"
    "v_mfma_f32_16x16x32_bf16 v[64:67], a[128:131], a[0:3], v[120:123]\n"
    "ds_read_b64_tr_b16 a[80:81], v10 offset:20480\n"
    "ds_read_b64_tr_b16 a[82:83], v10 offset:22528\n"
    "s_add_u32 m0, s62, 0x4000\n"
    "v_mfma_f32_16x16x32_bf16 v[68:71], a[132:135], a[0:3], v[120:123]\n"
    "ds_read_b64_tr_b16 a[84:85], v11 offset:20480\n"
    "s_add_u32 s63, s63, 0x4000\n"
    "v_mfma_f32_16x16x32_bf16 v[76:79], a[132:135], a[4:7], v[124:127]\n"
    "buffer_load_dwordx4 v18, s[36:39], s63 offen nt lds\n"
    "v_mfma_f32_16x16x32_bf16 v[72:75], a[128:131], a[4:7], v[124:127]\n"
    "v_exp_f32 v48, v88\n"
    "v_mfma_f32_16x16x32_bf16 v[64:67], a[136:139], a[8:11], v[64:67]\n"
    "v_exp_f32 v49, v89\n"
    "v_mfma_f32_16x16x32_bf16 v[68:71], a[140:143], a[8:11], v[68:71]\n"
    "v_exp_f32 v50, v90\n"
    "v_mfma_f32_16x16x32_bf16 v[76:79], a[140:143], a[12:15], v[76:79]\n"
    "v_exp_f32 v51, v91\n"
    "v_mfma_f32_16x16x32_bf16 v[72:75], a[136:139], a[12:15], v[72:75]\n"
    "ds_read_b64_tr_b16 a[86:87], v11 offset:22528\n"
    "v_add_f32 v43, v48, v49\n"
    "v_mfma_f32_16x16x32_bf16 v[64:67], a[144:147], a[16:19], v[64:67]\n"
    "ds_read_b64_tr_b16 a[88:89], v10 offset:20992\n"
    "buffer_load_dwordx4 v19, s[36:39], s63 offen offset:1024 nt lds\n"
    "v_mfma_f32_16x16x32_bf16 v[68:71], a[148:151], a[16:19], v[68:71]\n"
    "v_exp_f32 v52, v92\n"
    "v_mfma_f32_16x16x32_bf16 v[76:79], a[148:151], a[20:23], v[76:79]\n"
    "ds_read_b64_tr_b16 a[90:91], v10 offset:23040\n"
    "v_add_f32 v43, v43, v50\n"
    "v_mfma_f32_16x16x32_bf16 v[72:75], a[144:147], a[20:23], v[72:75]\n"
    "v_exp_f32 v53, v93\n"
    "v_mfma_f32_16x16x32_bf16 v[64:67], a[152:155], a[24:27], v[64:67]\n"
    "ds_read_b64_tr_b16 a[92:93], v11 offset:20992\n"
    "v_add_f32 v43, v43, v51\n"
    "v_mfma_f32_16x16x32_bf16 v[68:71], a[156:159], a[24:27], v[68:71]\n"
    "ds_read_b64_tr_b16 a[94:95], v11 offset:23040\n"
    "v_add_f32 v43, v43, v52\n"
    "v_mfma_f32_16x16x32_bf16 v[76:79], a[156:159], a[28:31], v[76:79]\n"
    "ds_read_b64_tr_b16 a[96:97], v10 offset:24576\n"
    "v_add_f32 v43, v43, v53\n"
    "v_mfma_f32_16x16x32_bf16 v[72:75], a[152:155], a[28:31], v[72:75]\n"
    "v_exp_f32 v54, v94\n"
    "v_mfma_f32_16x16x32_bf16 v[64:67], a[160:163], a[32:35], v[64:67]\n"
    "ds_read_b64_tr_b16 a[98:99], v10 offset:26624\n"
    "buffer_load_dwordx4 v20, s[36:39], s63 offen offset:2048 nt lds\n"
    "v_mfma_f32_16x16x32_bf16 v[68:71], a[164:167], a[32:35], v[68:71]\n"
    "ds_read_b64_tr_b16 a[100:101], v11 offset:24576\n"
    "v_add_f32 v43, v43, v54\n"
    "v_mfma_f32_16x16x32_bf16 v[76:79], a[164:167], a[36:39], v[76:79]\n"
    "v_exp_f32 v55, v95\n"
    "v_mfma_f32_16x16x32_bf16 v[72:75], a[160:163], a[36:39], v[72:75]\n"
    "ds_read_b64_tr_b16 a[102:103], v11 offset:26624\n"
    "v_add_f32 v43, v43, v55\n"
    "v_mfma_f32_16x16x32_bf16 v[64:67], a[168:171], a[40:43], v[64:67]\n"
    "ds_read_b64_tr_b16 a[104:105], v10 offset:25088\n"
    "v_add_f32 v44, v42, v43\n"
    "v_mfma_f32_16x16x32_bf16 v[68:71], a[172:175], a[40:43], v[68:71]\n"
    "ds_read_b64_tr_b16 a[106:107], v10 offset:27136\n"
    "v_cmp_nge_f32 vcc, s66, v44\n"
    "v_mfma_f32_16x16x32_bf16 v[76:79], a[172:175], a[44:47], v[76:79]\n"
    "ds_read_b64_tr_b16 a[108:109], v11 offset:25088\n"
    "v_cvt_pk_bf16_f32 v108, v48, v49\n"
    "v_mfma_f32_16x16x32_bf16 v[72:75], a[168:171], a[44:47], v[72:75]\n"
    "ds_read_b64_tr_b16 a[110:111], v11 offset:27136\n"
    "v_cvt_pk_bf16_f32 v109, v50, v51\n"
    "v_mfma_f32_16x16x32_bf16 v[64:67], a[176:179], a[48:51], v[64:67]\n"
    "ds_read_b64_tr_b16 a[112:113], v10 offset:28672\n"
    "v_cvt_pk_bf16_f32 v110, v52, v53\n"
    "v_mfma_f32_16x16x32_bf16 v[68:71], a[180:183], a[48:51], v[68:71]\n"
    "ds_read_b64_tr_b16 a[114:115], v10 offset:30720\n"
    "buffer_load_dwordx4 v21, s[36:39], s63 offen offset:3072 nt lds\n"
    "v_mfma_f32_16x16x32_bf16 v[76:79], a[180:183], a[52:55], v[76:79]\n"
    "ds_read_b64_tr_b16 a[116:117], v11 offset:28672\n"
    "v_cvt_pk_bf16_f32 v111, v54, v55\n"
    "v_mfma_f32_16x16x32_bf16 v[72:75], a[176:179], a[52:55], v[72:75]\n"
    "ds_read_b64_tr_b16 a[118:119], v11 offset:30720\n"
    "ds_read_b64_tr_b16 a[120:121], v10 offset:29184\n"
    "v_mfma_f32_16x16x32_bf16 v[64:67], a[184:187], a[56:59], v[64:67]\n"
    "ds_read_b64_tr_b16 a[122:123], v10 offset:31232\n"
    "ds_read_b64_tr_b16 a[124:125], v11 offset:29184\n"
    "v_mfma_f32_16x16x32_bf16 v[68:71], a[188:191], a[56:59], v[68:71]\n"
    "ds_read_b64_tr_b16 a[126:127], v11 offset:31232\n"
    "v_mfma_f32_16x16x32_bf16 v[76:79], a[188:191], a[60:63], v[76:79]\n"
    "v_mfma_f32_16x16x32_bf16 v[72:75], a[184:187], a[60:63], v[72:75]\n"
    "s_cbranch_vccnz L_stub4\n"
    "L_ret4:\n"
    "s_waitcnt vmcnt(12) lgkmcnt(6)\n"
    "s_barrier\n"
    "v_mfma_f32_16x16x32_bf16 v[128:131], a[64:67], v[104:107], v[128:131]\n"
    "ds_read_b128 a[128:131], v4 offset:49152\n"
    "s_add_u32 m0, s62, 0x10000\n"
    "v_mfma_f32_16x16x32_bf16 v[136:139], a[64:67], v[108:111], v[136:139]\n"
    "ds_read_b128 a[132:135], v4 offset:51200\n"
    "buffer_load_dwordx4 v22, s[40:43], s63 offen nt lds\n"
    "v_mfma_f32_16x16x32_bf16 v[140:143], a[68:71], v[108:111], v[140:143]\n"
    "ds_read_b128 a[136:139], v5 offset:49152\n"
    "v_add_f32 v40, v40, v42\n"
    "v_mfma_f32_16x16x32_bf16 v[132:135], a[68:71], v[104:107], v[132:135]\n"
    "v_exp_f32 v48, v64\n"
    "v_mfma_f32_16x16x32_bf16 v[144:147], a[72:75], v[104:107], v[144:147]\n"
    "v_exp_f32 v49, v65\n"
    "v_mfma_f32_16x16x32_bf16 v[152:155], a[72:75], v[108:111], v[152:155]\n"
    "ds_read_b128 a[140:143], v5 offset:51200\n"
    "v_cvt_pk_bf16_f32 v96, v48, v49\n"
    "v_mfma_f32_16x16x32_bf16 v[156:159], a[76:79], v[108:111], v[156:159]\n"
    "ds_read_b128 a[144:147], v4 offset:53248\n"
    "v_exp_f32 v50, v66\n"
    "v_mfma_f32_16x16x32_bf16 v[148:151], a[76:79], v[104:107], v[148:151]\n"
    "v_exp_f32 v51, v67\n"
    "v_mfma_f32_16x16x32_bf16 v[160:163], a[80:83], v[104:107], v[160:163]\n"
    "ds_read_b128 a[148:151], v4 offset:55296\n"
    "v_cvt_pk_bf16_f32 v97, v50, v51\n"
    "v_mfma_f32_16x16x32_bf16 v[168:171], a[80:83], v[108:111], v[168:171]\n"
    "v_exp_f32 v52, v68\n"
    "v_mfma_f32_16x16x32_bf16 v[172:175], a[84:87], v[108:111], v[172:175]\n"
    "buffer_load_dwordx4 v23, s[40:43], s63 offen offset:1024 nt lds\n"
    "v_mfma_f32_16x16x32_bf16 v[164:167], a[84:87], v[104:107], v[164:167]\n"
    "ds_read_b128 a[152:155], v5 offset:53248\n"
    "v_add_f32 v41, v41, v43\n"
    "v_mfma_f32_16x16x32_bf16 v[176:179], a[88:91], v[104:107], v[176:179]\n"
    "ds_read_b128 a[156:159], v5 offset:55296\n"
    "v_add_f32 v42, v48, v49\n"
    "v_mfma_f32_16x16x32_bf16 v[184:187], a[88:91], v[108:111], v[184:187]\n"
    "v_exp_f32 v53, v69\n"
    "v_mfma_f32_16x16x32_bf16 v[188:191], a[92:95], v[108:111], v[188:191]\n"
    "ds_read_b128 a[160:163], v4 offset:57344\n"
    "v_cvt_pk_bf16_f32 v98, v52, v53\n"
    "v_mfma_f32_16x16x32_bf16 v[180:183], a[92:95], v[104:107], v[180:183]\n"
    "ds_read_b128 a[164:167], v4 offset:59392\n"
    "v_add_f32 v42, v42, v50\n"
    "v_mfma_f32_16x16x32_bf16 v[192:195], a[96:99], v[104:107], v[192:195]\n"
    "ds_read_b128 a[168:171], v5 offset:57344\n"
    "v_add_f32 v42, v42, v51\n"
    "v_mfma_f32_16x16x32_bf16 v[200:203], a[96:99], v[108:111], v[200:203]\n"
    "buffer_load_dwordx4 v24, s[40:43], s63 offen offset:2048 nt lds\n"
    "v_mfma_f32_16x16x32_bf16 v[204:207], a[100:103], v[108:111], v[204:207]\n"
    "v_exp_f32 v54, v70\n"
    "v_mfma_f32_16x16x32_bf16 v[196:199], a[100:103], v[104:107], v[196:199]\n"
    "ds_read_b128 a[172:175], v5 offset:59392\n"
    "v_add_f32 v42, v42, v52\n"
    "v_mfma_f32_16x16x32_bf16 v[208:211], a[104:107], v[104:107], v[208:211]\n"
    "ds_read_b128 a[176:179], v4 offset:61440\n"
    "v_add_f32 v42, v42, v53\n"
    "v_mfma_f32_16x16x32_bf16 v[216:219], a[104:107], v[108:111], v[216:219]\n"
    "v_exp_f32 v55, v71\n"
    "v_mfma_f32_16x16x32_bf16 v[220:223], a[108:111], v[108:111], v[220:223]\n"
    "ds_read_b128 a[180:183], v4 offset:63488\n"
    "v_cvt_pk_bf16_f32 v99, v54, v55\n"
    "v_mfma_f32_16x16x32_bf16 v[212:215], a[108:111], v[104:107], v[212:215]\n"
    "ds_read_b128 a[184:187], v5 offset:61440\n"
    "v_add_f32 v42, v42, v54\n"
    "v_mfma_f32_16x16x32_bf16 v[224:227], a[112:115], v[104:107], v[224:227]\n"
    "ds_read_b128 a[188:191], v5 offset:63488\n"
    "v_add_f32 v42, v42, v55\n"
    "v_mfma_f32_16x16x32_bf16 v[232:235], a[112:115], v[108:111], v[232:235]\n"
    "ds_read_b64_tr_b16 a[64:65], v10 offset:32768\n"
    "s_waitcnt lgkmcnt(15)\n"
    "v_mfma_f32_16x16x32_bf16 v[236:239], a[116:119], v[108:111], v[236:239]\n"
    "ds_read_b64_tr_b16 a[66:67], v10 offset:34816\n"
    "v_mfma_f32_16x16x32_bf16 v[228:231], a[116:119], v[104:107], v[228:231]\n"
    "ds_read_b64_tr_b16 a[68:69], v11 offset:32768\n"
    "v_mfma_f32_16x16x32_bf16 v[240:243], a[120:123], v[104:107], v[240:243]\n"
    "buffer_load_dwordx4 v25, s[40:43], s63 offen offset:3072 nt lds\n"
    "v_mfma_f32_16x16x32_bf16 v[248:251], a[120:123], v[108:111], v[248:251]\n"
    "ds_read_b64_tr_b16 a[70:71], v11 offset:34816\n"
    "ds_read_b64_tr_b16 a[72:73], v10 offset:33280\n"
    "v_mfma_f32_16x16x32_bf16 v[252:255], a[124:127], v[108:111], v[252:255]\n"
    "ds_read_b64_tr_b16 a[74:75], v10 offset:35328\n"
    "ds_read_b64_tr_b16 a[76:77], v11 offset:33280\n"
    "v_mfma_f32_16x16x32_bf16 v[244:247], a[124:127], v[104:107], v[244:247]\n"
    "ds_read_b64_tr_b16 a[78:79], v11 offset:35328\n"
    "s_waitcnt lgkmcnt(8)\n"
    "v_mfma_f32_16x16x32_bf16 v[80:83], a[128:131], a[0:3], v[120:123]\n"
    "ds_read_b64_tr_b16 a[80:81], v10 offset:36864\n"
    "ds_read_b64_tr_b16 a[82:83], v10 offset:38912\n"
    "s_add_u32 m0, s62, 0x8000\n"
    "v_mfma_f32_16x16x32_bf16 v[84:87], a[132:135], a[0:3], v[120:123]\n"
    "ds_read_b64_tr_b16 a[84:85], v11 offset:36864\n"
    "s_add_u32 s63, s63, 0x4000\n"
    "v_mfma_f32_16x16x32_bf16 v[92:95], a[132:135], a[4:7], v[124:127]\n"
    "buffer_load_dwordx4 v18, s[36:39], s63 offen nt lds\n"
    "v_mfma_f32_16x16x32_bf16 v[88:91], a[128:131], a[4:7], v[124:127]\n"
    "v_exp_f32 v48, v72\n"
    "v_mfma_f32_16x16x32_bf16 v[80:83], a[136:139], a[8:11], v[80:83]\n"
    "v_exp_f32 v49, v73\n"
    "v_mfma_f32_16x16x32_bf16 v[84:87], a[140:143], a[8:11], v[84:87]\n"
    "v_exp_f32 v50, v74\n"
    "v_mfma_f32_16x16x32_bf16 v[92:95], a[140:143], a[12:15], v[92:95]\n"
    "v_exp_f32 v51, v75\n"
    "v_mfma_f32_16x16x32_bf16 v[88:91], a[136:139], a[12:15], v[88:91]\n"
    "ds_read_b64_tr_b16 a[86:87], v11 offset:38912\n"
    "v_add_f32 v43, v48, v49\n"
    "v_mfma_f32_16x16x32_bf16 v[80:83], a[144:147], a[16:19], v[80:83]\n"
    "ds_read_b64_tr_b16 a[88:89], v10 offset:37376\n"
    "buffer_load_dwordx4 v19, s[36:39], s63 offen offset:1024 nt lds\n"
    "v_mfma_f32_16x16x32_bf16 v[84:87], a[148:151], a[16:19], v[84:87]\n"
    "v_exp_f32 v52, v76\n"
    "v_mfma_f32_16x16x32_bf16 v[92:95], a[148:151], a[20:23], v[92:95]\n"
    "ds_read_b64_tr_b16 a[90:91], v10 offset:39424\n"
    "v_add_f32 v43, v43, v50\n"
    "v_mfma_f32_16x16x32_bf16 v[88:91], a[144:147], a[20:23], v[88:91]\n"
    "v_exp_f32 v53, v77\n"
    "v_mfma_f32_16x16x32_bf16 v[80:83], a[152:155], a[24:27], v[80:83]\n"
    "ds_read_b64_tr_b16 a[92:93], v11 offset:37376\n"
    "v_add_f32 v43, v43, v51\n"
    "v_mfma_f32_16x16x32_bf16 v[84:87], a[156:159], a[24:27], v[84:87]\n"
    "ds_read_b64_tr_b16 a[94:95], v11 offset:39424\n"
    "v_add_f32 v43, v43, v52\n"
    "v_mfma_f32_16x16x32_bf16 v[92:95], a[156:159], a[28:31], v[92:95]\n"
    "ds_read_b64_tr_b16 a[96:97], v10 offset:40960\n"
    "v_add_f32 v43, v43, v53\n"
    "v_mfma_f32_16x16x32_bf16 v[88:91], a[152:155], a[28:31], v[88:91]\n"
    "v_exp_f32 v54, v78\n"
    "v_mfma_f32_16x16x32_bf16 v[80:83], a[160:163], a[32:35], v[80:83]\n"
    "ds_read_b64_tr_b16 a[98:99], v10 offset:43008\n"
    "buffer_load_dwordx4 v20, s[36:39], s63 offen offset:2048 nt lds\n"
    "v_mfma_f32_16x16x32_bf16 v[84:87], a[164:167], a[32:35], v[84:87]\n"
    "ds_read_b64_tr_b16 a[100:101], v11 offset:40960\n"
    "v_add_f32 v43, v43, v54\n"
    "v_mfma_f32_16x16x32_bf16 v[92:95], a[164:167], a[36:39], v[92:95]\n"
    "v_exp_f32 v55, v79\n"
    "v_mfma_f32_16x16x32_bf16 v[88:91], a[160:163], a[36:39], v[88:91]\n"
    "ds_read_b64_tr_b16 a[102:103], v11 offset:43008\n"
    "v_add_f32 v43, v43, v55\n"
    "v_mfma_f32_16x16x32_bf16 v[80:83], a[168:171], a[40:43], v[80:83]\n"
    "ds_read_b64_tr_b16 a[104:105], v10 offset:41472\n"
    "v_add_f32 v44, v42, v43\n"
    "v_mfma_f32_16x16x32_bf16 v[84:87], a[172:175], a[40:43], v[84:87]\n"
    "ds_read_b64_tr_b16 a[106:107], v10 offset:43520\n"
    "v_cmp_nge_f32 vcc, s66, v44\n"
    "v_mfma_f32_16x16x32_bf16 v[92:95], a[172:175], a[44:47], v[92:95]\n"
    "ds_read_b64_tr_b16 a[108:109], v11 offset:41472\n"
    "v_cvt_pk_bf16_f32 v100, v48, v49\n"
    "v_mfma_f32_16x16x32_bf16 v[88:91], a[168:171], a[44:47], v[88:91]\n"
    "ds_read_b64_tr_b16 a[110:111], v11 offset:43520\n"
    "v_cvt_pk_bf16_f32 v101, v50, v51\n"
    "v_mfma_f32_16x16x32_bf16 v[80:83], a[176:179], a[48:51], v[80:83]\n"
    "ds_read_b64_tr_b16 a[112:113], v10 offset:45056\n"
    "v_cvt_pk_bf16_f32 v102, v52, v53\n"
    "v_mfma_f32_16x16x32_bf16 v[84:87], a[180:183], a[48:51], v[84:87]\n"
    "ds_read_b64_tr_b16 a[114:115], v10 offset:47104\n"
    "buffer_load_dwordx4 v21, s[36:39], s63 offen offset:3072 nt lds\n"
    "v_mfma_f32_16x16x32_bf16 v[92:95], a[180:183], a[52:55], v[92:95]\n"
    "ds_read_b64_tr_b16 a[116:117], v11 offset:45056\n"
    "v_cvt_pk_bf16_f32 v103, v54, v55\n"
    "v_mfma_f32_16x16x32_bf16 v[88:91], a[176:179], a[52:55], v[88:91]\n"
    "ds_read_b64_tr_b16 a[118:119], v11 offset:47104\n"
    "ds_read_b64_tr_b16 a[120:121], v10 offset:45568\n"
    "v_mfma_f32_16x16x32_bf16 v[80:83], a[184:187], a[56:59], v[80:83]\n"
    "ds_read_b64_tr_b16 a[122:123], v10 offset:47616\n"
    "ds_read_b64_tr_b16 a[124:125], v11 offset:45568\n"
    "v_mfma_f32_16x16x32_bf16 v[84:87], a[188:191], a[56:59], v[84:87]\n"
    "ds_read_b64_tr_b16 a[126:127], v11 offset:47616\n"
    "v_mfma_f32_16x16x32_bf16 v[92:95], a[188:191], a[60:63], v[92:95]\n"
    "v_mfma_f32_16x16x32_bf16 v[88:91], a[184:187], a[60:63], v[88:91]\n"
    "s_cbranch_vccnz L_stub5\n"
    "L_ret5:\n"
    "s_waitcnt vmcnt(12) lgkmcnt(6)\n"
    "s_barrier\n"
    "v_mfma_f32_16x16x32_bf16 v[128:131], a[64:67], v[96:99], v[128:131]\n"
    "ds_read_b128 a[128:131], v4 offset:0\n"
    "s_add_u32 m0, s62, 0x14000\n"
    "v_mfma_f32_16x16x32_bf16 v[136:139], a[64:67], v[100:103], v[136:139]\n"
    "ds_read_b128 a[132:135], v4 offset:2048\n"
    "buffer_load_dwordx4 v22, s[40:43], s63 offen nt lds\n"
    "v_mfma_f32_16x16x32_bf16 v[140:143], a[68:71], v[100:103], v[140:143]\n"
    "ds_read_b128 a[136:139], v5 offset:0\n"
    "v_add_f32 v40, v40, v42\n"
    "v_mfma_f32_16x16x32_bf16 v[132:135], a[68:71], v[96:99], v[132:135]\n"
    "v_exp_f32 v48, v80\n"
    "v_mfma_f32_16x16x32_bf16 v[144:147], a[72:75], v[96:99], v[144:147]\n"
    "v_exp_f32 v49, v81\n"
    "v_mfma_f32_16x16x32_bf16 v[152:155], a[72:75], v[100:103], v[152:155]\n"
    "ds_read_b128 a[140:143], v5 offset:2048\n"
    "v_cvt_pk_bf16_f32 v104, v48, v49\n"
    "v_mfma_f32_16x16x32_bf16 v[156:159], a[76:79], v[100:103], v[156:159]\n"
    "ds_read_b128 a[144:147], v4 offset:4096\n"
    "v_exp_f32 v50, v82\n"
    "v_mfma_f32_16x16x32_bf16 v[148:151], a[76:79], v[96:99], v[148:151]\n"
    "v_exp_f32 v51, v83\n"
    "v_mfma_f32_16x16x32_bf16 v[160:163], a[80:83], v[96:99], v[160:163]\n"
    "ds_read_b128 a[148:151], v4 offset:6144\n"
    "v_cvt_pk_bf16_f32 v105, v50, v51\n"
    "v_mfma_f32_16x16x32_bf16 v[168:171], a[80:83], v[100:103], v[168:171]\n"
    "v_exp_f32 v52, v84\n"
    "v_mfma_f32_16x16x32_bf16 v[172:175], a[84:87], v[100:103], v[172:175]\n"
    "buffer_load_dwordx4 v23, s[40:43], s63 offen offset:1024 nt lds\n"
    "v_mfma_f32_16x16x32_bf16 v[164:167], a[84:87], v[96:99], v[164:167]\n"
    "ds_read_b128 a[152:155], v5 offset:4096\n"
    "v_add_f32 v41, v41, v43\n"
    "v_mfma_f32_16x16x32_bf16 v[176:179], a[88:91], v[96:99], v[176:179]\n"
    "ds_read_b128 a[156:159], v5 offset:6144\n"
    "v_add_f32 v42, v48, v49\n"
    "v_mfma_f32_16x16x32_bf16 v[184:187], a[88:91], v[100:103], v[184:187]\n"
    "v_exp_f32 v53, v85\n"
    "v_mfma_f32_16x16x32_bf16 v[188:191], a[92:95], v[100:103], v[188:191]\n"
    "ds_read_b128 a[160:163], v4 offset:8192\n"
    "v_cvt_pk_bf16_f32 v106, v52, v53\n"
    "v_mfma_f32_16x16x32_bf16 v[180:183], a[92:95], v[96:99], v[180:183]\n"
    "ds_read_b128 a[164:167], v4 offset:10240\n"
    "v_add_f32 v42, v42, v50\n"
    "v_mfma_f32_16x16x32_bf16 v[192:195], a[96:99], v[96:99], v[192:195]\n"
    "ds_read_b128 a[168:171], v5 offset:8192\n"
    "v_add_f32 v42, v42, v51\n"
    "v_mfma_f32_16x16x32_bf16 v[200:203], a[96:99], v[100:103], v[200:203]\n"
    "buffer_load_dwordx4 v24, s[40:43], s63 offen offset:2048 nt lds\n"
    "v_mfma_f32_16x16x32_bf16 v[204:207], a[100:103], v[100:103], v[204:207]\n"
    "v_exp_f32 v54, v86\n"
    "v_mfma_f32_16x16x32_bf16 v[196:199], a[100:103], v[96:99], v[196:199]\n"
    "ds_read_b128 a[172:175], v5 offset:10240\n"
    "v_add_f32 v42, v42, v52\n"
    "v_mfma_f32_16x16x32_bf16 v[208:211], a[104:107], v[96:99], v[208:211]\n"
    "ds_read_b128 a[176:179], v4 offset:12288\n"
    "v_add_f32 v42, v42, v53\n"
    "v_mfma_f32_16x16x32_bf16 v[216:219], a[104:107], v[100:103], v[216:219]\n"
    "v_exp_f32 v55, v87\n"
    "v_mfma_f32_16x16x32_bf16 v[220:223], a[108:111], v[100:103], v[220:223]\n"
    "ds_read_b128 a[180:183], v4 offset:14336\n"
    "v_cvt_pk_bf16_f32 v107, v54, v55\n"
    "v_mfma_f32_16x16x32_bf16 v[212:215], a[108:111], v[96:99], v[212:215]\n"
    "ds_read_b128 a[184:187], v5 offset:12288\n"
    "v_add_f32 v42, v42, v54\n"
    "v_mfma_f32_16x16x32_bf16 v[224:227], a[112:115], v[96:99], v[224:227]\n"
    "ds_read_b128 a[188:191], v5 offset:14336\n"
    "v_add_f32 v42, v42, v55\n"
    "v_mfma_f32_16x16x32_bf16 v[232:235], a[112:115], v[100:103], v[232:235]\n"
    "ds_read_b64_tr_b16 a[64:65], v10 offset:49152\n"
    "s_waitcnt lgkmcnt(15)\n"
    "v_mfma_f32_16x16x32_bf16 v[236:239], a[116:119], v[100:103], v[236:239]\n"
    "ds_read_b64_tr_b16 a[66:67], v10 offset:51200\n"
    "v_mfma_f32_16x16x32_bf16 v[228:231], a[116:119], v[96:99], v[228:231]\n"
    "ds_read_b64_tr_b16 a[68:69], v11 offset:49152\n"
    "v_mfma_f32_16x16x32_bf16 v[240:243], a[120:123], v[96:99], v[240:243]\n"
    "buffer_load_dwordx4 v25, s[40:43], s63 offen offset:3072 nt lds\n"
    "v_mfma_f32_16x16x32_bf16 v[248:251], a[120:123], v[100:103], v[248:251]\n"
    "ds_read_b64_tr_b16 a[70:71], v11 offset:51200\n"
    "ds_read_b64_tr_b16 a[72:73], v10 offset:49664\n"
    "v_mfma_f32_16x16x32_bf16 v[252:255], a[124:127], v[100:103], v[252:255]\n"
    "ds_read_b64_tr_b16 a[74:75], v10 offset:51712\n"
    "ds_read_b64_tr_b16 a[76:77], v11 offset:49664\n"
    "v_mfma_f32_16x16x32_bf16 v[244:247], a[124:127], v[96:99], v[244:247]\n"
    "ds_read_b64_tr_b16 a[78:79], v11 offset:51712\n"
    "s_waitcnt lgkmcnt(8)\n"
    "s_sub_u32 s64, s64, 1\n"
    "s_cmp_lg_u32 s64, 0\n"
    "s_cbranch_scc1 L_loop\n"
    "v_mfma_f32_16x16x32_bf16 v[64:67], a[128:131], a[0:3], v[120:123]\n"
    "ds_read_b64_tr_b16 a[80:81], v10 offset:53248\n"
    "ds_read_b64_tr_b16 a[82:83], v10 offset:55296\n"
    "s_add_u32 m0, s62, 0xc000\n"
    "v_mfma_f32_16x16x32_bf16 v[68:71], a[132:135], a[0:3], v[120:123]\n"
    "ds_read_b64_tr_b16 a[84:85], v11 offset:53248\n"
    "s_add_u32 s63, s63, 0x4000\n"
    "v_mfma_f32_16x16x32_bf16 v[76:79], a[132:135], a[4:7], v[124:127]\n"
    "buffer_load_dwordx4 v18, s[36:39], s63 offen nt lds\n"
    "v_mfma_f32_16x16x32_bf16 v[72:75], a[128:131], a[4:7], v[124:127]\n"
    "v_exp_f32 v48, v88\n"
    "v_mfma_f32_16x16x32_bf16 v[64:67], a[136:139], a[8:11], v[64:67]\n"
    "v_exp_f32 v49, v89\n"
    "v_mfma_f32_16x16x32_bf16 v[68:71], a[140:143], a[8:11], v[68:71]\n"
    "v_exp_f32 v50, v90\n"
    "v_mfma_f32_16x16x32_bf16 v[76:79], a[140:143], a[12:15], v[76:79]\n"
    "v_exp_f32 v51, v91\n"
    "v_mfma_f32_16x16x32_bf16 v[72:75], a[136:139], a[12:15], v[72:75]\n"
    "ds_read_b64_tr_b16 a[86:87], v11 offset:55296\n"
    "v_add_f32 v43, v48, v49\n"
    "v_mfma_f32_16x16x32_bf16 v[64:67], a[144:147], a[16:19], v[64:67]\n"
    "ds_read_b64_tr_b16 a[88:89], v10 offset:53760\n"
    "buffer_load_dwordx4 v19, s[36:39], s63 offen offset:1024 nt lds\n"
    "v_mfma_f32_16x16x32_bf16 v[68:71], a[148:151], a[16:19], v[68:71]\n"
    "v_exp_f32 v52, v92\n"
    "v_mfma_f32_16x16x32_bf16 v[76:79], a[148:151], a[20:23], v[76:79]\n"
    "ds_read_b64_tr_b16 a[90:91], v10 offset:55808\n"
    "v_add_f32 v43, v43, v50\n"
    "v_mfma_f32_16x16x32_bf16 v[72:75], a[144:147], a[20:23], v[72:75]\n"
    "v_exp_f32 v53, v93\n"
    "v_mfma_f32_16x16x32_bf16 v[64:67], a[152:155], a[24:27], v[64:67]\n"
    "ds_read_b64_tr_b16 a[92:93], v11 offset:53760\n"
    "v_add_f32 v43, v43, v51\n"
    "v_mfma_f32_16x16x32_bf16 v[68:71], a[156:159], a[24:27], v[68:71]\n"
    "ds_read_b64_tr_b16 a[94:95], v11 offset:55808\n"
    "v_add_f32 v43, v43, v52\n"
    "v_mfma_f32_16x16x32_bf16 v[76:79], a[156:159], a[28:31], v[76:79]\n"
    "ds_read_b64_tr_b16 a[96:97], v10 offset:57344\n"
    "v_add_f32 v43, v43, v53\n"
    "v_mfma_f32_16x16x32_bf16 v[72:75], a[152:155], a[28:31], v[72:75]\n"
    "v_exp_f32 v54, v94\n"
    "v_mfma_f32_16x16x32_bf16 v[64:67], a[160:163], a[32:35], v[64:67]\n"
    "ds_read_b64_tr_b16 a[98:99], v10 offset:59392\n"
    "buffer_load_dwordx4 v20, s[36:39], s63 offen offset:2048 nt lds\n"
    "v_mfma_f32_16x16x32_bf16 v[68:71], a[164:167], a[32:35], v[68:71]\n"
    "ds_read_b64_tr_b16 a[100:101], v11 offset:57344\n"
    "v_add_f32 v43, v43, v54\n"
    "v_mfma_f32_16x16x32_bf16 v[76:79], a[164:167], a[36:39], v[76:79]\n"
    "v_exp_f32 v55, v95\n"
    "v_mfma_f32_16x16x32_bf16 v[72:75], a[160:163], a[36:39], v[72:75]\n"
    "ds_read_b64_tr_b16 a[102:103], v11 offset:59392\n"
    "v_add_f32 v43, v43, v55\n"
    "v_mfma_f32_16x16x32_bf16 v[64:67], a[168:171], a[40:43], v[64:67]\n"
    "ds_read_b64_tr_b16 a[104:105], v10 offset:57856\n"
    "v_add_f32 v44, v42, v43\n"
    "v_mfma_f32_16x16x32_bf16 v[68:71], a[172:175], a[40:43], v[68:71]\n"
    "ds_read_b64_tr_b16 a[106:107], v10 offset:59904\n"
    "v_cmp_nge_f32 vcc, s66, v44\n"
    "v_mfma_f32_16x16x32_bf16 v[76:79], a[172:175], a[44:47], v[76:79]\n"
    "ds_read_b64_tr_b16 a[108:109], v11 offset:57856\n"
    "v_cvt_pk_bf16_f32 v108, v48, v49\n"
    "v_mfma_f32_16x16x32_bf16 v[72:75], a[168:171], a[44:47], v[72:75]\n"
    "ds_read_b64_tr_b16 a[110:111], v11 offset:59904\n"
    "v_cvt_pk_bf16_f32 v109, v50, v51\n"
    "v_mfma_f32_16x16x32_bf16 v[64:67], a[176:179], a[48:51], v[64:67]\n"
    "ds_read_b64_tr_b16 a[112:113], v10 offset:61440\n"
    "v_cvt_pk_bf16_f32 v110, v52, v53\n"
    "v_mfma_f32_16x16x32_bf16 v[68:71], a[180:183], a[48:51], v[68:71]\n"
    "ds_read_b64_tr_b16 a[114:115], v10 offset:63488\n"
    "buffer_load_dwordx4 v21, s[36:39], s63 offen offset:3072 nt lds\n"
    "v_mfma_f32_16x16x32_bf16 v[76:79], a[180:183], a[52:55], v[76:79]\n"
    "ds_read_b64_tr_b16 a[116:117], v11 offset:61440\n"
    "v_cvt_pk_bf16_f32 v111, v54, v55\n"
    "v_mfma_f32_16x16x32_bf16 v[72:75], a[176:179], a[52:55], v[72:75]\n"
    "ds_read_b64_tr_b16 a[118:119], v11 offset:63488\n"
    "ds_read_b64_tr_b16 a[120:121], v10 offset:61952\n"
    "v_mfma_f32_16x16x32_bf16 v[64:67], a[184:187], a[56:59], v[64:67]\n"
    "ds_read_b64_tr_b16 a[122:123], v10 offset:64000\n"
    "ds_read_b64_tr_b16 a[124:125], v11 offset:61952\n"
    "v_mfma_f32_16x16x32_bf16 v[68:71], a[188:191], a[56:59], v[68:71]\n"
    "ds_read_b64_tr_b16 a[126:127], v11 offset:64000\n"
    "v_mfma_f32_16x16x32_bf16 v[76:79], a[188:191], a[60:63], v[76:79]\n"
    "v_mfma_f32_16x16x32_bf16 v[72:75], a[184:187], a[60:63], v[72:75]\n"
    "s_cbranch_vccnz L_stub6\n"
    "L_ret6:\n"
    "s_waitcnt vmcnt(12) lgkmcnt(6)\n"
    "s_barrier\n"
    "v_mfma_f32_16x16x32_bf16 v[128:131], a[64:67], v[104:107], v[128:131]\n"
    "ds_read_b128 a[128:131], v4 offset:16384\n"
    "s_add_u32 m0, s62, 0x18000\n"
    "v_mfma_f32_16x16x32_bf16 v[136:139], a[64:67], v[108:111], v[136:139]\n"
    "ds_read_b128 a[132:135], v4 offset:18432\n"
    "buffer_load_dwordx4 v22, s[40:43], s63 offen nt lds\n"
    "v_mfma_f32_16x16x32_bf16 v[140:143], a[68:71], v[108:111], v[140:143]\n"
    "ds_read_b128 a[136:139], v5 offset:16384\n"
    "v_add_f32 v40, v40, v42\n"
    "v_mfma_f32_16x16x32_bf16 v[132:135], a[68:71], v[104:107], v[132:135]\n"
    "v_exp_f32 v48, v64\n"
    "v_mfma_f32_16x16x32_bf16 v[144:147], a[72:75], v[104:107], v[144:147]\n"
    "v_exp_f32 v49, v65\n"
    "v_mfma_f32_16x16x32_bf16 v[152:155], a[72:75], v[108:111], v[152:155]\n"
    "ds_read_b128 a[140:143], v5 offset:18432\n"
    "v_cvt_pk_bf16_f32 v96, v48, v49\n"
    "v_mfma_f32_16x16x32_bf16 v[156:159], a[76:79], v[108:111], v[156:159]\n"
    "ds_read_b128 a[144:147], v4 offset:20480\n"
    "v_exp_f32 v50, v66\n"
    "v_mfma_f32_16x16x32_bf16 v[148:151], a[76:79], v[104:107], v[148:151]\n"
    "v_exp_f32 v51, v67\n"
    "v_mfma_f32_16x16x32_bf16 v[160:163], a[80:83], v[104:107], v[160:163]\n"
    "ds_read_b128 a[148:151], v4 offset:22528\n"
    "v_cvt_pk_bf16_f32 v97, v50, v51\n"
    "v_mfma_f32_16x16x32_bf16 v[168:171], a[80:83], v[108:111], v[168:171]\n"
    "v_exp_f32 v52, v68\n"
    "v_mfma_f32_16x16x32_bf16 v[172:175], a[84:87], v[108:111], v[172:175]\n"
    "buffer_load_dwordx4 v23, s[40:43], s63 offen offset:1024 nt lds\n"
    "v_mfma_f32_16x16x32_bf16 v[164:167], a[84:87], v[104:107], v[164:167]\n"
    "ds_read_b128 a[152:155], v5 offset:20480\n"
    "v_add_f32 v41, v41, v43\n"
    "v_mfma_f32_16x16x32_bf16 v[176:179], a[88:91], v[104:107], v[176:179]\n"
    "ds_read_b128 a[156:159], v5 offset:22528\n"
    "v_add_f32 v42, v48, v49\n"
    "v_mfma_f32_16x16x32_bf16 v[184:187], a[88:91], v[108:111], v[184:187]\n"
    "v_exp_f32 v53, v69\n"
    "v_mfma_f32_16x16x32_bf16 v[188:191], a[92:95], v[108:111], v[188:191]\n"
    "ds_read_b128 a[160:163], v4 offset:24576\n"
    "v_cvt_pk_bf16_f32 v98, v52, v53\n"
    "v_mfma_f32_16x16x32_bf16 v[180:183], a[92:95], v[104:107], v[180:183]\n"
    "ds_read_b128 a[164:167], v4 offset:26624\n"
    "v_add_f32 v42, v42, v50\n"
    "v_mfma_f32_16x16x32_bf16 v[192:195], a[96:99], v[104:107], v[192:195]\n"
    "ds_read_b128 a[168:171], v5 offset:24576\n"
    "v_add_f32 v42, v42, v51\n"
    "v_mfma_f32_16x16x32_bf16 v[200:203], a[96:99], v[108:111], v[200:203]\n"
    "buffer_load_dwordx4 v24, s[40:43], s63 offen offset:2048 nt lds\n"
    "v_mfma_f32_16x16x32_bf16 v[204:207], a[100:103], v[108:111], v[204:207]\n"
    "v_exp_f32 v54, v70\n"
    "v_mfma_f32_16x16x32_bf16 v[196:199], a[100:103], v[104:107], v[196:199]\n"
    "ds_read_b128 a[172:175], v5 offset:26624\n"
    "v_add_f32 v42, v42, v52\n"
    "v_mfma_f32_16x16x32_bf16 v[208:211], a[104:107], v[104:107], v[208:211]\n"
    "ds_read_b128 a[176:179], v4 offset:28672\n"
    "v_add_f32 v42, v42, v53\n"
    "v_mfma_f32_16x16x32_bf16 v[216:219], a[104:107], v[108:111], v[216:219]\n"
    "v_exp_f32 v55, v71\n"
    "v_mfma_f32_16x16x32_bf16 v[220:223], a[108:111], v[108:111], v[220:223]\n"
    "ds_read_b128 a[180:183], v4 offset:30720\n"
    "v_cvt_pk_bf16_f32 v99, v54, v55\n"
    "v_mfma_f32_16x16x32_bf16 v[212:215], a[108:111], v[104:107], v[212:215]\n"
    "ds_read_b128 a[184:187], v5 offset:28672\n"
    "v_add_f32 v42, v42, v54\n"
    "v_mfma_f32_16x16x32_bf16 v[224:227], a[112:115], v[104:107], v[224:227]\n"
    "ds_read_b128 a[188:191], v5 offset:30720\n"
    "v_add_f32 v42, v42, v55\n"
    "v_mfma_f32_16x16x32_bf16 v[232:235], a[112:115], v[108:111], v[232:235]\n"
    "ds_read_b64_tr_b16 a[64:65], v10 offset:0\n"
    "s_waitcnt lgkmcnt(15)\n"
    "v_mfma_f32_16x16x32_bf16 v[236:239], a[116:119], v[108:111], v[236:239]\n"
    "ds_read_b64_tr_b16 a[66:67], v10 offset:2048\n"
    "v_mfma_f32_16x16x32_bf16 v[228:231], a[116:119], v[104:107], v[228:231]\n"
    "ds_read_b64_tr_b16 a[68:69], v11 offset:0\n"
    "v_mfma_f32_16x16x32_bf16 v[240:243], a[120:123], v[104:107], v[240:243]\n"
    "buffer_load_dwordx4 v25, s[40:43], s63 offen offset:3072 nt lds\n"
    "v_mfma_f32_16x16x32_bf16 v[248:251], a[120:123], v[108:111], v[248:251]\n"
    "ds_read_b64_tr_b16 a[70:71], v11 offset:2048\n"
    "ds_read_b64_tr_b16 a[72:73], v10 offset:512\n"
    "v_mfma_f32_16x16x32_bf16 v[252:255], a[124:127], v[108:111], v[252:255]\n"
    "ds_read_b64_tr_b16 a[74:75], v10 offset:2560\n"
    "ds_read_b64_tr_b16 a[76:77], v11 offset:512\n"
    "v_mfma_f32_16x16x32_bf16 v[244:247], a[124:127], v[104:107], v[244:247]\n"
    "ds_read_b64_tr_b16 a[78:79], v11 offset:2560\n"
    "s_waitcnt lgkmcnt(8)\n"
    "v_mfma_f32_16x16x32_bf16 v[80:83], a[128:131], a[0:3], v[120:123]\n"
    "ds_read_b64_tr_b16 a[80:81], v10 offset:4096\n"
    "v_exp_f32 v48, v72\n"
    "v_mfma_f32_16x16x32_bf16 v[84:87], a[132:135], a[0:3], v[120:123]\n"
    "ds_read_b64_tr_b16 a[82:83], v10 offset:6144\n"
    "s_add_u32 m0, s62, 0x24000\n"
    "v_mfma_f32_16x16x32_bf16 v[92:95], a[132:135], a[4:7], v[124:127]\n"
    "buffer_load_dwordx4 v34, s[52:55], s11 offen lds\n"
    "v_mfma_f32_16x16x32_bf16 v[88:91], a[128:131], a[4:7], v[124:127]\n"
    "ds_read_b64_tr_b16 a[84:85], v11 offset:4096\n"
    "v_exp_f32 v49, v73\n"
    "v_mfma_f32_16x16x32_bf16 v[80:83], a[136:139], a[8:11], v[80:83]\n"
    "v_exp_f32 v50, v74\n"
    "v_mfma_f32_16x16x32_bf16 v[84:87], a[140:143], a[8:11], v[84:87]\n"
    "ds_read_b64_tr_b16 a[86:87], v11 offset:6144\n"
    "v_add_f32 v43, v48, v49\n"
    "v_mfma_f32_16x16x32_bf16 v[92:95], a[140:143], a[12:15], v[92:95]\n"
    "v_exp_f32 v51, v75\n"
    "v_mfma_f32_16x16x32_bf16 v[88:91], a[136:139], a[12:15], v[88:91]\n"
    "ds_read_b64_tr_b16 a[88:89], v10 offset:4608\n"
    "v_exp_f32 v52, v76\n"
    "v_mfma_f32_16x16x32_bf16 v[80:83], a[144:147], a[16:19], v[80:83]\n"
    "ds_read_b64_tr_b16 a[90:91], v10 offset:6656\n"
    "v_add_f32 v43, v43, v50\n"
    "v_mfma_f32_16x16x32_bf16 v[84:87], a[148:151], a[16:19], v[84:87]\n"
    "ds_read_b64_tr_b16 a[92:93], v11 offset:4608\n"
    "v_add_f32 v43, v43, v51\n"
    "v_mfma_f32_16x16x32_bf16 v[92:95], a[148:151], a[20:23], v[92:95]\n"
    "ds_read_b64_tr_b16 a[94:95], v11 offset:6656\n"
    "buffer_load_dwordx4 v35, s[52:55], s11 offen offset:1024 lds\n"
    "v_mfma_f32_16x16x32_bf16 v[88:91], a[144:147], a[20:23], v[88:91]\n"
    "v_exp_f32 v53, v77\n"
    "v_mfma_f32_16x16x32_bf16 v[80:83], a[152:155], a[24:27], v[80:83]\n"
    "v_exp_f32 v54, v78\n"
    "v_mfma_f32_16x16x32_bf16 v[84:87], a[156:159], a[24:27], v[84:87]\n"
    "ds_read_b64_tr_b16 a[96:97], v10 offset:8192\n"
    "v_add_f32 v43, v43, v52\n"
    "v_mfma_f32_16x16x32_bf16 v[92:95], a[156:159], a[28:31], v[92:95]\n"
    "ds_read_b64_tr_b16 a[98:99], v10 offset:10240\n"
    "v_exp_f32 v55, v79\n"
    "v_mfma_f32_16x16x32_bf16 v[88:91], a[152:155], a[28:31], v[88:91]\n"
    "ds_read_b64_tr_b16 a[100:101], v11 offset:8192\n"
    "v_add_f32 v43, v43, v53\n"
    "v_mfma_f32_16x16x32_bf16 v[80:83], a[160:163], a[32:35], v[80:83]\n"
    "buffer_load_dwordx4 v36, s[52:55], s11 offen offset:2048 lds\n"
    "v_mfma_f32_16x16x32_bf16 v[84:87], a[164:167], a[32:35], v[84:87]\n"
    "ds_read_b64_tr_b16 a[102:103], v11 offset:10240\n"
    "v_add_f32 v43, v43, v54\n"
    "v_cvt_pk_bf16_f32 v100, v48, v49\n"
    "v_mfma_f32_16x16x32_bf16 v[92:95], a[164:167], a[36:39], v[92:95]\n"
    "ds_read_b64_tr_b16 a[104:105], v10 offset:8704\n"
    "v_add_f32 v43, v43, v55\n"
    "v_mfma_f32_16x16x32_bf16 v[88:91], a[160:163], a[36:39], v[88:91]\n"
    "ds_read_b64_tr_b16 a[106:107], v10 offset:10752\n"
    "v_add_f32 v44, v42, v43\n"
    "v_mfma_f32_16x16x32_bf16 v[80:83], a[168:171], a[40:43], v[80:83]\n"
    "ds_read_b64_tr_b16 a[108:109], v11 offset:8704\n"
    "v_cmp_nge_f32 vcc, s66, v44\n"
    "v_mfma_f32_16x16x32_bf16 v[84:87], a[172:175], a[40:43], v[84:87]\n"
    "ds_read_b64_tr_b16 a[110:111], v11 offset:10752\n"
    "v_cvt_pk_bf16_f32 v101, v50, v51\n"
    "v_add_u32 v18, 0x1c000, v34\n"
    "v_mfma_f32_16x16x32_bf16 v[92:95], a[172:175], a[44:47], v[92:95]\n"
    "ds_read_b64_tr_b16 a[112:113], v10 offset:12288\n"
    "v_cvt_pk_bf16_f32 v102, v52, v53\n"
    "v_mfma_f32_16x16x32_bf16 v[88:91], a[168:171], a[44:47], v[88:91]\n"
    "ds_read_b64_tr_b16 a[114:115], v10 offset:14336\n"
    "v_cvt_pk_bf16_f32 v103, v54, v55\n"
    "v_mfma_f32_16x16x32_bf16 v[80:83], a[176:179], a[48:51], v[80:83]\n"
    "ds_read_b64_tr_b16 a[116:117], v11 offset:12288\n"
    "buffer_load_dwordx4 v37, s[52:55], s11 offen offset:3072 lds\n"
    "v_mfma_f32_16x16x32_bf16 v[84:87], a[180:183], a[48:51], v[84:87]\n"
    "ds_read_b64_tr_b16 a[118:119], v11 offset:14336\n"
    "v_add_u32 v19, 0x1c000, v35\n"
    "v_mfma_f32_16x16x32_bf16 v[92:95], a[180:183], a[52:55], v[92:95]\n"
    "ds_read_b64_tr_b16 a[120:121], v10 offset:12800\n"
    "v_add_u32 v20, 0x1c000, v36\n"
    "v_mfma_f32_16x16x32_bf16 v[88:91], a[176:179], a[52:55], v[88:91]\n"
    "ds_read_b64_tr_b16 a[122:123], v10 offset:14848\n"
    "v_add_u32 v21, 0x1c000, v37\n"
    "v_mfma_f32_16x16x32_bf16 v[80:83], a[184:187], a[56:59], v[80:83]\n"
    "ds_read_b64_tr_b16 a[124:125], v11 offset:12800\n"
    "v_add_u32 v15, 0x7fffff00, v45\n"
    "v_mfma_f32_16x16x32_bf16 v[84:87], a[188:191], a[56:59], v[84:87]\n"
    "ds_read_b64_tr_b16 a[126:127], v11 offset:14848\n"
    "v_mfma_f32_16x16x32_bf16 v[92:95], a[188:191], a[60:63], v[92:95]\n"
    "v_mfma_f32_16x16x32_bf16 v[88:91], a[184:187], a[60:63], v[88:91]\n"
    "s_cbranch_vccnz L_stub7\n"
    "L_ret7:\n"
    "s_waitcnt vmcnt(12) lgkmcnt(6)\n"
    "s_barrier\n"
    "v_mfma_f32_16x16x32_bf16 v[128:131], a[64:67], v[96:99], v[128:131]\n"
    "ds_read_b128 a[128:131], v4 offset:32768\n"
    "s_add_u32 m0, s62, 0x1c000\n"
    "v_add_f32 v40, v40, v42\n"
    "v_mfma_f32_16x16x32_bf16 v[136:139], a[64:67], v[100:103], v[136:139]\n"
    "ds_read_b128 a[132:135], v4 offset:34816\n"
    "s_mov_b32 s67, 0x80000\n"
    "v_mfma_f32_16x16x32_bf16 v[140:143], a[68:71], v[100:103], v[140:143]\n"
    "buffer_load_dwordx4 v22, s[40:43], s67 offen nt lds\n"
    "v_mfma_f32_16x16x32_bf16 v[132:135], a[68:71], v[96:99], v[132:135]\n"
    "v_exp_f32 v48, v80\n"
    "v_mfma_f32_16x16x32_bf16 v[144:147], a[72:75], v[96:99], v[144:147]\n"
    "v_exp_f32 v49, v81\n"
    "v_mfma_f32_16x16x32_bf16 v[152:155], a[72:75], v[100:103], v[152:155]\n"
    "v_exp_f32 v50, v82\n"
    "v_mfma_f32_16x16x32_bf16 v[156:159], a[76:79], v[100:103], v[156:159]\n"
    "v_exp_f32 v51, v83\n"
    "v_mfma_f32_16x16x32_bf16 v[148:151], a[76:79], v[96:99], v[148:151]\n"
    "v_exp_f32 v52, v84\n"
    "v_mfma_f32_16x16x32_bf16 v[160:163], a[80:83], v[96:99], v[160:163]\n"
    "ds_read_b128 a[136:139], v5 offset:32768\n"
    "buffer_load_dwordx4 v23, s[40:43], s67 offen offset:1024 nt lds\n"
    "v_mfma_f32_16x16x32_bf16 v[168:171], a[80:83], v[100:103], v[168:171]\n"
    "v_exp_f32 v53, v85\n"
    "v_mfma_f32_16x16x32_bf16 v[172:175], a[84:87], v[100:103], v[172:175]\n"
    "v_exp_f32 v54, v86\n"
    "v_mfma_f32_16x16x32_bf16 v[164:167], a[84:87], v[96:99], v[164:167]\n"
    "v_exp_f32 v55, v87\n"
    "v_mfma_f32_16x16x32_bf16 v[176:179], a[88:91], v[96:99], v[176:179]\n"
    "ds_read_b128 a[140:143], v5 offset:34816\n"
    "v_cvt_pk_bf16_f32 v104, v48, v49\n"
    "v_mfma_f32_16x16x32_bf16 v[184:187], a[88:91], v[100:103], v[184:187]\n"
    "ds_read_b128 a[144:147], v4 offset:36864\n"
    "v_cvt_pk_bf16_f32 v105, v50, v51\n"
    "v_mfma_f32_16x16x32_bf16 v[188:191], a[92:95], v[100:103], v[188:191]\n"
    "ds_read_b128 a[148:151], v4 offset:38912\n"
    "v_cvt_pk_bf16_f32 v106, v52, v53\n"
    "v_mfma_f32_16x16x32_bf16 v[180:183], a[92:95], v[96:99], v[180:183]\n"
    "ds_read_b128 a[152:155], v5 offset:36864\n"
    "v_cvt_pk_bf16_f32 v107, v54, v55\n"
    "v_mfma_f32_16x16x32_bf16 v[192:195], a[96:99], v[96:99], v[192:195]\n"
    "ds_read_b128 a[156:159], v5 offset:38912\n"
    "buffer_load_dwordx4 v24, s[40:43], s67 offen offset:2048 nt lds\n"
    "v_mfma_f32_16x16x32_bf16 v[200:203], a[96:99], v[100:103], v[200:203]\n"
    "ds_read_b128 a[160:163], v4 offset:40960\n"
    "v_add_f32 v42, v48, v49\n"
    "v_mfma_f32_16x16x32_bf16 v[204:207], a[100:103], v[100:103], v[204:207]\n"
    "ds_read_b128 a[164:167], v4 offset:43008\n"
    "v_add_f32 v42, v42, v50\n"
    "v_mfma_f32_16x16x32_bf16 v[196:199], a[100:103], v[96:99], v[196:199]\n"
    "ds_read_b128 a[168:171], v5 offset:40960\n"
    "v_add_f32 v42, v42, v51\n"
    "v_mfma_f32_16x16x32_bf16 v[208:211], a[104:107], v[96:99], v[208:211]\n"
    "ds_read_b128 a[172:175], v5 offset:43008\n"
    "v_add_f32 v42, v42, v52\n"
    "v_mfma_f32_16x16x32_bf16 v[216:219], a[104:107], v[100:103], v[216:219]\n"
    "ds_read_b128 a[176:179], v4 offset:45056\n"
    "v_add_f32 v42, v42, v53\n"
    "v_mfma_f32_16x16x32_bf16 v[220:223], a[108:111], v[100:103], v[220:223]\n"
    "ds_read_b128 a[180:183], v4 offset:47104\n"
    "v_add_f32 v42, v42, v54\n"
    "v_mfma_f32_16x16x32_bf16 v[212:215], a[108:111], v[96:99], v[212:215]\n"
    "ds_read_b128 a[184:187], v5 offset:45056\n"
    "v_add_f32 v42, v42, v55\n"
    "v_mfma_f32_16x16x32_bf16 v[224:227], a[112:115], v[96:99], v[224:227]\n"
    "ds_read_b128 a[188:191], v5 offset:47104\n"
    "buffer_load_dwordx4 v25, s[40:43], s67 offen offset:3072 nt lds\n"
    "v_mfma_f32_16x16x32_bf16 v[232:235], a[112:115], v[100:103], v[232:235]\n"
    "ds_read_b64_tr_b16 a[64:65], v10 offset:16384\n"
    "v_add_f32 v41, v41, v43\n"
    "s_waitcnt lgkmcnt(15)\n"
    "v_mfma_f32_16x16x32_bf16 v[236:239], a[116:119], v[100:103], v[236:239]\n"
    "ds_read_b64_tr_b16 a[66:67], v10 offset:18432\n"
    "v_mfma_f32_16x16x32_bf16 v[228:231], a[116:119], v[96:99], v[228:231]\n"
    "ds_read_b64_tr_b16 a[68:69], v11 offset:16384\n"
    "v_mfma_f32_16x16x32_bf16 v[240:243], a[120:123], v[96:99], v[240:243]\n"
    "ds_read_b64_tr_b16 a[70:71], v11 offset:18432\n"
    "v_mfma_f32_16x16x32_bf16 v[248:251], a[120:123], v[100:103], v[248:251]\n"
    "ds_read_b64_tr_b16 a[72:73], v10 offset:16896\n"
    "v_mfma_f32_16x16x32_bf16 v[252:255], a[124:127], v[100:103], v[252:255]\n"
    "ds_read_b64_tr_b16 a[74:75], v10 offset:18944\n"
    "v_mfma_f32_16x16x32_bf16 v[244:247], a[124:127], v[96:99], v[244:247]\n"
    "ds_read_b64_tr_b16 a[76:77], v11 offset:16896\n"
    "ds_read_b64_tr_b16 a[78:79], v11 offset:18944\n"
    "s_waitcnt lgkmcnt(8)\n"
    "v_mfma_f32_16x16x32_bf16 v[64:67], a[128:131], a[0:3], v[120:123]\n"
    "ds_read_b64_tr_b16 a[80:81], v10 offset:20480\n"
    "ds_read_b64_tr_b16 a[82:83], v10 offset:22528\n"
    "s_add_u32 m0, s62, 0\n"
    "v_mfma_f32_16x16x32_bf16 v[68:71], a[132:135], a[0:3], v[120:123]\n"
    "ds_read_b64_tr_b16 a[84:85], v11 offset:20480\n"
    "buffer_load_dwordx4 v34, s[52:55], s12 offen lds\n"
    "v_mfma_f32_16x16x32_bf16 v[76:79], a[132:135], a[4:7], v[124:127]\n"
    "v_exp_f32 v48, v88\n"
    "v_mfma_f32_16x16x32_bf16 v[72:75], a[128:131], a[4:7], v[124:127]\n"
    "ds_read_b64_tr_b16 a[86:87], v11 offset:22528\n"
    "v_exp_f32 v49, v89\n"
    "v_mfma_f32_16x16x32_bf16 v[64:67], a[136:139], a[8:11], v[64:67]\n"
    "buffer_load_dwordx4 v35, s[52:55], s12 offen offset:1024 lds\n"
    "v_mfma_f32_16x16x32_bf16 v[68:71], a[140:143], a[8:11], v[68:71]\n"
    "ds_read_b64_tr_b16 a[88:89], v10 offset:20992\n"
    "v_exp_f32 v50, v90\n"
    "v_mfma_f32_16x16x32_bf16 v[76:79], a[140:143], a[12:15], v[76:79]\n"
    "v_exp_f32 v51, v91\n"
    "v_mfma_f32_16x16x32_bf16 v[72:75], a[136:139], a[12:15], v[72:75]\n"
    "ds_read_b64_tr_b16 a[90:91], v10 offset:23040\n"
    "v_exp_f32 v52, v92\n"
    "v_mfma_f32_16x16x32_bf16 v[64:67], a[144:147], a[16:19], v[64:67]\n"
    "buffer_load_dwordx4 v36, s[52:55], s12 offen offset:2048 lds\n"
    "v_mfma_f32_16x16x32_bf16 v[68:71], a[148:151], a[16:19], v[68:71]\n"
    "ds_read_b64_tr_b16 a[92:93], v11 offset:20992\n"
    "v_exp_f32 v53, v93\n"
    "v_mfma_f32_16x16x32_bf16 v[76:79], a[148:151], a[20:23], v[76:79]\n"
    "v_exp_f32 v54, v94\n"
    "v_mfma_f32_16x16x32_bf16 v[72:75], a[144:147], a[20:23], v[72:75]\n"
    "ds_read_b64_tr_b16 a[94:95], v11 offset:23040\n"
    "v_exp_f32 v55, v95\n"
    "v_mfma_f32_16x16x32_bf16 v[64:67], a[152:155], a[24:27], v[64:67]\n"
    "buffer_load_dwordx4 v37, s[52:55], s12 offen offset:3072 lds\n"
    "v_mfma_f32_16x16x32_bf16 v[68:71], a[156:159], a[24:27], v[68:71]\n"
    "ds_read_b64_tr_b16 a[96:97], v10 offset:24576\n"
    "s_add_u32 m0, s62, 0x20000\n"
    "v_add_f32 v43, v48, v49\n"
    "v_mfma_f32_16x16x32_bf16 v[76:79], a[156:159], a[28:31], v[76:79]\n"
    "ds_read_b64_tr_b16 a[98:99], v10 offset:26624\n"
    "v_add_f32 v43, v43, v50\n"
    "v_mfma_f32_16x16x32_bf16 v[72:75], a[152:155], a[28:31], v[72:75]\n"
    "ds_read_b64_tr_b16 a[100:101], v11 offset:24576\n"
    "v_add_f32 v43, v43, v51\n"
    "v_cvt_pk_bf16_f32 v108, v48, v49\n"
    "v_mfma_f32_16x16x32_bf16 v[64:67], a[160:163], a[32:35], v[64:67]\n"
    "ds_read_b64_tr_b16 a[102:103], v11 offset:26624\n"
    "buffer_load_dwordx4 v18, s[52:55], s11 offen lds\n"
    "v_mfma_f32_16x16x32_bf16 v[68:71], a[164:167], a[32:35], v[68:71]\n"
    "ds_read_b64_tr_b16 a[104:105], v10 offset:25088\n"
    "v_add_f32 v43, v43, v52\n"
    "v_mfma_f32_16x16x32_bf16 v[76:79], a[164:167], a[36:39], v[76:79]\n"
    "ds_read_b64_tr_b16 a[106:107], v10 offset:27136\n"
    "v_add_f32 v43, v43, v53\n"
    "v_cvt_pk_bf16_f32 v109, v50, v51\n"
    "v_mfma_f32_16x16x32_bf16 v[72:75], a[160:163], a[36:39], v[72:75]\n"
    "ds_read_b64_tr_b16 a[108:109], v11 offset:25088\n"
    "v_add_f32 v43, v43, v54\n"
    "v_mfma_f32_16x16x32_bf16 v[64:67], a[168:171], a[40:43], v[64:67]\n"
    "ds_read_b64_tr_b16 a[110:111], v11 offset:27136\n"
    "buffer_load_dwordx4 v19, s[52:55], s11 offen offset:1024 lds\n"
    "v_mfma_f32_16x16x32_bf16 v[68:71], a[172:175], a[40:43], v[68:71]\n"
    "ds_read_b64_tr_b16 a[112:113], v10 offset:28672\n"
    "v_add_f32 v43, v43, v55\n"
    "v_mfma_f32_16x16x32_bf16 v[76:79], a[172:175], a[44:47], v[76:79]\n"
    "ds_read_b64_tr_b16 a[114:115], v10 offset:30720\n"
    "v_add_f32 v44, v42, v43\n"
    "v_cvt_pk_bf16_f32 v110, v52, v53\n"
    "v_mfma_f32_16x16x32_bf16 v[72:75], a[168:171], a[44:47], v[72:75]\n"
    "ds_read_b64_tr_b16 a[116:117], v11 offset:28672\n"
    "v_cmp_nge_f32 vcc, s66, v44\n"
    "v_mfma_f32_16x16x32_bf16 v[64:67], a[176:179], a[48:51], v[64:67]\n"
    "ds_read_b64_tr_b16 a[118:119], v11 offset:30720\n"
    "buffer_load_dwordx4 v20, s[52:55], s11 offen offset:2048 lds\n"
    "v_mfma_f32_16x16x32_bf16 v[68:71], a[180:183], a[48:51], v[68:71]\n"
    "ds_read_b64_tr_b16 a[120:121], v10 offset:29184\n"
    "v_cvt_pk_bf16_f32 v111, v54, v55\n"
    "v_mfma_f32_16x16x32_bf16 v[76:79], a[180:183], a[52:55], v[76:79]\n"
    "ds_read_b64_tr_b16 a[122:123], v10 offset:31232\n"
    "ds_read_b64_tr_b16 a[124:125], v11 offset:29184\n"
    "v_mfma_f32_16x16x32_bf16 v[72:75], a[176:179], a[52:55], v[72:75]\n"
    "ds_read_b64_tr_b16 a[126:127], v11 offset:31232\n"
    "v_mfma_f32_16x16x32_bf16 v[64:67], a[184:187], a[56:59], v[64:67]\n"
    "buffer_load_dwordx4 v21, s[52:55], s11 offen offset:3072 lds\n"
    "v_mfma_f32_16x16x32_bf16 v[68:71], a[188:191], a[56:59], v[68:71]\n"
    "v_mfma_f32_16x16x32_bf16 v[76:79], a[188:191], a[60:63], v[76:79]\n"
    "v_mfma_f32_16x16x32_bf16 v[72:75], a[184:187], a[60:63], v[72:75]\n"
    "s_cbranch_vccnz L_stub8\n"
    "L_ret8:\n"
    "s_waitcnt vmcnt(16) lgkmcnt(6)\n"
    "s_barrier\n"
    "v_mfma_f32_16x16x32_bf16 v[128:131], a[64:67], v[104:107], v[128:131]\n"
    "ds_read_b128 a[128:131], v4 offset:49152\n"
    "s_add_u32 m0, s62, 0x4000\n"
    "v_mfma_f32_16x16x32_bf16 v[136:139], a[64:67], v[108:111], v[136:139]\n"
    "buffer_load_dwordx4 v34, s[52:55], s13 offen lds\n"
    "v_mfma_f32_16x16x32_bf16 v[140:143], a[68:71], v[108:111], v[140:143]\n"
    "ds_read_b128 a[132:135], v4 offset:51200\n"
    "ds_read_b128 a[136:139], v5 offset:49152\n"
    "v_add_f32 v40, v40, v42\n"
    "v_mfma_f32_16x16x32_bf16 v[132:135], a[68:71], v[104:107], v[132:135]\n"
    "ds_read_b128 a[140:143], v5 offset:51200\n"
    "v_add_f32 v41, v41, v43\n"
    "v_mfma_f32_16x16x32_bf16 v[144:147], a[72:75], v[104:107], v[144:147]\n"
    "buffer_load_dwordx4 v35, s[52:55], s13 offen offset:1024 lds\n"
    "v_mfma_f32_16x16x32_bf16 v[152:155], a[72:75], v[108:111], v[152:155]\n"
    "v_exp_f32 v48, v64\n"
    "v_mfma_f32_16x16x32_bf16 v[156:159], a[76:79], v[108:111], v[156:159]\n"
    "ds_read_b128 a[144:147], v4 offset:53248\n"
    "ds_read_b128 a[148:151], v4 offset:55296\n"
    "v_mfma_f32_16x16x32_bf16 v[148:151], a[76:79], v[104:107], v[148:151]\n"
    "ds_read_b128 a[152:155], v5 offset:53248\n"
    "v_exp_f32 v49, v65\n"
    "v_mfma_f32_16x16x32_bf16 v[160:163], a[80:83], v[104:107], v[160:163]\n"
    "ds_read_b128 a[156:159], v5 offset:55296\n"
    "buffer_load_dwordx4 v36, s[52:55], s13 offen offset:2048 lds\n"
    "v_mfma_f32_16x16x32_bf16 v[168:171], a[80:83], v[108:111], v[168:171]\n"
    "v_exp_f32 v50, v66\n"
    "v_mfma_f32_16x16x32_bf16 v[172:175], a[84:87], v[108:111], v[172:175]\n"
    "ds_read_b128 a[160:163], v4 offset:57344\n"
    "v_cvt_pk_bf16_f32 v96, v48, v49\n"
    "v_mfma_f32_16x16x32_bf16 v[164:167], a[84:87], v[104:107], v[164:167]\n"
    "ds_read_b128 a[164:167], v4 offset:59392\n"
    "v_add_f32 v42, v48, v49\n"
    "L_iw4:\n"
    "s_cbranch_execz L_iw8\n"
    "v_mfma_f32_16x16x32_bf16 v[176:179], a[88:91], v[104:107], v[176:179]\n"
    "buffer_load_dwordx4 v37, s[52:55], s13 offen offset:3072 lds\n"
    "v_mfma_f32_16x16x32_bf16 v[184:187], a[88:91], v[108:111], v[184:187]\n"
    "ds_read_b128 a[168:171], v5 offset:57344\n"
    "s_add_u32 m0, s62, 0x10000\n"
    "v_mfma_f32_16x16x32_bf16 v[188:191], a[92:95], v[108:111], v[188:191]\n"
    "ds_read_b128 a[172:175], v5 offset:59392\n"
    "v_add_f32 v42, v42, v50\n"
    "v_mfma_f32_16x16x32_bf16 v[180:183], a[92:95], v[104:107], v[180:183]\n"
    "ds_read_b128 a[176:179], v4 offset:61440\n"
    "v_mfma_f32_16x16x32_bf16 v[192:195], a[96:99], v[104:107], v[192:195]\n"
    "ds_read_b128 a[180:183], v4 offset:63488\n"
    "buffer_load_dwordx4 v18, s[52:55], s12 offen lds\n"
    "v_mfma_f32_16x16x32_bf16 v[200:203], a[96:99], v[108:111], v[200:203]\n"
    "ds_read_b128 a[184:187], v5 offset:61440\n"
    "v_exp_f32 v51, v67\n"
    "v_mfma_f32_16x16x32_bf16 v[204:207], a[100:103], v[108:111], v[204:207]\n"
    "v_exp_f32 v52, v68\n"
    "v_mfma_f32_16x16x32_bf16 v[196:199], a[100:103], v[104:107], v[196:199]\n"
    "ds_read_b128 a[188:191], v5 offset:63488\n"
    "v_cvt_pk_bf16_f32 v97, v50, v51\n"
    "v_mfma_f32_16x16x32_bf16 v[208:211], a[104:107], v[104:107], v[208:211]\n"
    "ds_read_b64_tr_b16 a[64:65], v10 offset:32768\n"
    "v_add_f32 v42, v42, v51\n"
    "v_mfma_f32_16x16x32_bf16 v[216:219], a[104:107], v[108:111], v[216:219]\n"
    "buffer_load_dwordx4 v19, s[52:55], s12 offen offset:1024 lds\n"
    "v_mfma_f32_16x16x32_bf16 v[220:223], a[108:111], v[108:111], v[220:223]\n"
    "ds_read_b64_tr_b16 a[66:67], v10 offset:34816\n"
    "v_add_f32 v42, v42, v52\n"
    "v_mfma_f32_16x16x32_bf16 v[212:215], a[108:111], v[104:107], v[212:215]\n"
    "v_exp_f32 v53, v69\n"
    "v_mfma_f32_16x16x32_bf16 v[224:227], a[112:115], v[104:107], v[224:227]\n"
    "ds_read_b64_tr_b16 a[68:69], v11 offset:32768\n"
    "v_cvt_pk_bf16_f32 v98, v52, v53\n"
    "v_mfma_f32_16x16x32_bf16 v[232:235], a[112:115], v[108:111], v[232:235]\n"
    "ds_read_b64_tr_b16 a[70:71], v11 offset:34816\n"
    "buffer_load_dwordx4 v20, s[52:55], s12 offen offset:2048 lds\n"
    "v_add_f32 v42, v42, v53\n"
    "s_waitcnt lgkmcnt(15)\n"
    "v_mfma_f32_16x16x32_bf16 v[236:239], a[116:119], v[108:111], v[236:239]\n"
    "v_exp_f32 v54, v70\n"
    "v_mfma_f32_16x16x32_bf16 v[228:231], a[116:119], v[104:107], v[228:231]\n"
    "v_exp_f32 v55, v71\n"
    "v_mfma_f32_16x16x32_bf16 v[240:243], a[120:123], v[104:107], v[240:243]\n"
    "ds_read_b64_tr_b16 a[72:73], v10 offset:33280\n"
    "v_cvt_pk_bf16_f32 v99, v54, v55\n"
    "v_mfma_f32_16x16x32_bf16 v[248:251], a[120:123], v[108:111], v[248:251]\n"
    "ds_read_b64_tr_b16 a[74:75], v10 offset:35328\n"
    "v_add_f32 v42, v42, v54\n"
    "v_mfma_f32_16x16x32_bf16 v[252:255], a[124:127], v[108:111], v[252:255]\n"
    "ds_read_b64_tr_b16 a[76:77], v11 offset:33280\n"
    "v_add_f32 v42, v42, v55\n"
    "v_mfma_f32_16x16x32_bf16 v[244:247], a[124:127], v[104:107], v[244:247]\n"
    "ds_read_b64_tr_b16 a[78:79], v11 offset:35328\n"
    "buffer_load_dwordx4 v21, s[52:55], s12 offen offset:3072 lds\n"
    "s_waitcnt lgkmcnt(8)\n"
    "v_mfma_f32_16x16x32_bf16 v[80:83], a[128:131], a[0:3], v[120:123]\n"
    "ds_read_b64_tr_b16 a[80:81], v10 offset:36864\n"
    "v_exp_f32 v48, v72\n"
    "v_mfma_f32_16x16x32_bf16 v[84:87], a[132:135], a[0:3], v[120:123]\n"
    "ds_read_b64_tr_b16 a[82:83], v10 offset:38912\n"
    "s_add_u32 m0, s62, 0x8000\n"
    "v_mfma_f32_16x16x32_bf16 v[92:95], a[132:135], a[4:7], v[124:127]\n"
    "ds_read_b64_tr_b16 a[84:85], v11 offset:36864\n"
    "ds_read_b64_tr_b16 a[86:87], v11 offset:38912\n"
    "v_mfma_f32_16x16x32_bf16 v[88:91], a[128:131], a[4:7], v[124:127]\n"
    "ds_read_b64_tr_b16 a[88:89], v10 offset:37376\n"
    "buffer_load_dwordx4 v34, s[52:55], s14 offen lds\n"
    "v_exp_f32 v49, v73\n"
    "v_mfma_f32_16x16x32_bf16 v[80:83], a[136:139], a[8:11], v[80:83]\n"
    "ds_read_b64_tr_b16 a[90:91], v10 offset:39424\n"
    "v_exp_f32 v50, v74\n"
    "v_mfma_f32_16x16x32_bf16 v[84:87], a[140:143], a[8:11], v[84:87]\n"
    "ds_read_b64_tr_b16 a[92:93], v11 offset:37376\n"
    "v_exp_f32 v51, v75\n"
    "v_mfma_f32_16x16x32_bf16 v[92:95], a[140:143], a[12:15], v[92:95]\n"
    "ds_read_b64_tr_b16 a[94:95], v11 offset:39424\n"
    "v_add_f32 v43, v48, v49\n"
    "v_mfma_f32_16x16x32_bf16 v[88:91], a[136:139], a[12:15], v[88:91]\n"
    "v_exp_f32 v52, v76\n"
    "v_mfma_f32_16x16x32_bf16 v[80:83], a[144:147], a[16:19], v[80:83]\n"
    "buffer_load_dwordx4 v35, s[52:55], s14 offen offset:1024 lds\n"
    "v_mfma_f32_16x16x32_bf16 v[84:87], a[148:151], a[16:19], v[84:87]\n"
    "ds_read_b64_tr_b16 a[96:97], v10 offset:40960\n"
    "v_add_f32 v43, v43, v50\n"
    "v_mfma_f32_16x16x32_bf16 v[92:95], a[148:151], a[20:23], v[92:95]\n"
    "ds_read_b64_tr_b16 a[98:99], v10 offset:43008\n"
    "v_exp_f32 v53, v77\n"
    "v_mfma_f32_16x16x32_bf16 v[88:91], a[144:147], a[20:23], v[88:91]\n"
    "ds_read_b64_tr_b16 a[100:101], v11 offset:40960\n"
    "v_add_f32 v43, v43, v51\n"
    "v_mfma_f32_16x16x32_bf16 v[80:83], a[152:155], a[24:27], v[80:83]\n"
    "v_exp_f32 v54, v78\n"
    "v_mfma_f32_16x16x32_bf16 v[84:87], a[156:159], a[24:27], v[84:87]\n"
    "ds_read_b64_tr_b16 a[102:103], v11 offset:43008\n"
    "v_exp_f32 v55, v79\n"
    "v_mfma_f32_16x16x32_bf16 v[92:95], a[156:159], a[28:31], v[92:95]\n"
    "ds_read_b64_tr_b16 a[104:105], v10 offset:41472\n"
    "v_add_f32 v43, v43, v52\n"
    "v_mfma_f32_16x16x32_bf16 v[88:91], a[152:155], a[28:31], v[88:91]\n"
    "ds_read_b64_tr_b16 a[106:107], v10 offset:43520\n"
    "v_add_f32 v43, v43, v53\n"
    "v_mfma_f32_16x16x32_bf16 v[80:83], a[160:163], a[32:35], v[80:83]\n"
    "ds_read_b64_tr_b16 a[108:109], v11 offset:41472\n"
    "buffer_load_dwordx4 v36, s[52:55], s14 offen offset:2048 lds\n"
    "v_mfma_f32_16x16x32_bf16 v[84:87], a[164:167], a[32:35], v[84:87]\n"
    "ds_read_b64_tr_b16 a[110:111], v11 offset:43520\n"
    "v_add_f32 v43, v43, v54\n"
    "v_mfma_f32_16x16x32_bf16 v[92:95], a[164:167], a[36:39], v[92:95]\n"
    "ds_read_b64_tr_b16 a[112:113], v10 offset:45056\n"
    "v_add_f32 v43, v43, v55\n"
    "v_mfma_f32_16x16x32_bf16 v[88:91], a[160:163], a[36:39], v[88:91]\n"
    "ds_read_b64_tr_b16 a[114:115], v10 offset:47104\n"
    "v_add_f32 v44, v42, v43\n"
    "v_mfma_f32_16x16x32_bf16 v[80:83], a[168:171], a[40:43], v[80:83]\n"
    "ds_read_b64_tr_b16 a[116:117], v11 offset:45056\n"
    "v_cmp_nge_f32 vcc, s66, v44\n"
    "v_mfma_f32_16x16x32_bf16 v[84:87], a[172:175], a[40:43], v[84:87]\n"
    "ds_read_b64_tr_b16 a[118:119], v11 offset:47104\n"
    "v_cvt_pk_bf16_f32 v100, v48, v49\n"
    "v_mfma_f32_16x16x32_bf16 v[92:95], a[172:175], a[44:47], v[92:95]\n"
    "ds_read_b64_tr_b16 a[120:121], v10 offset:45568\n"
    "v_cvt_pk_bf16_f32 v101, v50, v51\n"
    "v_mfma_f32_16x16x32_bf16 v[88:91], a[168:171], a[44:47], v[88:91]\n"
    "ds_read_b64_tr_b16 a[122:123], v10 offset:47616\n"
    "v_cvt_pk_bf16_f32 v102, v52, v53\n"
    "v_mfma_f32_16x16x32_bf16 v[80:83], a[176:179], a[48:51], v[80:83]\n"
    "ds_read_b64_tr_b16 a[124:125], v11 offset:45568\n"
    "v_cvt_pk_bf16_f32 v103, v54, v55\n"
    "v_mfma_f32_16x16x32_bf16 v[84:87], a[180:183], a[48:51], v[84:87]\n"
    "ds_read_b64_tr_b16 a[126:127], v11 offset:47616\n"
    "v_mfma_f32_16x16x32_bf16 v[92:95], a[180:183], a[52:55], v[92:95]\n"
    "buffer_load_dwordx4 v37, s[52:55], s14 offen offset:3072 lds\n"
    "v_mfma_f32_16x16x32_bf16 v[88:91], a[176:179], a[52:55], v[88:91]\n"
    "v_mfma_f32_16x16x32_bf16 v[80:83], a[184:187], a[56:59], v[80:83]\n"
    "v_mfma_f32_16x16x32_bf16 v[84:87], a[188:191], a[56:59], v[84:87]\n"
    "v_mfma_f32_16x16x32_bf16 v[92:95], a[188:191], a[60:63], v[92:95]\n"
    "v_mfma_f32_16x16x32_bf16 v[88:91], a[184:187], a[60:63], v[88:91]\n"
    "s_cbranch_vccnz L_stub9\n"
    "L_ret9:\n"
    "s_waitcnt vmcnt(20) lgkmcnt(0)\n"
    "s_barrier\n"
    "v_mfma_f32_16x16x32_bf16 v[128:131], a[64:67], v[96:99], v[128:131]\n"
    "ds_read_b64_tr_b16 a[128:129], v10 offset:49152\n"
    "ds_read_b64_tr_b16 a[130:131], v10 offset:51200\n"
    "s_add_u32 m0, s62, 0x14000\n"
    "v_add_f32 v40, v40, v42\n"
    "v_mfma_f32_16x16x32_bf16 v[136:139], a[64:67], v[100:103], v[136:139]\n"
    "buffer_load_dwordx4 v18, s[52:55], s13 offen lds\n"
    "v_mfma_f32_16x16x32_bf16 v[140:143], a[68:71], v[100:103], v[140:143]\n"
    "ds_read_b64_tr_b16 a[132:133], v11 offset:49152\n"
    "ds_read_b64_tr_b16 a[134:135], v11 offset:51200\n"
    "v_add_f32 v41, v41, v43\n"
    "v_mfma_f32_16x16x32_bf16 v[132:135], a[68:71], v[96:99], v[132:135]\n"
    "ds_read_b64_tr_b16 a[136:137], v10 offset:49664\n"
    "ds_read_b64_tr_b16 a[138:139], v10 offset:51712\n"
    "v_exp_f32 v48, v80\n"
    "v_exp_f32 v49, v81\n"
    "v_mfma_f32_16x16x32_bf16 v[144:147], a[72:75], v[96:99], v[144:147]\n"
    "buffer_load_dwordx4 v19, s[52:55], s13 offen offset:1024 lds\n"
    "v_exp_f32 v50, v82\n"
    "v_mfma_f32_16x16x32_bf16 v[152:155], a[72:75], v[100:103], v[152:155]\n"
    "ds_read_b64_tr_b16 a[140:141], v11 offset:49664\n"
    "v_exp_f32 v51, v83\n"
    "v_mfma_f32_16x16x32_bf16 v[156:159], a[76:79], v[100:103], v[156:159]\n"
    "ds_read_b64_tr_b16 a[142:143], v11 offset:51712\n"
    "v_exp_f32 v52, v84\n"
    "v_cvt_pk_bf16_f32 v104, v48, v49\n"
    "v_mfma_f32_16x16x32_bf16 v[148:151], a[76:79], v[96:99], v[148:151]\n"
    "ds_read_b64_tr_b16 a[144:145], v10 offset:53248\n"
    "v_exp_f32 v53, v85\n"
    "v_cvt_pk_bf16_f32 v105, v50, v51\n"
    "v_add_f32 v42, v48, v49\n"
    "v_exp_f32 v54, v86\n"
    "v_mfma_f32_16x16x32_bf16 v[160:163], a[80:83], v[96:99], v[160:163]\n"
    "buffer_load_dwordx4 v20, s[52:55], s13 offen offset:2048 lds\n"
    "v_exp_f32 v55, v87\n"
    "v_mfma_f32_16x16x32_bf16 v[168:171], a[80:83], v[100:103], v[168:171]\n"
    "ds_read_b64_tr_b16 a[146:147], v10 offset:55296\n"
    "ds_read_b64_tr_b16 a[148:149], v11 offset:53248\n"
    "v_cvt_pk_bf16_f32 v106, v52, v53\n"
    "v_add_f32 v42, v42, v50\n"
    "v_cvt_pk_bf16_f32 v107, v54, v55\n"
    "v_mfma_f32_16x16x32_bf16 v[172:175], a[84:87], v[100:103], v[172:175]\n"
    "ds_read_b64_tr_b16 a[150:151], v11 offset:55296\n"
    "ds_read_b64_tr_b16 a[152:153], v10 offset:53760\n"
    "v_add_f32 v42, v42, v51\n"
    "v_add_f32 v42, v42, v52\n"
    "v_add_f32 v42, v42, v53\n"
    "v_mfma_f32_16x16x32_bf16 v[164:167], a[84:87], v[96:99], v[164:167]\n"
    "ds_read_b64_tr_b16 a[154:155], v10 offset:55808\n"
    "ds_read_b64_tr_b16 a[156:157], v11 offset:53760\n"
    "v_add_f32 v42, v42, v54\n"
    "v_add_f32 v42, v42, v55\n"
    "v_mfma_f32_16x16x32_bf16 v[176:179], a[88:91], v[96:99], v[176:179]\n"
    "buffer_load_dwordx4 v21, s[52:55], s13 offen offset:3072 lds\n"
    "v_exp_f32 v48, v88\n"
    "v_mfma_f32_16x16x32_bf16 v[184:187], a[88:91], v[100:103], v[184:187]\n"
    "ds_read_b64_tr_b16 a[158:159], v11 offset:55808\n"
    "s_add_u32 m0, s62, 0xc000\n"
    "v_exp_f32 v49, v89\n"
    "v_mfma_f32_16x16x32_bf16 v[188:191], a[92:95], v[100:103], v[188:191]\n"
    "ds_read_b64_tr_b16 a[160:161], v10 offset:57344\n"
    "v_exp_f32 v50, v90\n"
    "v_cvt_pk_bf16_f32 v108, v48, v49\n"
    "v_mfma_f32_16x16x32_bf16 v[180:183], a[92:95], v[96:99], v[180:183]\n"
    "ds_read_b64_tr_b16 a[162:163], v10 offset:59392\n"
    "v_exp_f32 v51, v91\n"
    "v_add_f32 v43, v48, v49\n"
    "v_exp_f32 v52, v92\n"
    "v_mfma_f32_16x16x32_bf16 v[192:195], a[96:99], v[96:99], v[192:195]\n"
    "buffer_load_dwordx4 v34, s[52:55], s15 offen lds\n"
    "v_exp_f32 v53, v93\n"
    "v_mfma_f32_16x16x32_bf16 v[200:203], a[96:99], v[100:103], v[200:203]\n"
    "ds_read_b64_tr_b16 a[164:165], v11 offset:57344\n"
    "v_exp_f32 v54, v94\n"
    "v_cvt_pk_bf16_f32 v109, v50, v51\n"
    "v_add_f32 v43, v43, v50\n"
    "v_exp_f32 v55, v95\n"
    "v_mfma_f32_16x16x32_bf16 v[204:207], a[100:103], v[100:103], v[204:207]\n"
    "ds_read_b64_tr_b16 a[166:167], v11 offset:59392\n"
    "ds_read_b64_tr_b16 a[168:169], v10 offset:57856\n"
    "v_cvt_pk_bf16_f32 v110, v52, v53\n"
    "v_add_f32 v43, v43, v51\n"
    "v_mfma_f32_16x16x32_bf16 v[196:199], a[100:103], v[96:99], v[196:199]\n"
    "ds_read_b64_tr_b16 a[170:171], v10 offset:59904\n"
    "ds_read_b64_tr_b16 a[172:173], v11 offset:57856\n"
    "v_cvt_pk_bf16_f32 v111, v54, v55\n"
    "v_add_f32 v43, v43, v52\n"
    "v_mfma_f32_16x16x32_bf16 v[208:211], a[104:107], v[96:99], v[208:211]\n"
    "ds_read_b64_tr_b16 a[174:175], v11 offset:59904\n"
    "buffer_load_dwordx4 v35, s[52:55], s15 offen offset:1024 lds\n"
    "v_mfma_f32_16x16x32_bf16 v[216:219], a[104:107], v[100:103], v[216:219]\n"
    "ds_read_b64_tr_b16 a[176:177], v10 offset:61440\n"
    "ds_read_b64_tr_b16 a[178:179], v10 offset:63488\n"
    "v_add_f32 v43, v43, v53\n"
    "v_mfma_f32_16x16x32_bf16 v[220:223], a[108:111], v[100:103], v[220:223]\n"
    "ds_read_b64_tr_b16 a[180:181], v11 offset:61440\n"
    "ds_read_b64_tr_b16 a[182:183], v11 offset:63488\n"
    "v_add_f32 v43, v43, v54\n"
    "v_add_f32 v43, v43, v55\n"
    "v_mfma_f32_16x16x32_bf16 v[212:215], a[108:111], v[96:99], v[212:215]\n"
    "ds_read_b64_tr_b16 a[184:185], v10 offset:61952\n"
    "ds_read_b64_tr_b16 a[186:187], v10 offset:64000\n"
    "v_add_f32 v44, v42, v43\n"
    "v_mfma_f32_16x16x32_bf16 v[224:227], a[112:115], v[96:99], v[224:227]\n"
    "ds_read_b64_tr_b16 a[188:189], v11 offset:61952\n"
    "buffer_load_dwordx4 v36, s[52:55], s15 offen offset:2048 lds\n"
    "v_cmp_nge_f32 vcc, s66, v44\n"
    "v_mfma_f32_16x16x32_bf16 v[232:235], a[112:115], v[100:103], v[232:235]\n"
    "ds_read_b64_tr_b16 a[190:191], v11 offset:64000\n"
    "v_mfma_f32_16x16x32_bf16 v[236:239], a[116:119], v[100:103], v[236:239]\n"
    "v_mfma_f32_16x16x32_bf16 v[228:231], a[116:119], v[96:99], v[228:231]\n"
    "v_mfma_f32_16x16x32_bf16 v[240:243], a[120:123], v[96:99], v[240:243]\n"
    "buffer_load_dwordx4 v37, s[52:55], s15 offen offset:3072 lds\n"
    "v_mfma_f32_16x16x32_bf16 v[248:251], a[120:123], v[100:103], v[248:251]\n"
    "v_mfma_f32_16x16x32_bf16 v[252:255], a[124:127], v[100:103], v[252:255]\n"
    "v_mfma_f32_16x16x32_bf16 v[244:247], a[124:127], v[96:99], v[244:247]\n"
    "s_cbranch_vccnz L_stub10\n"
    "L_ret10:\n"
    "s_waitcnt vmcnt(20) lgkmcnt(0)\n"
    "s_barrier\n"
    "v_mfma_f32_16x16x32_bf16 v[128:131], a[128:131], v[104:107], v[128:131]\n"
    "ds_read_b64_tr_b16 a[64:65], v12 offset:16384\n"
    "s_add_u32 m0, s62, 0x18000\n"
    "v_add_f32 v40, v40, v42\n"
    "v_mfma_f32_16x16x32_bf16 v[136:139], a[128:131], v[108:111], v[136:139]\n"
    "ds_read_b64_tr_b16 a[66:67], v12 offset:18432\n"
    "ds_read_b64_tr_b16 a[68:69], v13 offset:16384\n"
    "buffer_load_dwordx4 v18, s[52:55], s14 offen lds\n"
    "v_mfma_f32_16x16x32_bf16 v[140:143], a[132:135], v[108:111], v[140:143]\n"
    "ds_read_b64_tr_b16 a[70:71], v13 offset:18432\n"
    "v_mov_b32 v56, v40\n"
    "s_nop 1\n"
    "v_permlane32_swap_b32 v40, v56\n"
    "v_mfma_f32_16x16x32_bf16 v[132:135], a[132:135], v[104:107], v[132:135]\n"
    "ds_read_b64_tr_b16 a[72:73], v12 offset:20480\n"
    "ds_read_b64_tr_b16 a[74:75], v12 offset:22528\n"
    "v_add_f32 v40, v40, v56\n"
    "v_mfma_f32_16x16x32_bf16 v[144:147], a[136:139], v[104:107], v[144:147]\n"
    "ds_read_b64_tr_b16 a[76:77], v13 offset:20480\n"
    "ds_read_b64_tr_b16 a[78:79], v13 offset:22528\n"
    "buffer_load_dwordx4 v19, s[52:55], s14 offen offset:1024 lds\n"
    "v_mfma_f32_16x16x32_bf16 v[152:155], a[136:139], v[108:111], v[152:155]\n"
    "ds_read_b64_tr_b16 a[80:81], v12 offset:24576\n"
    "ds_read_b64_tr_b16 a[82:83], v12 offset:26624\n"
    "v_mov_b32 v56, v40\n"
    "s_nop 1\n"
    "v_permlane16_swap_b32 v40, v56\n"
    "v_mfma_f32_16x16x32_bf16 v[156:159], a[140:143], v[108:111], v[156:159]\n"
    "ds_read_b64_tr_b16 a[84:85], v13 offset:24576\n"
    "v_add_f32 v40, v40, v56\n"
    "v_mfma_f32_16x16x32_bf16 v[148:151], a[140:143], v[104:107], v[148:151]\n"
    "ds_read_b64_tr_b16 a[86:87], v13 offset:26624\n"
    "v_cvt_pk_bf16_f32 v128, v128, v129\n"
    "v_mfma_f32_16x16x32_bf16 v[160:163], a[144:147], v[104:107], v[160:163]\n"
    "buffer_load_dwordx4 v20, s[52:55], s14 offen offset:2048 lds\n"
    "v_mfma_f32_16x16x32_bf16 v[168:171], a[144:147], v[108:111], v[168:171]\n"
    "ds_read_b64_tr_b16 a[88:89], v12 offset:28672\n"
    "ds_read_b64_tr_b16 a[90:91], v12 offset:30720\n"
    "v_cvt_pk_bf16_f32 v129, v130, v131\n"
    "v_mfma_f32_16x16x32_bf16 v[172:175], a[148:151], v[108:111], v[172:175]\n"
    "ds_read_b64_tr_b16 a[92:93], v13 offset:28672\n"
    "ds_read_b64_tr_b16 a[94:95], v13 offset:30720\n"
    "v_cvt_pk_bf16_f32 v130, v132, v133\n"
    "v_cvt_pk_bf16_f32 v131, v134, v135\n"
    "v_mfma_f32_16x16x32_bf16 v[164:167], a[148:151], v[104:107], v[164:167]\n"
    "ds_read_b64_tr_b16 a[96:97], v12 offset:0\n"
    "ds_read_b64_tr_b16 a[98:99], v12 offset:2048\n"
    "v_cvt_pk_bf16_f32 v136, v136, v137\n"
    "v_mfma_f32_16x16x32_bf16 v[176:179], a[152:155], v[104:107], v[176:179]\n"
    "buffer_load_dwordx4 v21, s[52:55], s14 offen offset:3072 lds\n"
    "v_mfma_f32_16x16x32_bf16 v[184:187], a[152:155], v[108:111], v[184:187]\n"
    "ds_read_b64_tr_b16 a[100:101], v13 offset:0\n"
    "v_cvt_pk_bf16_f32 v137, v138, v139\n"
    "v_mfma_f32_16x16x32_bf16 v[188:191], a[156:159], v[108:111], v[188:191]\n"
    "ds_read_b64_tr_b16 a[102:103], v13 offset:2048\n"
    "ds_read_b64_tr_b16 a[104:105], v12 offset:4096\n"
    "s_add_u32 m0, s62, 0x1c000\n"
    "v_cvt_pk_bf16_f32 v138, v140, v141\n"
    "v_mfma_f32_16x16x32_bf16 v[180:183], a[156:159], v[104:107], v[180:183]\n"
    "ds_read_b64_tr_b16 a[106:107], v12 offset:6144\n"
    "v_cvt_pk_bf16_f32 v139, v142, v143\n"
    "v_rcp_f32 v46, v40\n"
    "s_nop 0\n"
    "v_mfma_f32_16x16x32_bf16 v[192:195], a[160:163], v[104:107], v[192:195]\n"
    "ds_read_b64_tr_b16 a[108:109], v13 offset:4096\n"
    "buffer_load_dwordx4 v18, s[52:55], s15 offen lds\n"
    "v_cvt_pk_bf16_f32 v144, v144, v145\n"
    "v_mfma_f32_16x16x32_bf16 v[200:203], a[160:163], v[108:111], v[200:203]\n"
    "ds_read_b64_tr_b16 a[110:111], v13 offset:6144\n"
    "ds_read_b64_tr_b16 a[112:113], v12 offset:8192\n"
    "v_cvt_pk_bf16_f32 v145, v146, v147\n"
    "v_mfma_f32_16x16x32_bf16 v[204:207], a[164:167], v[108:111], v[204:207]\n"
    "ds_read_b64_tr_b16 a[114:115], v12 offset:10240\n"
    "ds_read_b64_tr_b16 a[116:117], v13 offset:8192\n"
    "v_cvt_pk_bf16_f32 v146, v148, v149\n"
    "v_cvt_pk_bf16_f32 v147, v150, v151\n"
    "v_cvt_pk_bf16_f32 v152, v152, v153\n"
    "v_cvt_pk_bf16_f32 v153, v154, v155\n"
    "v_mfma_f32_16x16x32_bf16 v[196:199], a[164:167], v[104:107], v[196:199]\n"
    "ds_read_b64_tr_b16 a[118:119], v13 offset:10240\n"
    "ds_read_b64_tr_b16 a[120:121], v12 offset:12288\n"
    "v_cvt_pk_bf16_f32 v154, v156, v157\n"
    "v_cvt_pk_bf16_f32 v155, v158, v159\n"
    "v_mfma_f32_16x16x32_bf16 v[208:211], a[168:171], v[104:107], v[208:211]\n"
    "ds_read_b64_tr_b16 a[122:123], v12 offset:14336\n"
    "buffer_load_dwordx4 v19, s[52:55], s15 offen offset:1024 lds\n"
    "v_cvt_pk_bf16_f32 v160, v160, v161\n"
    "v_mfma_f32_16x16x32_bf16 v[216:219], a[168:171], v[108:111], v[216:219]\n"
    "ds_read_b64_tr_b16 a[124:125], v13 offset:12288\n"
    "v_cvt_pk_bf16_f32 v161, v162, v163\n"
    "v_mfma_f32_16x16x32_bf16 v[220:223], a[172:175], v[108:111], v[220:223]\n"
    "ds_read_b64_tr_b16 a[126:127], v13 offset:14336\n"
    "v_cvt_pk_bf16_f32 v162, v164, v165\n"
    "v_mfma_f32_16x16x32_bf16 v[212:215], a[172:175], v[104:107], v[212:215]\n"
    "ds_read_b64_tr_b16 a[128:129], v12 offset:16896\n"
    "ds_read_b64_tr_b16 a[130:131], v12 offset:18944\n"
    "v_cvt_pk_bf16_f32 v163, v166, v167\n"
    "v_cvt_pk_bf16_f32 v168, v168, v169\n"
    "v_cvt_pk_bf16_f32 v169, v170, v171\n"
    "v_mfma_f32_16x16x32_bf16 v[224:227], a[176:179], v[104:107], v[224:227]\n"
    "ds_read_b64_tr_b16 a[132:133], v13 offset:16896\n"
    "buffer_load_dwordx4 v20, s[52:55], s15 offen offset:2048 lds\n"
    "v_cvt_pk_bf16_f32 v170, v172, v173\n"
    "v_mfma_f32_16x16x32_bf16 v[232:235], a[176:179], v[108:111], v[232:235]\n"
    "ds_read_b64_tr_b16 a[134:135], v13 offset:18944\n"
    "ds_read_b64_tr_b16 a[136:137], v12 offset:20992\n"
    "v_cvt_pk_bf16_f32 v171, v174, v175\n"
    "v_mfma_f32_16x16x32_bf16 v[236:239], a[180:183], v[108:111], v[236:239]\n"
    "ds_read_b64_tr_b16 a[138:139], v12 offset:23040\n"
    "ds_read_b64_tr_b16 a[140:141], v13 offset:20992\n"
    "v_cvt_pk_bf16_f32 v176, v176, v177\n"
    "v_mfma_f32_16x16x32_bf16 v[228:231], a[180:183], v[104:107], v[228:231]\n"
    "ds_read_b64_tr_b16 a[142:143], v13 offset:23040\n"
    "v_cvt_pk_bf16_f32 v177, v178, v179\n"
    "v_mfma_f32_16x16x32_bf16 v[240:243], a[184:187], v[104:107], v[240:243]\n"
    "buffer_load_dwordx4 v21, s[52:55], s15 offen offset:3072 lds\n"
    "v_cvt_pk_bf16_f32 v178, v180, v181\n"
    "v_cvt_pk_bf16_f32 v179, v182, v183\n"
    "v_mfma_f32_16x16x32_bf16 v[248:251], a[184:187], v[108:111], v[248:251]\n"
    "v_cvt_pk_bf16_f32 v184, v184, v185\n"
    "v_cvt_pk_bf16_f32 v185, v186, v187\n"
    "v_cvt_pk_bf16_f32 v186, v188, v189\n"
    "v_cvt_pk_bf16_f32 v187, v190, v191\n"
    "v_mfma_f32_16x16x32_bf16 v[252:255], a[188:191], v[108:111], v[252:255]\n"
    "v_cvt_pk_bf16_f32 v192, v192, v193\n"
    "v_cvt_pk_bf16_f32 v193, v194, v195\n"
    "v_mfma_f32_16x16x32_bf16 v[244:247], a[188:191], v[104:107], v[244:247]\n"
    "v_cvt_pk_bf16_f32 v194, v196, v197\n"
    "v_cvt_pk_bf16_f32 v195, v198, v199\n"
    "v_cvt_pk_bf16_f32 v200, v200, v201\n"
    "v_cvt_pk_bf16_f32 v201, v202, v203\n"
    "v_cvt_pk_bf16_f32 v202, v204, v205\n"
    "v_cvt_pk_bf16_f32 v203, v206, v207\n"
    "L_os:\n"
    "s_waitcnt vmcnt(32) lgkmcnt(8)\n"
    "s_barrier\n"
    "v_mfma_f32_16x16x32_bf16 v[64:67], a[64:67], v[128:131], 0\n"
    "ds_read_b64_tr_b16 a[144:145], v12 offset:25088\n"
    "v_cvt_pk_bf16_f32 v208, v208, v209\n"
    "v_add_f32 v41, v41, v43\n"
    "v_mfma_f32_16x16x32_bf16 v[72:75], a[64:67], v[136:139], 0\n"
    "ds_read_b64_tr_b16 a[146:147], v12 offset:27136\n"
    "v_cvt_pk_bf16_f32 v209, v210, v211\n"
    "v_mfma_f32_16x16x32_bf16 v[76:79], a[68:71], v[136:139], 0\n"
    "ds_read_b64_tr_b16 a[148:149], v13 offset:25088\n"
    "v_cvt_pk_bf16_f32 v210, v212, v213\n"
    "v_mfma_f32_16x16x32_bf16 v[68:71], a[68:71], v[128:131], 0\n"
    "ds_read_b64_tr_b16 a[150:151], v13 offset:27136\n"
    "ds_read_b64_tr_b16 a[152:153], v12 offset:29184\n"
    "v_cvt_pk_bf16_f32 v211, v214, v215\n"
    "v_mfma_f32_16x16x32_bf16 v[64:67], a[72:75], v[144:147], v[64:67]\n"
    "ds_read_b64_tr_b16 a[154:155], v12 offset:31232\n"
    "v_cvt_pk_bf16_f32 v216, v216, v217\n"
    "v_mfma_f32_16x16x32_bf16 v[72:75], a[72:75], v[152:155], v[72:75]\n"
    "ds_read_b64_tr_b16 a[156:157], v13 offset:29184\n"
    "v_cvt_pk_bf16_f32 v217, v218, v219\n"
    "v_mfma_f32_16x16x32_bf16 v[76:79], a[76:79], v[152:155], v[76:79]\n"
    "ds_read_b64_tr_b16 a[158:159], v13 offset:31232\n"
    "ds_read_b64_tr_b16 a[160:161], v12 offset:512\n"
    "v_cvt_pk_bf16_f32 v218, v220, v221\n"
    "v_mfma_f32_16x16x32_bf16 v[68:71], a[76:79], v[144:147], v[68:71]\n"
    "ds_read_b64_tr_b16 a[162:163], v12 offset:2560\n"
    "v_cvt_pk_bf16_f32 v219, v222, v223\n"
    "v_mfma_f32_16x16x32_bf16 v[64:67], a[80:83], v[160:163], v[64:67]\n"
    "ds_read_b64_tr_b16 a[164:165], v13 offset:512\n"
    "v_cvt_pk_bf16_f32 v224, v224, v225\n"
    "v_mfma_f32_16x16x32_bf16 v[72:75], a[80:83], v[168:171], v[72:75]\n"
    "ds_read_b64_tr_b16 a[166:167], v13 offset:2560\n"
    "ds_read_b64_tr_b16 a[168:169], v12 offset:4608\n"
    "v_cvt_pk_bf16_f32 v225, v226, v227\n"
    "v_mfma_f32_16x16x32_bf16 v[76:79], a[84:87], v[168:171], v[76:79]\n"
    "ds_read_b64_tr_b16 a[170:171], v12 offset:6656\n"
    "v_cvt_pk_bf16_f32 v226, v228, v229\n"
    "v_mfma_f32_16x16x32_bf16 v[68:71], a[84:87], v[160:163], v[68:71]\n"
    "ds_read_b64_tr_b16 a[172:173], v13 offset:4608\n"
    "v_cvt_pk_bf16_f32 v227, v230, v231\n"
    "v_mfma_f32_16x16x32_bf16 v[64:67], a[88:91], v[176:179], v[64:67]\n"
    "ds_read_b64_tr_b16 a[174:175], v13 offset:6656\n"
    "ds_read_b64_tr_b16 a[176:177], v12 offset:8704\n"
    "v_cvt_pk_bf16_f32 v232, v232, v233\n"
    "v_mfma_f32_16x16x32_bf16 v[72:75], a[88:91], v[184:187], v[72:75]\n"
    "ds_read_b64_tr_b16 a[178:179], v12 offset:10752\n"
    "v_cvt_pk_bf16_f32 v233, v234, v235\n"
    "v_mfma_f32_16x16x32_bf16 v[76:79], a[92:95], v[184:187], v[76:79]\n"
    "ds_read_b64_tr_b16 a[180:181], v13 offset:8704\n"
    "v_cvt_pk_bf16_f32 v234, v236, v237\n"
    "v_mfma_f32_16x16x32_bf16 v[68:71], a[92:95], v[176:179], v[68:71]\n"
    "ds_read_b64_tr_b16 a[182:183], v13 offset:10752\n"
    "v_cvt_pk_bf16_f32 v235, v238, v239\n"
    "v_mfma_f32_16x16x32_bf16 v[64:67], a[96:99], v[192:195], v[64:67]\n"
    "ds_read_b64_tr_b16 a[184:185], v12 offset:12800\n"
    "ds_read_b64_tr_b16 a[186:187], v12 offset:14848\n"
    "v_cvt_pk_bf16_f32 v240, v240, v241\n"
    "v_mfma_f32_16x16x32_bf16 v[72:75], a[96:99], v[200:203], v[72:75]\n"
    "ds_read_b64_tr_b16 a[188:189], v13 offset:12800\n"
    "v_cvt_pk_bf16_f32 v241, v242, v243\n"
    "v_mfma_f32_16x16x32_bf16 v[76:79], a[100:103], v[200:203], v[76:79]\n"
    "ds_read_b64_tr_b16 a[190:191], v13 offset:14848\n"
    "v_cvt_pk_bf16_f32 v242, v244, v245\n"
    "v_mfma_f32_16x16x32_bf16 v[68:71], a[100:103], v[192:195], v[68:71]\n"
    "ds_read_b64_tr_b16 a[64:65], v8 offset:0\n"
    "ds_read_b64_tr_b16 a[66:67], v8 offset:2048\n"
    "v_cvt_pk_bf16_f32 v243, v246, v247\n"
    "v_mfma_f32_16x16x32_bf16 v[64:67], a[104:107], v[208:211], v[64:67]\n"
    "ds_read_b64_tr_b16 a[68:69], v9 offset:0\n"
    "v_cvt_pk_bf16_f32 v248, v248, v249\n"
    "v_mfma_f32_16x16x32_bf16 v[72:75], a[104:107], v[216:219], v[72:75]\n"
    "ds_read_b64_tr_b16 a[70:71], v9 offset:2048\n"
    "v_cvt_pk_bf16_f32 v249, v250, v251\n"
    "v_mfma_f32_16x16x32_bf16 v[76:79], a[108:111], v[216:219], v[76:79]\n"
    "ds_read_b64_tr_b16 a[72:73], v8 offset:4096\n"
    "ds_read_b64_tr_b16 a[74:75], v8 offset:6144\n"
    "v_cvt_pk_bf16_f32 v250, v252, v253\n"
    "v_mfma_f32_16x16x32_bf16 v[68:71], a[108:111], v[208:211], v[68:71]\n"
    "ds_read_b64_tr_b16 a[76:77], v9 offset:4096\n"
    "v_cvt_pk_bf16_f32 v251, v254, v255\n"
    "v_mfma_f32_16x16x32_bf16 v[64:67], a[112:115], v[224:227], v[64:67]\n"
    "ds_read_b64_tr_b16 a[78:79], v9 offset:6144\n"
    "v_mfma_f32_16x16x32_bf16 v[72:75], a[112:115], v[232:235], v[72:75]\n"
    "buffer_load_dword v63, v29, s[20:23], 0 offen sc1\n"
    "v_mfma_f32_16x16x32_bf16 v[76:79], a[116:119], v[232:235], v[76:79]\n"
    "v_mov_b32 v57, v41\n"
    "s_nop 1\n"
    "v_permlane32_swap_b32 v41, v57\n"
    "v_mfma_f32_16x16x32_bf16 v[68:71], a[116:119], v[224:227], v[68:71]\n"
    "v_add_f32 v41, v41, v57\n"
    "v_mfma_f32_16x16x32_bf16 v[64:67], a[120:123], v[240:243], v[64:67]\n"
    "v_mov_b32 v57, v41\n"
    "s_nop 1\n"
    "v_permlane16_swap_b32 v41, v57\n"
    "v_mfma_f32_16x16x32_bf16 v[72:75], a[120:123], v[248:251], v[72:75]\n"
    "v_add_f32 v41, v41, v57\n"
    "v_mfma_f32_16x16x32_bf16 v[76:79], a[124:127], v[248:251], v[76:79]\n"
    "v_rcp_f32 v47, v41\n"
    "s_nop 0\n"
    "v_mfma_f32_16x16x32_bf16 v[68:71], a[124:127], v[240:243], v[68:71]\n"
    "s_waitcnt vmcnt(21) lgkmcnt(8)\n"
    "s_barrier\n"
    "v_mfma_f32_16x16x32_bf16 v[80:83], a[128:131], v[128:131], 0\n"
    "ds_read_b64_tr_b16 a[80:81], v8 offset:8192\n"
    "s_cmp_eq_u32 s72, 0\n"
    "s_cselect_b64 s[74:75], -1, 0\n"
    "v_mfma_f32_16x16x32_bf16 v[88:91], a[128:131], v[136:139], 0\n"
    "ds_read_b64_tr_b16 a[82:83], v8 offset:10240\n"
    "v_cndmask_b32_e64 v14, v45, v15, s[74:75]\n"
    "v_mfma_f32_16x16x32_bf16 v[92:95], a[132:135], v[136:139], 0\n"
    "ds_read_b64_tr_b16 a[84:85], v9 offset:8192\n"
    "s_add_u32 m0, s62, 0x24000\n"
    "v_mfma_f32_16x16x32_bf16 v[84:87], a[132:135], v[128:131], 0\n"
    "buffer_load_dwordx4 v34, s[52:55], s16 offen lds\n"
    "v_mfma_f32_16x16x32_bf16 v[80:83], a[136:139], v[144:147], v[80:83]\n"
    "ds_read_b64_tr_b16 a[86:87], v9 offset:10240\n"
    "ds_read_b64_tr_b16 a[88:89], v8 offset:12288\n"
    "v_mul_f32 v64, v64, v46\n"
    "v_mfma_f32_16x16x32_bf16 v[88:91], a[136:139], v[152:155], v[88:91]\n"
    "ds_read_b64_tr_b16 a[90:91], v8 offset:14336\n"
    "v_mul_f32 v65, v65, v46\n"
    "v_mfma_f32_16x16x32_bf16 v[92:95], a[140:143], v[152:155], v[92:95]\n"
    "ds_read_b64_tr_b16 a[92:93], v9 offset:12288\n"
    "ds_read_b64_tr_b16 a[94:95], v9 offset:14336\n"
    "v_mul_f32 v66, v66, v46\n"
    "v_mfma_f32_16x16x32_bf16 v[84:87], a[140:143], v[144:147], v[84:87]\n"
    "ds_read_b64_tr_b16 a[96:97], v10 offset:0\n"
    "v_mul_f32 v67, v67, v46\n"
    "v_mfma_f32_16x16x32_bf16 v[80:83], a[144:147], v[160:163], v[80:83]\n"
    "ds_read_b64_tr_b16 a[98:99], v10 offset:2048\n"
    "buffer_load_dwordx4 v35, s[52:55], s16 offen offset:1024 lds\n"
    "v_mfma_f32_16x16x32_bf16 v[88:91], a[144:147], v[168:171], v[88:91]\n"
    "ds_read_b64_tr_b16 a[100:101], v11 offset:0\n"
    "v_mul_f32 v68, v68, v46\n"
    "v_mfma_f32_16x16x32_bf16 v[92:95], a[148:151], v[168:171], v[92:95]\n"
    "ds_read_b64_tr_b16 a[102:103], v11 offset:2048\n"
    "ds_read_b64_tr_b16 a[104:105], v10 offset:4096\n"
    "v_mul_f32 v69, v69, v46\n"
    "v_mfma_f32_16x16x32_bf16 v[84:87], a[148:151], v[160:163], v[84:87]\n"
    "ds_read_b64_tr_b16 a[106:107], v10 offset:6144\n"
    "v_mul_f32 v70, v70, v46\n"
    "v_mfma_f32_16x16x32_bf16 v[80:83], a[152:155], v[176:179], v[80:83]\n"
    "ds_read_b64_tr_b16 a[108:109], v11 offset:4096\n"
    "v_mul_f32 v71, v71, v46\n"
    "v_cvt_pk_bf16_f32 v64, v64, v65\n"
    "v_mfma_f32_16x16x32_bf16 v[88:91], a[152:155], v[184:187], v[88:91]\n"
    "ds_read_b64_tr_b16 a[110:111], v11 offset:6144\n"
    "v_cvt_pk_bf16_f32 v65, v66, v67\n"
    "v_mfma_f32_16x16x32_bf16 v[92:95], a[156:159], v[184:187], v[92:95]\n"
    "ds_read_b64_tr_b16 a[112:113], v10 offset:8192\n"
    "ds_read_b64_tr_b16 a[114:115], v10 offset:10240\n"
    "v_cvt_pk_bf16_f32 v66, v68, v69\n"
    "v_mfma_f32_16x16x32_bf16 v[84:87], a[156:159], v[176:179], v[84:87]\n"
    "ds_read_b64_tr_b16 a[116:117], v11 offset:8192\n"
    "v_cvt_pk_bf16_f32 v67, v70, v71\n"
    "v_mfma_f32_16x16x32_bf16 v[80:83], a[160:163], v[192:195], v[80:83]\n"
    "ds_read_b64_tr_b16 a[118:119], v11 offset:10240\n"
    "s_mov_b32 s68, 0\n"
    "v_mfma_f32_16x16x32_bf16 v[88:91], a[160:163], v[200:203], v[88:91]\n"
    "ds_read_b64_tr_b16 a[120:121], v10 offset:12288\n"
    "ds_read_b64_tr_b16 a[122:123], v10 offset:14336\n"
    "v_cndmask_b32_e64 v22, v22, v64, s[74:75]\n"
    "v_mfma_f32_16x16x32_bf16 v[92:95], a[164:167], v[200:203], v[92:95]\n"
    "buffer_load_dwordx4 v36, s[52:55], s16 offen offset:2048 lds\n"
    "v_mfma_f32_16x16x32_bf16 v[84:87], a[164:167], v[192:195], v[84:87]\n"
    "ds_read_b64_tr_b16 a[124:125], v11 offset:12288\n"
    "ds_read_b64_tr_b16 a[126:127], v11 offset:14336\n"
    "v_cndmask_b32_e64 v23, v23, v65, s[74:75]\n"
    "v_mfma_f32_16x16x32_bf16 v[80:83], a[168:171], v[208:211], v[80:83]\n"
    "ds_read_b64_tr_b16 a[128:129], v8 offset:512\n"
    "v_cndmask_b32_e64 v24, v24, v66, s[74:75]\n"
    "L_iw5:\n"
    "s_cbranch_execz L_iwback\n"
    "v_mfma_f32_16x16x32_bf16 v[88:91], a[168:171], v[216:219], v[88:91]\n"
    "ds_read_b64_tr_b16 a[130:131], v8 offset:2560\n"
    "ds_read_b64_tr_b16 a[132:133], v9 offset:512\n"
    "v_cndmask_b32_e64 v25, v25, v67, s[74:75]\n"
    "v_mfma_f32_16x16x32_bf16 v[92:95], a[172:175], v[216:219], v[92:95]\n"
    "buffer_store_dwordx4 v[64:67], v14, s[56:59], s68 offen sc1\n"
    "v_mfma_f32_16x16x32_bf16 v[84:87], a[172:175], v[208:211], v[84:87]\n"
    "ds_read_b64_tr_b16 a[134:135], v9 offset:2560\n"
    "v_mul_f32 v72, v72, v47\n"
    "v_mul_f32 v73, v73, v47\n"
    "v_mfma_f32_16x16x32_bf16 v[80:83], a[176:179], v[224:227], v[80:83]\n"
    "ds_read_b64_tr_b16 a[136:137], v8 offset:4608\n"
    "v_mul_f32 v74, v74, v47\n"
    "v_mfma_f32_16x16x32_bf16 v[88:91], a[176:179], v[232:235], v[88:91]\n"
    "ds_read_b64_tr_b16 a[138:139], v8 offset:6656\n"
    "v_mul_f32 v75, v75, v47\n"
    "v_mul_f32 v76, v76, v47\n"
    "v_mfma_f32_16x16x32_bf16 v[92:95], a[180:183], v[232:235], v[92:95]\n"
    "buffer_load_dwordx4 v37, s[52:55], s16 offen offset:3072 lds\n"
    "v_mfma_f32_16x16x32_bf16 v[84:87], a[180:183], v[224:227], v[84:87]\n"
    "ds_read_b64_tr_b16 a[140:141], v9 offset:4608\n"
    "v_mul_f32 v77, v77, v47\n"
    "v_mul_f32 v78, v78, v47\n"
    "v_mfma_f32_16x16x32_bf16 v[80:83], a[184:187], v[240:243], v[80:83]\n"
    "ds_read_b64_tr_b16 a[142:143], v9 offset:6656\n"
    "v_mul_f32 v79, v79, v47\n"
    "v_mfma_f32_16x16x32_bf16 v[88:91], a[184:187], v[248:251], v[88:91]\n"
    "v_cvt_pk_bf16_f32 v72, v72, v73\n"
    "v_cvt_pk_bf16_f32 v73, v74, v75\n"
    "v_mfma_f32_16x16x32_bf16 v[92:95], a[188:191], v[248:251], v[92:95]\n"
    "v_cvt_pk_bf16_f32 v74, v76, v77\n"
    "v_cvt_pk_bf16_f32 v75, v78, v79\n"
    "v_mfma_f32_16x16x32_bf16 v[84:87], a[188:191], v[240:243], v[84:87]\n"
    "buffer_store_dwordx4 v[72:75], v45, s[56:59], s68 offen offset:1024 sc1\n"
    "s_waitcnt vmcnt(31) lgkmcnt(8)\n"
    "s_barrier\n"
    "v_mfma_f32_16x16x32_bf16 v[96:99], a[64:67], v[128:131], 0\n"
    "ds_read_b64_tr_b16 a[144:145], v8 offset:8704\n"
    "ds_read_b64_tr_b16 a[146:147], v8 offset:10752\n"
    "s_add_u32 m0, s62, 0x20000\n"
    "v_mfma_f32_16x16x32_bf16 v[104:107], a[64:67], v[136:139], 0\n"
    "ds_read_b64_tr_b16 a[148:149], v9 offset:8704\n"
    "ds_read_b64_tr_b16 a[150:151], v9 offset:10752\n"
    "v_mfma_f32_16x16x32_bf16 v[108:111], a[68:71], v[136:139], 0\n"
    "buffer_load_dwordx4 v18, s[52:55], s16 offen lds\n"
    "v_mfma_f32_16x16x32_bf16 v[100:103], a[68:71], v[128:131], 0\n"
    "ds_read_b64_tr_b16 a[152:153], v8 offset:12800\n"
    "ds_read_b64_tr_b16 a[154:155], v8 offset:14848\n"
    "v_mfma_f32_16x16x32_bf16 v[96:99], a[72:75], v[144:147], v[96:99]\n"
    "ds_read_b64_tr_b16 a[156:157], v9 offset:12800\n"
    "v_mul_f32 v80, v80, v46\n"
    "v_mfma_f32_16x16x32_bf16 v[104:107], a[72:75], v[152:155], v[104:107]\n"
    "ds_read_b64_tr_b16 a[158:159], v9 offset:14848\n"
    "ds_read_b64_tr_b16 a[160:161], v10 offset:512\n"
    "v_mul_f32 v81, v81, v46\n"
    "v_mfma_f32_16x16x32_bf16 v[108:111], a[76:79], v[152:155], v[108:111]\n"
    "ds_read_b64_tr_b16 a[162:163], v10 offset:2560\n"
    "v_mul_f32 v82, v82, v46\n"
    "v_mfma_f32_16x16x32_bf16 v[100:103], a[76:79], v[144:147], v[100:103]\n"
    "ds_read_b64_tr_b16 a[164:165], v11 offset:512\n"
    "v_mul_f32 v83, v83, v46\n"
    "v_mfma_f32_16x16x32_bf16 v[96:99], a[80:83], v[160:163], v[96:99]\n"
    "buffer_load_dwordx4 v19, s[52:55], s16 offen offset:1024 lds\n"
    "v_mfma_f32_16x16x32_bf16 v[104:107], a[80:83], v[168:171], v[104:107]\n"
    "ds_read_b64_tr_b16 a[166:167], v11 offset:2560\n"
    "v_mul_f32 v84, v84, v46\n"
    "v_mfma_f32_16x16x32_bf16 v[108:111], a[84:87], v[168:171], v[108:111]\n"
    "ds_read_b64_tr_b16 a[168:169], v10 offset:4608\n"
    "ds_read_b64_tr_b16 a[170:171], v10 offset:6656\n"
    "v_mul_f32 v85, v85, v46\n"
    "v_mfma_f32_16x16x32_bf16 v[100:103], a[84:87], v[160:163], v[100:103]\n"
    "ds_read_b64_tr_b16 a[172:173], v11 offset:4608\n"
    "v_mul_f32 v86, v86, v46\n"
    "v_mfma_f32_16x16x32_bf16 v[96:99], a[88:91], v[176:179], v[96:99]\n"
    "ds_read_b64_tr_b16 a[174:175], v11 offset:6656\n"
    "v_mul_f32 v87, v87, v46\n"
    "v_mfma_f32_16x16x32_bf16 v[104:107], a[88:91], v[184:187], v[104:107]\n"
    "ds_read_b64_tr_b16 a[176:177], v10 offset:8704\n"
    "v_cvt_pk_bf16_f32 v80, v80, v81\n"
    "v_mfma_f32_16x16x32_bf16 v[108:111], a[92:95], v[184:187], v[108:111]\n"
    "ds_read_b64_tr_b16 a[178:179], v10 offset:10752\n"
    "v_cvt_pk_bf16_f32 v81, v82, v83\n"
    "v_mfma_f32_16x16x32_bf16 v[100:103], a[92:95], v[176:179], v[100:103]\n"
    "ds_read_b64_tr_b16 a[180:181], v11 offset:8704\n"
    "v_cvt_pk_bf16_f32 v82, v84, v85\n"
    "v_mfma_f32_16x16x32_bf16 v[96:99], a[96:99], v[192:195], v[96:99]\n"
    "ds_read_b64_tr_b16 a[182:183], v11 offset:10752\n"
    "buffer_load_dwordx4 v20, s[52:55], s16 offen offset:2048 lds\n"
    "v_mfma_f32_16x16x32_bf16 v[104:107], a[96:99], v[200:203], v[104:107]\n"
    "ds_read_b64_tr_b16 a[184:185], v10 offset:12800\n"
    "v_cvt_pk_bf16_f32 v83, v86, v87\n"
    "v_mfma_f32_16x16x32_bf16 v[108:111], a[100:103], v[200:203], v[108:111]\n"
    "ds_read_b64_tr_b16 a[186:187], v10 offset:14848\n"
    "s_mov_b32 s68, 0\n"
    "v_mfma_f32_16x16x32_bf16 v[100:103], a[100:103], v[192:195], v[100:103]\n"
    "ds_read_b64_tr_b16 a[188:189], v11 offset:12800\n"
    "ds_read_b64_tr_b16 a[190:191], v11 offset:14848\n"
    "v_mfma_f32_16x16x32_bf16 v[96:99], a[104:107], v[208:211], v[96:99]\n"
    "buffer_store_dwordx4 v[80:83], v45, s[56:59], s68 offen offset:2048 sc1\n"
    "v_mfma_f32_16x16x32_bf16 v[104:107], a[104:107], v[216:219], v[104:107]\n"
    "ds_read_b64_tr_b16 a[64:65], v8 offset:16384\n"
    "ds_read_b64_tr_b16 a[66:67], v8 offset:18432\n"
    "v_mul_f32 v88, v88, v47\n"
    "v_mfma_f32_16x16x32_bf16 v[108:111], a[108:111], v[216:219], v[108:111]\n"
    "ds_read_b64_tr_b16 a[68:69], v9 offset:16384\n"
    "v_mul_f32 v89, v89, v47\n"
    "v_mfma_f32_16x16x32_bf16 v[100:103], a[108:111], v[208:211], v[100:103]\n"
    "ds_read_b64_tr_b16 a[70:71], v9 offset:18432\n"
    "v_mul_f32 v90, v90, v47\n"
    "v_mfma_f32_16x16x32_bf16 v[96:99], a[112:115], v[224:227], v[96:99]\n"
    "buffer_load_dwordx4 v21, s[52:55], s16 offen offset:3072 lds\n"
    "v_mfma_f32_16x16x32_bf16 v[104:107], a[112:115], v[232:235], v[104:107]\n"
    "ds_read_b64_tr_b16 a[72:73], v8 offset:20480\n"
    "v_mul_f32 v91, v91, v47\n"
    "v_mfma_f32_16x16x32_bf16 v[108:111], a[116:119], v[232:235], v[108:111]\n"
    "ds_read_b64_tr_b16 a[74:75], v8 offset:22528\n"
    "ds_read_b64_tr_b16 a[76:77], v9 offset:20480\n"
    "v_mul_f32 v92, v92, v47\n"
    "v_mfma_f32_16x16x32_bf16 v[100:103], a[116:119], v[224:227], v[100:103]\n"
    "ds_read_b64_tr_b16 a[78:79], v9 offset:22528\n"
    "v_mul_f32 v93, v93, v47\n"
    "v_mfma_f32_16x16x32_bf16 v[96:99], a[120:123], v[240:243], v[96:99]\n"
    "v_mul_f32 v94, v94, v47\n"
    "v_mul_f32 v95, v95, v47\n"
    "v_mfma_f32_16x16x32_bf16 v[104:107], a[120:123], v[248:251], v[104:107]\n"
    "v_cvt_pk_bf16_f32 v88, v88, v89\n"
    "v_cvt_pk_bf16_f32 v89, v90, v91\n"
    "v_mfma_f32_16x16x32_bf16 v[108:111], a[124:127], v[248:251], v[108:111]\n"
    "v_cvt_pk_bf16_f32 v90, v92, v93\n"
    "v_cvt_pk_bf16_f32 v91, v94, v95\n"
    "v_mfma_f32_16x16x32_bf16 v[100:103], a[124:127], v[240:243], v[100:103]\n"
    "buffer_store_dwordx4 v[88:91], v45, s[56:59], s68 offen offset:3072 sc1\n"
    "s_waitcnt vmcnt(13) lgkmcnt(8)\n"
    "s_barrier\n"
    "v_mfma_f32_16x16x32_bf16 v[112:115], a[128:131], v[128:131], 0\n"
    "ds_read_b64_tr_b16 a[80:81], v8 offset:24576\n"
    "s_cmp_eq_u32 s72, 1\n"
    "s_cselect_b64 s[74:75], -1, 0\n"
    "v_mfma_f32_16x16x32_bf16 v[120:123], a[128:131], v[136:139], 0\n"
    "ds_read_b64_tr_b16 a[82:83], v8 offset:26624\n"
    "v_cndmask_b32_e64 v14, v45, v15, s[74:75]\n"
    "v_mfma_f32_16x16x32_bf16 v[124:127], a[132:135], v[136:139], 0\n"
    "ds_read_b64_tr_b16 a[84:85], v9 offset:24576\n"
    "s_add_u32 m0, s62, 0\n"
    "v_mfma_f32_16x16x32_bf16 v[116:119], a[132:135], v[128:131], 0\n"
    "buffer_load_dwordx4 v34, s[52:55], s17 offen lds\n"
    "v_mfma_f32_16x16x32_bf16 v[112:115], a[136:139], v[144:147], v[112:115]\n"
    "ds_read_b64_tr_b16 a[86:87], v9 offset:26624\n"
    "ds_read_b64_tr_b16 a[88:89], v8 offset:28672\n"
    "v_mul_f32 v96, v96, v46\n"
    "v_mfma_f32_16x16x32_bf16 v[120:123], a[136:139], v[152:155], v[120:123]\n"
    "ds_read_b64_tr_b16 a[90:91], v8 offset:30720\n"
    "v_mul_f32 v97, v97, v46\n"
    "v_mfma_f32_16x16x32_bf16 v[124:127], a[140:143], v[152:155], v[124:127]\n"
    "ds_read_b64_tr_b16 a[92:93], v9 offset:28672\n"
    "ds_read_b64_tr_b16 a[94:95], v9 offset:30720\n"
    "v_mul_f32 v98, v98, v46\n"
    "v_mfma_f32_16x16x32_bf16 v[116:119], a[140:143], v[144:147], v[116:119]\n"
    "ds_read_b64_tr_b16 a[96:97], v10 offset:16384\n"
    "v_mul_f32 v99, v99, v46\n"
    "v_mfma_f32_16x16x32_bf16 v[112:115], a[144:147], v[160:163], v[112:115]\n"
    "ds_read_b64_tr_b16 a[98:99], v10 offset:18432\n"
    "buffer_load_dwordx4 v35, s[52:55], s17 offen offset:1024 lds\n"
    "v_mfma_f32_16x16x32_bf16 v[120:123], a[144:147], v[168:171], v[120:123]\n"
    "ds_read_b64_tr_b16 a[100:101], v11 offset:16384\n"
    "v_mul_f32 v100, v100, v46\n"
    "v_mfma_f32_16x16x32_bf16 v[124:127], a[148:151], v[168:171], v[124:127]\n"
    "ds_read_b64_tr_b16 a[102:103], v11 offset:18432\n"
    "ds_read_b64_tr_b16 a[104:105], v10 offset:20480\n"
    "v_mul_f32 v101, v101, v46\n"
    "v_mfma_f32_16x16x32_bf16 v[116:119], a[148:151], v[160:163], v[116:119]\n"
    "ds_read_b64_tr_b16 a[106:107], v10 offset:22528\n"
    "v_mul_f32 v102, v102, v46\n"
    "v_mfma_f32_16x16x32_bf16 v[112:115], a[152:155], v[176:179], v[112:115]\n"
    "ds_read_b64_tr_b16 a[108:109], v11 offset:20480\n"
    "v_mul_f32 v103, v103, v46\n"
    "v_cvt_pk_bf16_f32 v96, v96, v97\n"
    "v_mfma_f32_16x16x32_bf16 v[120:123], a[152:155], v[184:187], v[120:123]\n"
    "ds_read_b64_tr_b16 a[110:111], v11 offset:22528\n"
    "v_cvt_pk_bf16_f32 v97, v98, v99\n"
    "v_mfma_f32_16x16x32_bf16 v[124:127], a[156:159], v[184:187], v[124:127]\n"
    "ds_read_b64_tr_b16 a[112:113], v10 offset:24576\n"
    "ds_read_b64_tr_b16 a[114:115], v10 offset:26624\n"
    "v_cvt_pk_bf16_f32 v98, v100, v101\n"
    "v_mfma_f32_16x16x32_bf16 v[116:119], a[156:159], v[176:179], v[116:119]\n"
    "ds_read_b64_tr_b16 a[116:117], v11 offset:24576\n"
    "v_cvt_pk_bf16_f32 v99, v102, v103\n"
    "v_mfma_f32_16x16x32_bf16 v[112:115], a[160:163], v[192:195], v[112:115]\n"
    "ds_read_b64_tr_b16 a[118:119], v11 offset:26624\n"
    "s_mov_b32 s68, 0x4000\n"
    "v_mfma_f32_16x16x32_bf16 v[120:123], a[160:163], v[200:203], v[120:123]\n"
    "ds_read_b64_tr_b16 a[120:121], v10 offset:28672\n"
    "ds_read_b64_tr_b16 a[122:123], v10 offset:30720\n"
    "v_cndmask_b32_e64 v22, v22, v96, s[74:75]\n"
    "v_mfma_f32_16x16x32_bf16 v[124:127], a[164:167], v[200:203], v[124:127]\n"
    "buffer_load_dwordx4 v36, s[52:55], s17 offen offset:2048 lds\n"
    "v_mfma_f32_16x16x32_bf16 v[116:119], a[164:167], v[192:195], v[116:119]\n"
    "ds_read_b64_tr_b16 a[124:125], v11 offset:28672\n"
    "ds_read_b64_tr_b16 a[126:127], v11 offset:30720\n"
    "v_cndmask_b32_e64 v23, v23, v97, s[74:75]\n"
    "v_mfma_f32_16x16x32_bf16 v[112:115], a[168:171], v[208:211], v[112:115]\n"
    "ds_read_b64_tr_b16 a[128:129], v8 offset:16896\n"
    "v_cndmask_b32_e64 v24, v24, v98, s[74:75]\n"
    "v_mfma_f32_16x16x32_bf16 v[120:123], a[168:171], v[216:219], v[120:123]\n"
    "ds_read_b64_tr_b16 a[130:131], v8 offset:18944\n"
    "ds_read_b64_tr_b16 a[132:133], v9 offset:16896\n"
    "v_cndmask_b32_e64 v25, v25, v99, s[74:75]\n"
    "v_mfma_f32_16x16x32_bf16 v[124:127], a[172:175], v[216:219], v[124:127]\n"
    "buffer_store_dwordx4 v[96:99], v14, s[56:59], s68 offen sc1\n"
    "v_mfma_f32_16x16x32_bf16 v[116:119], a[172:175], v[208:211], v[116:119]\n"
    "ds_read_b64_tr_b16 a[134:135], v9 offset:18944\n"
    "v_mul_f32 v104, v104, v47\n"
    "v_mul_f32 v105, v105, v47\n"
    "v_mfma_f32_16x16x32_bf16 v[112:115], a[176:179], v[224:227], v[112:115]\n"
    "ds_read_b64_tr_b16 a[136:137], v8 offset:20992\n"
    "v_mul_f32 v106, v106, v47\n"
    "v_mfma_f32_16x16x32_bf16 v[120:123], a[176:179], v[232:235], v[120:123]\n"
    "ds_read_b64_tr_b16 a[138:139], v8 offset:23040\n"
    "v_mul_f32 v107, v107, v47\n"
    "v_mul_f32 v108, v108, v47\n"
    "v_mfma_f32_16x16x32_bf16 v[124:127], a[180:183], v[232:235], v[124:127]\n"
    "buffer_load_dwordx4 v37, s[52:55], s17 offen offset:3072 lds\n"
    "v_mfma_f32_16x16x32_bf16 v[116:119], a[180:183], v[224:227], v[116:119]\n"
    "ds_read_b64_tr_b16 a[140:141], v9 offset:20992\n"
    "v_mul_f32 v109, v109, v47\n"
    "v_mul_f32 v110, v110, v47\n"
    "v_mfma_f32_16x16x32_bf16 v[112:115], a[184:187], v[240:243], v[112:115]\n"
    "ds_read_b64_tr_b16 a[142:143], v9 offset:23040\n"
    "v_mul_f32 v111, v111, v47\n"
    "v_mfma_f32_16x16x32_bf16 v[120:123], a[184:187], v[248:251], v[120:123]\n"
    "v_cvt_pk_bf16_f32 v104, v104, v105\n"
    "v_cvt_pk_bf16_f32 v105, v106, v107\n"
    "v_mfma_f32_16x16x32_bf16 v[124:127], a[188:191], v[248:251], v[124:127]\n"
    "v_cvt_pk_bf16_f32 v106, v108, v109\n"
    "v_cvt_pk_bf16_f32 v107, v110, v111\n"
    "v_mfma_f32_16x16x32_bf16 v[116:119], a[188:191], v[240:243], v[116:119]\n"
    "buffer_store_dwordx4 v[104:107], v45, s[56:59], s68 offen offset:1024 sc1\n"
    "s_waitcnt lgkmcnt(8)\n"
    "v_mfma_f32_16x16x32_bf16 v[64:67], a[64:67], v[128:131], 0\n"
    "ds_read_b64_tr_b16 a[144:145], v8 offset:25088\n"
    "ds_read_b64_tr_b16 a[146:147], v8 offset:27136\n"
    "s_add_u32 m0, s62, 0x10000\n"
    "v_mfma_f32_16x16x32_bf16 v[72:75], a[64:67], v[136:139], 0\n"
    "ds_read_b64_tr_b16 a[148:149], v9 offset:25088\n"
    "ds_read_b64_tr_b16 a[150:151], v9 offset:27136\n"
    "v_mfma_f32_16x16x32_bf16 v[76:79], a[68:71], v[136:139], 0\n"
    "buffer_load_dwordx4 v18, s[52:55], s17 offen lds\n"
    "v_mfma_f32_16x16x32_bf16 v[68:71], a[68:71], v[128:131], 0\n"
    "ds_read_b64_tr_b16 a[152:153], v8 offset:29184\n"
    "ds_read_b64_tr_b16 a[154:155], v8 offset:31232\n"
    "v_mfma_f32_16x16x32_bf16 v[64:67], a[72:75], v[144:147], v[64:67]\n"
    "ds_read_b64_tr_b16 a[156:157], v9 offset:29184\n"
    "v_mul_f32 v112, v112, v46\n"
    "v_mfma_f32_16x16x32_bf16 v[72:75], a[72:75], v[152:155], v[72:75]\n"
    "ds_read_b64_tr_b16 a[158:159], v9 offset:31232\n"
    "ds_read_b64_tr_b16 a[160:161], v10 offset:16896\n"
    "v_mul_f32 v113, v113, v46\n"
    "v_mfma_f32_16x16x32_bf16 v[76:79], a[76:79], v[152:155], v[76:79]\n"
    "ds_read_b64_tr_b16 a[162:163], v10 offset:18944\n"
    "v_mul_f32 v114, v114, v46\n"
    "v_mfma_f32_16x16x32_bf16 v[68:71], a[76:79], v[144:147], v[68:71]\n"
    "ds_read_b64_tr_b16 a[164:165], v11 offset:16896\n"
    "v_mul_f32 v115, v115, v46\n"
    "v_mfma_f32_16x16x32_bf16 v[64:67], a[80:83], v[160:163], v[64:67]\n"
    "buffer_load_dwordx4 v19, s[52:55], s17 offen offset:1024 lds\n"
    "v_mfma_f32_16x16x32_bf16 v[72:75], a[80:83], v[168:171], v[72:75]\n"
    "ds_read_b64_tr_b16 a[166:167], v11 offset:18944\n"
    "v_mul_f32 v116, v116, v46\n"
    "v_mfma_f32_16x16x32_bf16 v[76:79], a[84:87], v[168:171], v[76:79]\n"
    "ds_read_b64_tr_b16 a[168:169], v10 offset:20992\n"
    "ds_read_b64_tr_b16 a[170:171], v10 offset:23040\n"
    "v_mul_f32 v117, v117, v46\n"
    "v_mfma_f32_16x16x32_bf16 v[68:71], a[84:87], v[160:163], v[68:71]\n"
    "ds_read_b64_tr_b16 a[172:173], v11 offset:20992\n"
    "v_mul_f32 v118, v118, v46\n"
    "v_mfma_f32_16x16x32_bf16 v[64:67], a[88:91], v[176:179], v[64:67]\n"
    "ds_read_b64_tr_b16 a[174:175], v11 offset:23040\n"
    "v_mul_f32 v119, v119, v46\n"
    "v_mfma_f32_16x16x32_bf16 v[72:75], a[88:91], v[184:187], v[72:75]\n"
    "ds_read_b64_tr_b16 a[176:177], v10 offset:25088\n"
    "v_cvt_pk_bf16_f32 v112, v112, v113\n"
    "v_mfma_f32_16x16x32_bf16 v[76:79], a[92:95], v[184:187], v[76:79]\n"
    "ds_read_b64_tr_b16 a[178:179], v10 offset:27136\n"
    "v_cvt_pk_bf16_f32 v113, v114, v115\n"
    "v_mfma_f32_16x16x32_bf16 v[68:71], a[92:95], v[176:179], v[68:71]\n"
    "ds_read_b64_tr_b16 a[180:181], v11 offset:25088\n"
    "v_cvt_pk_bf16_f32 v114, v116, v117\n"
    "v_mfma_f32_16x16x32_bf16 v[64:67], a[96:99], v[192:195], v[64:67]\n"
    "ds_read_b64_tr_b16 a[182:183], v11 offset:27136\n"
    "buffer_load_dwordx4 v20, s[52:55], s17 offen offset:2048 lds\n"
    "v_mfma_f32_16x16x32_bf16 v[72:75], a[96:99], v[200:203], v[72:75]\n"
    "ds_read_b64_tr_b16 a[184:185], v10 offset:29184\n"
    "v_cvt_pk_bf16_f32 v115, v118, v119\n"
    "v_mfma_f32_16x16x32_bf16 v[76:79], a[100:103], v[200:203], v[76:79]\n"
    "ds_read_b64_tr_b16 a[186:187], v10 offset:31232\n"
    "s_mov_b32 s68, 0x4000\n"
    "v_mfma_f32_16x16x32_bf16 v[68:71], a[100:103], v[192:195], v[68:71]\n"
    "ds_read_b64_tr_b16 a[188:189], v11 offset:29184\n"
    "ds_read_b64_tr_b16 a[190:191], v11 offset:31232\n"
    "v_mfma_f32_16x16x32_bf16 v[64:67], a[104:107], v[208:211], v[64:67]\n"
    "buffer_store_dwordx4 v[112:115], v45, s[56:59], s68 offen offset:2048 sc1\n"
    "v_mfma_f32_16x16x32_bf16 v[72:75], a[104:107], v[216:219], v[72:75]\n"
    "ds_read_b64_tr_b16 a[64:65], v8 offset:32768\n"
    "ds_read_b64_tr_b16 a[66:67], v8 offset:34816\n"
    "v_mul_f32 v120, v120, v47\n"
    "v_mfma_f32_16x16x32_bf16 v[76:79], a[108:111], v[216:219], v[76:79]\n"
    "ds_read_b64_tr_b16 a[68:69], v9 offset:32768\n"
    "v_mul_f32 v121, v121, v47\n"
    "v_mfma_f32_16x16x32_bf16 v[68:71], a[108:111], v[208:211], v[68:71]\n"
    "ds_read_b64_tr_b16 a[70:71], v9 offset:34816\n"
    "v_mul_f32 v122, v122, v47\n"
    "v_mfma_f32_16x16x32_bf16 v[64:67], a[112:115], v[224:227], v[64:67]\n"
    "buffer_load_dwordx4 v21, s[52:55], s17 offen offset:3072 lds\n"
    "v_mfma_f32_16x16x32_bf16 v[72:75], a[112:115], v[232:235], v[72:75]\n"
    "ds_read_b64_tr_b16 a[72:73], v8 offset:36864\n"
    "v_mul_f32 v123, v123, v47\n"
    "v_mfma_f32_16x16x32_bf16 v[76:79], a[116:119], v[232:235], v[76:79]\n"
    "ds_read_b64_tr_b16 a[74:75], v8 offset:38912\n"
    "ds_read_b64_tr_b16 a[76:77], v9 offset:36864\n"
    "v_mul_f32 v124, v124, v47\n"
    "v_mfma_f32_16x16x32_bf16 v[68:71], a[116:119], v[224:227], v[68:71]\n"
    "ds_read_b64_tr_b16 a[78:79], v9 offset:38912\n"
    "v_mul_f32 v125, v125, v47\n"
    "v_mfma_f32_16x16x32_bf16 v[64:67], a[120:123], v[240:243], v[64:67]\n"
    "v_mul_f32 v126, v126, v47\n"
    "v_mul_f32 v127, v127, v47\n"
    "v_mfma_f32_16x16x32_bf16 v[72:75], a[120:123], v[248:251], v[72:75]\n"
    "v_cvt_pk_bf16_f32 v120, v120, v121\n"
    "v_cvt_pk_bf16_f32 v121, v122, v123\n"
    "v_mfma_f32_16x16x32_bf16 v[76:79], a[124:127], v[248:251], v[76:79]\n"
    "v_cvt_pk_bf16_f32 v122, v124, v125\n"
    "v_cvt_pk_bf16_f32 v123, v126, v127\n"
    "v_mfma_f32_16x16x32_bf16 v[68:71], a[124:127], v[240:243], v[68:71]\n"
    "buffer_store_dwordx4 v[120:123], v45, s[56:59], s68 offen offset:3072 sc1\n"
    "s_waitcnt lgkmcnt(8)\n"
    "v_mfma_f32_16x16x32_bf16 v[80:83], a[128:131], v[128:131], 0\n"
    "ds_read_b64_tr_b16 a[80:81], v8 offset:40960\n"
    "s_cmp_eq_u32 s72, 2\n"
    "s_cselect_b64 s[74:75], -1, 0\n"
    "v_mfma_f32_16x16x32_bf16 v[88:91], a[128:131], v[136:139], 0\n"
    "ds_read_b64_tr_b16 a[82:83], v8 offset:43008\n"
    "v_cndmask_b32_e64 v14, v45, v15, s[74:75]\n"
    "v_mfma_f32_16x16x32_bf16 v[92:95], a[132:135], v[136:139], 0\n"
    "ds_read_b64_tr_b16 a[84:85], v9 offset:40960\n"
    "ds_read_b64_tr_b16 a[86:87], v9 offset:43008\n"
    "v_mfma_f32_16x16x32_bf16 v[84:87], a[132:135], v[128:131], 0\n"
    "ds_read_b64_tr_b16 a[88:89], v8 offset:45056\n"
    "ds_read_b64_tr_b16 a[90:91], v8 offset:47104\n"
    "v_mfma_f32_16x16x32_bf16 v[80:83], a[136:139], v[144:147], v[80:83]\n"
    "ds_read_b64_tr_b16 a[92:93], v9 offset:45056\n"
    "v_mul_f32 v64, v64, v46\n"
    "v_mfma_f32_16x16x32_bf16 v[88:91], a[136:139], v[152:155], v[88:91]\n"
    "ds_read_b64_tr_b16 a[94:95], v9 offset:47104\n"
    "v_mul_f32 v65, v65, v46\n"
    "v_mfma_f32_16x16x32_bf16 v[92:95], a[140:143], v[152:155], v[92:95]\n"
    "ds_read_b64_tr_b16 a[96:97], v10 offset:32768\n"
    "v_mul_f32 v66, v66, v46\n"
    "v_mfma_f32_16x16x32_bf16 v[84:87], a[140:143], v[144:147], v[84:87]\n"
    "ds_read_b64_tr_b16 a[98:99], v10 offset:34816\n"
    "v_mul_f32 v67, v67, v46\n"
    "v_mfma_f32_16x16x32_bf16 v[80:83], a[144:147], v[160:163], v[80:83]\n"
    "ds_read_b64_tr_b16 a[100:101], v11 offset:32768\n"
    "ds_read_b64_tr_b16 a[102:103], v11 offset:34816\n"
    "v_mul_f32 v68, v68, v46\n"
    "v_mfma_f32_16x16x32_bf16 v[88:91], a[144:147], v[168:171], v[88:91]\n"
    "ds_read_b64_tr_b16 a[104:105], v10 offset:36864\n"
    "v_mul_f32 v69, v69, v46\n"
    "v_mfma_f32_16x16x32_bf16 v[92:95], a[148:151], v[168:171], v[92:95]\n"
    "ds_read_b64_tr_b16 a[106:107], v10 offset:38912\n"
    "v_mul_f32 v70, v70, v46\n"
    "v_mfma_f32_16x16x32_bf16 v[84:87], a[148:151], v[160:163], v[84:87]\n"
    "ds_read_b64_tr_b16 a[108:109], v11 offset:36864\n"
    "v_mul_f32 v71, v71, v46\n"
    "v_mfma_f32_16x16x32_bf16 v[80:83], a[152:155], v[176:179], v[80:83]\n"
    "ds_read_b64_tr_b16 a[110:111], v11 offset:38912\n"
    "v_cvt_pk_bf16_f32 v64, v64, v65\n"
    "v_mfma_f32_16x16x32_bf16 v[88:91], a[152:155], v[184:187], v[88:91]\n"
    "ds_read_b64_tr_b16 a[112:113], v10 offset:40960\n"
    "v_cvt_pk_bf16_f32 v65, v66, v67\n"
    "v_mfma_f32_16x16x32_bf16 v[92:95], a[156:159], v[184:187], v[92:95]\n"
    "ds_read_b64_tr_b16 a[114:115], v10 offset:43008\n"
    "v_cvt_pk_bf16_f32 v66, v68, v69\n"
    "v_mfma_f32_16x16x32_bf16 v[84:87], a[156:159], v[176:179], v[84:87]\n"
    "ds_read_b64_tr_b16 a[116:117], v11 offset:40960\n"
    "v_cvt_pk_bf16_f32 v67, v70, v71\n"
    "v_mfma_f32_16x16x32_bf16 v[80:83], a[160:163], v[192:195], v[80:83]\n"
    "ds_read_b64_tr_b16 a[118:119], v11 offset:43008\n"
    "ds_read_b64_tr_b16 a[120:121], v10 offset:45056\n"
    "s_mov_b32 s68, 0x8000\n"
    "v_mfma_f32_16x16x32_bf16 v[88:91], a[160:163], v[200:203], v[88:91]\n"
    "ds_read_b64_tr_b16 a[122:123], v10 offset:47104\n"
    "v_cndmask_b32_e64 v22, v22, v64, s[74:75]\n"
    "v_mfma_f32_16x16x32_bf16 v[92:95], a[164:167], v[200:203], v[92:95]\n"
    "ds_read_b64_tr_b16 a[124:125], v11 offset:45056\n"
    "v_cndmask_b32_e64 v23, v23, v65, s[74:75]\n"
    "v_mfma_f32_16x16x32_bf16 v[84:87], a[164:167], v[192:195], v[84:87]\n"
    "ds_read_b64_tr_b16 a[126:127], v11 offset:47104\n"
    "v_cndmask_b32_e64 v24, v24, v66, s[74:75]\n"
    "v_mfma_f32_16x16x32_bf16 v[80:83], a[168:171], v[208:211], v[80:83]\n"
    "ds_read_b64_tr_b16 a[128:129], v8 offset:33280\n"
    "v_cndmask_b32_e64 v25, v25, v67, s[74:75]\n"
    "v_mfma_f32_16x16x32_bf16 v[88:91], a[168:171], v[216:219], v[88:91]\n"
    "buffer_store_dwordx4 v[64:67], v14, s[56:59], s68 offen sc1\n"
    "v_mfma_f32_16x16x32_bf16 v[92:95], a[172:175], v[216:219], v[92:95]\n"
    "ds_read_b64_tr_b16 a[130:131], v8 offset:35328\n"
    "v_mul_f32 v72, v72, v47\n"
    "v_mfma_f32_16x16x32_bf16 v[84:87], a[172:175], v[208:211], v[84:87]\n"
    "ds_read_b64_tr_b16 a[132:133], v9 offset:33280\n"
    "v_mul_f32 v73, v73, v47\n"
    "v_mfma_f32_16x16x32_bf16 v[80:83], a[176:179], v[224:227], v[80:83]\n"
    "ds_read_b64_tr_b16 a[134:135], v9 offset:35328\n"
    "ds_read_b64_tr_b16 a[136:137], v8 offset:37376\n"
    "v_mul_f32 v74, v74, v47\n"
    "v_mfma_f32_16x16x32_bf16 v[88:91], a[176:179], v[232:235], v[88:91]\n"
    "ds_read_b64_tr_b16 a[138:139], v8 offset:39424\n"
    "v_mul_f32 v75, v75, v47\n"
    "v_mfma_f32_16x16x32_bf16 v[92:95], a[180:183], v[232:235], v[92:95]\n"
    "ds_read_b64_tr_b16 a[140:141], v9 offset:37376\n"
    "v_mul_f32 v76, v76, v47\n"
    "v_mfma_f32_16x16x32_bf16 v[84:87], a[180:183], v[224:227], v[84:87]\n"
    "ds_read_b64_tr_b16 a[142:143], v9 offset:39424\n"
    "v_mul_f32 v77, v77, v47\n"
    "v_mfma_f32_16x16x32_bf16 v[80:83], a[184:187], v[240:243], v[80:83]\n"
    "v_mul_f32 v78, v78, v47\n"
    "v_mul_f32 v79, v79, v47\n"
    "v_mfma_f32_16x16x32_bf16 v[88:91], a[184:187], v[248:251], v[88:91]\n"
    "v_cvt_pk_bf16_f32 v72, v72, v73\n"
    "v_cvt_pk_bf16_f32 v73, v74, v75\n"
    "v_mfma_f32_16x16x32_bf16 v[92:95], a[188:191], v[248:251], v[92:95]\n"
    "v_cvt_pk_bf16_f32 v74, v76, v77\n"
    "v_cvt_pk_bf16_f32 v75, v78, v79\n"
    "v_mfma_f32_16x16x32_bf16 v[84:87], a[188:191], v[240:243], v[84:87]\n"
    "buffer_store_dwordx4 v[72:75], v45, s[56:59], s68 offen offset:1024 sc1\n"
    "s_waitcnt lgkmcnt(8)\n"
    "v_mfma_f32_16x16x32_bf16 v[96:99], a[64:67], v[128:131], 0\n"
    "ds_read_b64_tr_b16 a[144:145], v8 offset:41472\n"
    "v_mfma_f32_16x16x32_bf16 v[104:107], a[64:67], v[136:139], 0\n"
    "ds_read_b64_tr_b16 a[146:147], v8 offset:43520\n"
    "ds_read_b64_tr_b16 a[148:149], v9 offset:41472\n"
    "v_mfma_f32_16x16x32_bf16 v[108:111], a[68:71], v[136:139], 0\n"
    "ds_read_b64_tr_b16 a[150:151], v9 offset:43520\n"
    "v_mfma_f32_16x16x32_bf16 v[100:103], a[68:71], v[128:131], 0\n"
    "ds_read_b64_tr_b16 a[152:153], v8 offset:45568\n"
    "ds_read_b64_tr_b16 a[154:155], v8 offset:47616\n"
    "v_mfma_f32_16x16x32_bf16 v[96:99], a[72:75], v[144:147], v[96:99]\n"
    "ds_read_b64_tr_b16 a[156:157], v9 offset:45568\n"
    "v_mul_f32 v80, v80, v46\n"
    "v_mfma_f32_16x16x32_bf16 v[104:107], a[72:75], v[152:155], v[104:107]\n"
    "ds_read_b64_tr_b16 a[158:159], v9 offset:47616\n"
    "v_mul_f32 v81, v81, v46\n"
    "v_mfma_f32_16x16x32_bf16 v[108:111], a[76:79], v[152:155], v[108:111]\n"
    "ds_read_b64_tr_b16 a[160:161], v10 offset:33280\n"
    "v_mul_f32 v82, v82, v46\n"
    "v_mfma_f32_16x16x32_bf16 v[100:103], a[76:79], v[144:147], v[100:103]\n"
    "ds_read_b64_tr_b16 a[162:163], v10 offset:35328\n"
    "v_mul_f32 v83, v83, v46\n"
    "v_mfma_f32_16x16x32_bf16 v[96:99], a[80:83], v[160:163], v[96:99]\n"
    "ds_read_b64_tr_b16 a[164:165], v11 offset:33280\n"
    "v_mul_f32 v84, v84, v46\n"
    "v_mfma_f32_16x16x32_bf16 v[104:107], a[80:83], v[168:171], v[104:107]\n"
    "ds_read_b64_tr_b16 a[166:167], v11 offset:35328\n"
    "v_mul_f32 v85, v85, v46\n"
    "v_mfma_f32_16x16x32_bf16 v[108:111], a[84:87], v[168:171], v[108:111]\n"
    "ds_read_b64_tr_b16 a[168:169], v10 offset:37376\n"
    "v_mul_f32 v86, v86, v46\n"
    "v_mfma_f32_16x16x32_bf16 v[100:103], a[84:87], v[160:163], v[100:103]\n"
    "ds_read_b64_tr_b16 a[170:171], v10 offset:39424\n"
    "v_mul_f32 v87, v87, v46\n"
    "v_mfma_f32_16x16x32_bf16 v[96:99], a[88:91], v[176:179], v[96:99]\n"
    "ds_read_b64_tr_b16 a[172:173], v11 offset:37376\n"
    "v_cvt_pk_bf16_f32 v80, v80, v81\n"
    "v_mfma_f32_16x16x32_bf16 v[104:107], a[88:91], v[184:187], v[104:107]\n"
    "ds_read_b64_tr_b16 a[174:175], v11 offset:39424\n"
    "v_cvt_pk_bf16_f32 v81, v82, v83\n"
    "v_mfma_f32_16x16x32_bf16 v[108:111], a[92:95], v[184:187], v[108:111]\n"
    "ds_read_b64_tr_b16 a[176:177], v10 offset:41472\n"
    "v_cvt_pk_bf16_f32 v82, v84, v85\n"
    "v_mfma_f32_16x16x32_bf16 v[100:103], a[92:95], v[176:179], v[100:103]\n"
    "ds_read_b64_tr_b16 a[178:179], v10 offset:43520\n"
    "v_cvt_pk_bf16_f32 v83, v86, v87\n"
    "v_mfma_f32_16x16x32_bf16 v[96:99], a[96:99], v[192:195], v[96:99]\n"
    "ds_read_b64_tr_b16 a[180:181], v11 offset:41472\n"
    "s_mov_b32 s68, 0x8000\n"
    "v_mfma_f32_16x16x32_bf16 v[104:107], a[96:99], v[200:203], v[104:107]\n"
    "buffer_store_dwordx4 v[80:83], v45, s[56:59], s68 offen offset:2048 sc1\n"
    "v_mfma_f32_16x16x32_bf16 v[108:111], a[100:103], v[200:203], v[108:111]\n"
    "ds_read_b64_tr_b16 a[182:183], v11 offset:43520\n"
    "v_mul_f32 v88, v88, v47\n"
    "v_mfma_f32_16x16x32_bf16 v[100:103], a[100:103], v[192:195], v[100:103]\n"
    "ds_read_b64_tr_b16 a[184:185], v10 offset:45568\n"
    "v_mul_f32 v89, v89, v47\n"
    "v_mfma_f32_16x16x32_bf16 v[96:99], a[104:107], v[208:211], v[96:99]\n"
    "ds_read_b64_tr_b16 a[186:187], v10 offset:47616\n"
    "v_mul_f32 v90, v90, v47\n"
    "v_mfma_f32_16x16x32_bf16 v[104:107], a[104:107], v[216:219], v[104:107]\n"
    "ds_read_b64_tr_b16 a[188:189], v11 offset:45568\n"
    "v_mul_f32 v91, v91, v47\n"
    "v_mfma_f32_16x16x32_bf16 v[108:111], a[108:111], v[216:219], v[108:111]\n"
    "ds_read_b64_tr_b16 a[190:191], v11 offset:47616\n"
    "v_mul_f32 v92, v92, v47\n"
    "v_mfma_f32_16x16x32_bf16 v[100:103], a[108:111], v[208:211], v[100:103]\n"
    "ds_read_b64_tr_b16 a[64:65], v8 offset:49152\n"
    "v_mul_f32 v93, v93, v47\n"
    "v_mfma_f32_16x16x32_bf16 v[96:99], a[112:115], v[224:227], v[96:99]\n"
    "ds_read_b64_tr_b16 a[66:67], v8 offset:51200\n"
    "v_mul_f32 v94, v94, v47\n"
    "v_mfma_f32_16x16x32_bf16 v[104:107], a[112:115], v[232:235], v[104:107]\n"
    "ds_read_b64_tr_b16 a[68:69], v9 offset:49152\n"
    "v_mul_f32 v95, v95, v47\n"
    "v_mfma_f32_16x16x32_bf16 v[108:111], a[116:119], v[232:235], v[108:111]\n"
    "ds_read_b64_tr_b16 a[70:71], v9 offset:51200\n"
    "v_cvt_pk_bf16_f32 v88, v88, v89\n"
    "v_mfma_f32_16x16x32_bf16 v[100:103], a[116:119], v[224:227], v[100:103]\n"
    "ds_read_b64_tr_b16 a[72:73], v8 offset:53248\n"
    "v_cvt_pk_bf16_f32 v89, v90, v91\n"
    "v_mfma_f32_16x16x32_bf16 v[96:99], a[120:123], v[240:243], v[96:99]\n"
    "ds_read_b64_tr_b16 a[74:75], v8 offset:55296\n"
    "v_cvt_pk_bf16_f32 v90, v92, v93\n"
    "v_mfma_f32_16x16x32_bf16 v[104:107], a[120:123], v[248:251], v[104:107]\n"
    "ds_read_b64_tr_b16 a[76:77], v9 offset:53248\n"
    "v_cvt_pk_bf16_f32 v91, v94, v95\n"
    "v_mfma_f32_16x16x32_bf16 v[108:111], a[124:127], v[248:251], v[108:111]\n"
    "buffer_store_dwordx4 v[88:91], v45, s[56:59], s68 offen offset:3072 sc1\n"
    "v_mfma_f32_16x16x32_bf16 v[100:103], a[124:127], v[240:243], v[100:103]\n"
    "ds_read_b64_tr_b16 a[78:79], v9 offset:55296\n"
    "s_waitcnt lgkmcnt(8)\n"
    "s_waitcnt vmcnt(16)\n"
    "buffer_store_dword v63, v30, s[20:23], 0 offen sc1\n"
    "v_mfma_f32_16x16x32_bf16 v[112:115], a[128:131], v[128:131], 0\n"
    "ds_read_b64_tr_b16 a[80:81], v8 offset:57344\n"
    "s_cmp_eq_u32 s72, 3\n"
    "s_cselect_b64 s[74:75], -1, 0\n"
    "v_mfma_f32_16x16x32_bf16 v[120:123], a[128:131], v[136:139], 0\n"
    "ds_read_b64_tr_b16 a[82:83], v8 offset:59392\n"
    "v_cndmask_b32_e64 v14, v45, v15, s[74:75]\n"
    "v_mfma_f32_16x16x32_bf16 v[124:127], a[132:135], v[136:139], 0\n"
    "ds_read_b64_tr_b16 a[84:85], v9 offset:57344\n"
    "ds_read_b64_tr_b16 a[86:87], v9 offset:59392\n"
    "v_mfma_f32_16x16x32_bf16 v[116:119], a[132:135], v[128:131], 0\n"
    "ds_read_b64_tr_b16 a[88:89], v8 offset:61440\n"
    "ds_read_b64_tr_b16 a[90:91], v8 offset:63488\n"
    "v_mfma_f32_16x16x32_bf16 v[112:115], a[136:139], v[144:147], v[112:115]\n"
    "ds_read_b64_tr_b16 a[92:93], v9 offset:61440\n"
    "v_mul_f32 v96, v96, v46\n"
    "v_mfma_f32_16x16x32_bf16 v[120:123], a[136:139], v[152:155], v[120:123]\n"
    "ds_read_b64_tr_b16 a[94:95], v9 offset:63488\n"
    "v_mul_f32 v97, v97, v46\n"
    "v_mfma_f32_16x16x32_bf16 v[124:127], a[140:143], v[152:155], v[124:127]\n"
    "ds_read_b64_tr_b16 a[96:97], v10 offset:49152\n"
    "v_mul_f32 v98, v98, v46\n"
    "v_mfma_f32_16x16x32_bf16 v[116:119], a[140:143], v[144:147], v[116:119]\n"
    "ds_read_b64_tr_b16 a[98:99], v10 offset:51200\n"
    "v_mul_f32 v99, v99, v46\n"
    "v_mfma_f32_16x16x32_bf16 v[112:115], a[144:147], v[160:163], v[112:115]\n"
    "ds_read_b64_tr_b16 a[100:101], v11 offset:49152\n"
    "ds_read_b64_tr_b16 a[102:103], v11 offset:51200\n"
    "v_mul_f32 v100, v100, v46\n"
    "v_mfma_f32_16x16x32_bf16 v[120:123], a[144:147], v[168:171], v[120:123]\n"
    "ds_read_b64_tr_b16 a[104:105], v10 offset:53248\n"
    "v_mul_f32 v101, v101, v46\n"
    "v_mfma_f32_16x16x32_bf16 v[124:127], a[148:151], v[168:171], v[124:127]\n"
    "ds_read_b64_tr_b16 a[106:107], v10 offset:55296\n"
    "v_mul_f32 v102, v102, v46\n"
    "v_mfma_f32_16x16x32_bf16 v[116:119], a[148:151], v[160:163], v[116:119]\n"
    "ds_read_b64_tr_b16 a[108:109], v11 offset:53248\n"
    "v_mul_f32 v103, v103, v46\n"
    "L_iw6:\n"
    "s_cbranch_execz L_iwback\n"
    "v_mfma_f32_16x16x32_bf16 v[112:115], a[152:155], v[176:179], v[112:115]\n"
    "ds_read_b64_tr_b16 a[110:111], v11 offset:55296\n"
    "v_cvt_pk_bf16_f32 v96, v96, v97\n"
    "v_mfma_f32_16x16x32_bf16 v[120:123], a[152:155], v[184:187], v[120:123]\n"
    "ds_read_b64_tr_b16 a[112:113], v10 offset:57344\n"
    "v_cvt_pk_bf16_f32 v97, v98, v99\n"
    "v_mfma_f32_16x16x32_bf16 v[124:127], a[156:159], v[184:187], v[124:127]\n"
    "ds_read_b64_tr_b16 a[114:115], v10 offset:59392\n"
    "v_cvt_pk_bf16_f32 v98, v100, v101\n"
    "v_mfma_f32_16x16x32_bf16 v[116:119], a[156:159], v[176:179], v[116:119]\n"
    "ds_read_b64_tr_b16 a[116:117], v11 offset:57344\n"
    "v_cvt_pk_bf16_f32 v99, v102, v103\n"
    "v_mfma_f32_16x16x32_bf16 v[112:115], a[160:163], v[192:195], v[112:115]\n"
    "ds_read_b64_tr_b16 a[118:119], v11 offset:59392\n"
    "ds_read_b64_tr_b16 a[120:121], v10 offset:61440\n"
    "s_mov_b32 s68, 0xc000\n"
    "v_mfma_f32_16x16x32_bf16 v[120:123], a[160:163], v[200:203], v[120:123]\n"
    "ds_read_b64_tr_b16 a[122:123], v10 offset:63488\n"
    "v_cndmask_b32_e64 v34, v34, v96, s[74:75]\n"
    "v_mfma_f32_16x16x32_bf16 v[124:127], a[164:167], v[200:203], v[124:127]\n"
    "ds_read_b64_tr_b16 a[124:125], v11 offset:61440\n"
    "v_cndmask_b32_e64 v35, v35, v97, s[74:75]\n"
    "v_mfma_f32_16x16x32_bf16 v[116:119], a[164:167], v[192:195], v[116:119]\n"
    "ds_read_b64_tr_b16 a[126:127], v11 offset:63488\n"
    "v_cndmask_b32_e64 v36, v36, v98, s[74:75]\n"
    "v_mfma_f32_16x16x32_bf16 v[112:115], a[168:171], v[208:211], v[112:115]\n"
    "ds_read_b64_tr_b16 a[128:129], v8 offset:49664\n"
    "v_cndmask_b32_e64 v37, v37, v99, s[74:75]\n"
    "v_mfma_f32_16x16x32_bf16 v[120:123], a[168:171], v[216:219], v[120:123]\n"
    "buffer_store_dwordx4 v[96:99], v14, s[56:59], s68 offen sc1\n"
    "v_mfma_f32_16x16x32_bf16 v[124:127], a[172:175], v[216:219], v[124:127]\n"
    "ds_read_b64_tr_b16 a[130:131], v8 offset:51712\n"
    "v_mul_f32 v104, v104, v47\n"
    "v_mfma_f32_16x16x32_bf16 v[116:119], a[172:175], v[208:211], v[116:119]\n"
    "ds_read_b64_tr_b16 a[132:133], v9 offset:49664\n"
    "v_mul_f32 v105, v105, v47\n"
    "v_mfma_f32_16x16x32_bf16 v[112:115], a[176:179], v[224:227], v[112:115]\n"
    "ds_read_b64_tr_b16 a[134:135], v9 offset:51712\n"
    "ds_read_b64_tr_b16 a[136:137], v8 offset:53760\n"
    "v_mul_f32 v106, v106, v47\n"
    "v_mfma_f32_16x16x32_bf16 v[120:123], a[176:179], v[232:235], v[120:123]\n"
    "ds_read_b64_tr_b16 a[138:139], v8 offset:55808\n"
    "v_mul_f32 v107, v107, v47\n"
    "v_mfma_f32_16x16x32_bf16 v[124:127], a[180:183], v[232:235], v[124:127]\n"
    "ds_read_b64_tr_b16 a[140:141], v9 offset:53760\n"
    "v_mul_f32 v108, v108, v47\n"
    "v_mfma_f32_16x16x32_bf16 v[116:119], a[180:183], v[224:227], v[116:119]\n"
    "ds_read_b64_tr_b16 a[142:143], v9 offset:55808\n"
    "v_mul_f32 v109, v109, v47\n"
    "v_mfma_f32_16x16x32_bf16 v[112:115], a[184:187], v[240:243], v[112:115]\n"
    "v_mul_f32 v110, v110, v47\n"
    "v_mul_f32 v111, v111, v47\n"
    "v_mfma_f32_16x16x32_bf16 v[120:123], a[184:187], v[248:251], v[120:123]\n"
    "v_cvt_pk_bf16_f32 v104, v104, v105\n"
    "v_cvt_pk_bf16_f32 v105, v106, v107\n"
    "v_mfma_f32_16x16x32_bf16 v[124:127], a[188:191], v[248:251], v[124:127]\n"
    "v_cvt_pk_bf16_f32 v106, v108, v109\n"
    "v_cvt_pk_bf16_f32 v107, v110, v111\n"
    "v_mfma_f32_16x16x32_bf16 v[116:119], a[188:191], v[240:243], v[116:119]\n"
    "buffer_store_dwordx4 v[104:107], v45, s[56:59], s68 offen offset:1024 sc1\n"
    "s_waitcnt vmcnt(20) lgkmcnt(8)\n"
    "s_barrier\n"
    "v_mfma_f32_16x16x32_bf16 v[64:67], a[64:67], v[128:131], 0\n"
    "ds_read_b64_tr_b16 a[144:145], v8 offset:57856\n"
    "v_mfma_f32_16x16x32_bf16 v[72:75], a[64:67], v[136:139], 0\n"
    "ds_read_b64_tr_b16 a[146:147], v8 offset:59904\n"
    "ds_read_b64_tr_b16 a[148:149], v9 offset:57856\n"
    "v_mfma_f32_16x16x32_bf16 v[76:79], a[68:71], v[136:139], 0\n"
    "ds_read_b64_tr_b16 a[150:151], v9 offset:59904\n"
    "v_mfma_f32_16x16x32_bf16 v[68:71], a[68:71], v[128:131], 0\n"
    "ds_read_b64_tr_b16 a[152:153], v8 offset:61952\n"
    "ds_read_b64_tr_b16 a[154:155], v8 offset:64000\n"
    "v_mfma_f32_16x16x32_bf16 v[64:67], a[72:75], v[144:147], v[64:67]\n"
    "ds_read_b64_tr_b16 a[156:157], v9 offset:61952\n"
    "v_mul_f32 v112, v112, v46\n"
    "v_mfma_f32_16x16x32_bf16 v[72:75], a[72:75], v[152:155], v[72:75]\n"
    "ds_read_b64_tr_b16 a[158:159], v9 offset:64000\n"
    "v_mul_f32 v113, v113, v46\n"
    "v_mfma_f32_16x16x32_bf16 v[76:79], a[76:79], v[152:155], v[76:79]\n"
    "ds_read_b64_tr_b16 a[160:161], v10 offset:49664\n"
    "v_mul_f32 v114, v114, v46\n"
    "v_mfma_f32_16x16x32_bf16 v[68:71], a[76:79], v[144:147], v[68:71]\n"
    "ds_read_b64_tr_b16 a[162:163], v10 offset:51712\n"
    "v_mul_f32 v115, v115, v46\n"
    "v_mfma_f32_16x16x32_bf16 v[64:67], a[80:83], v[160:163], v[64:67]\n"
    "ds_read_b64_tr_b16 a[164:165], v11 offset:49664\n"
    "v_mul_f32 v116, v116, v46\n"
    "v_mfma_f32_16x16x32_bf16 v[72:75], a[80:83], v[168:171], v[72:75]\n"
    "ds_read_b64_tr_b16 a[166:167], v11 offset:51712\n"
    "v_mul_f32 v117, v117, v46\n"
    "v_mfma_f32_16x16x32_bf16 v[76:79], a[84:87], v[168:171], v[76:79]\n"
    "ds_read_b64_tr_b16 a[168:169], v10 offset:53760\n"
    "v_mul_f32 v118, v118, v46\n"
    "v_mfma_f32_16x16x32_bf16 v[68:71], a[84:87], v[160:163], v[68:71]\n"
    "ds_read_b64_tr_b16 a[170:171], v10 offset:55808\n"
    "v_mul_f32 v119, v119, v46\n"
    "v_mfma_f32_16x16x32_bf16 v[64:67], a[88:91], v[176:179], v[64:67]\n"
    "ds_read_b64_tr_b16 a[172:173], v11 offset:53760\n"
    "v_cvt_pk_bf16_f32 v112, v112, v113\n"
    "v_mfma_f32_16x16x32_bf16 v[72:75], a[88:91], v[184:187], v[72:75]\n"
    "ds_read_b64_tr_b16 a[174:175], v11 offset:55808\n"
    "v_cvt_pk_bf16_f32 v113, v114, v115\n"
    "v_mfma_f32_16x16x32_bf16 v[76:79], a[92:95], v[184:187], v[76:79]\n"
    "ds_read_b64_tr_b16 a[176:177], v10 offset:57856\n"
    "v_cvt_pk_bf16_f32 v114, v116, v117\n"
    "v_mfma_f32_16x16x32_bf16 v[68:71], a[92:95], v[176:179], v[68:71]\n"
    "ds_read_b64_tr_b16 a[178:179], v10 offset:59904\n"
    "v_cvt_pk_bf16_f32 v115, v118, v119\n"
    "v_mfma_f32_16x16x32_bf16 v[64:67], a[96:99], v[192:195], v[64:67]\n"
    "ds_read_b64_tr_b16 a[180:181], v11 offset:57856\n"
    "s_mov_b32 s68, 0xc000\n"
    "v_mfma_f32_16x16x32_bf16 v[72:75], a[96:99], v[200:203], v[72:75]\n"
    "buffer_store_dwordx4 v[112:115], v45, s[56:59], s68 offen offset:2048 sc1\n"
    "v_mfma_f32_16x16x32_bf16 v[76:79], a[100:103], v[200:203], v[76:79]\n"
    "ds_read_b64_tr_b16 a[182:183], v11 offset:59904\n"
    "v_mul_f32 v120, v120, v47\n"
    "v_mfma_f32_16x16x32_bf16 v[68:71], a[100:103], v[192:195], v[68:71]\n"
    "ds_read_b64_tr_b16 a[184:185], v10 offset:61952\n"
    "v_mul_f32 v121, v121, v47\n"
    "v_mfma_f32_16x16x32_bf16 v[64:67], a[104:107], v[208:211], v[64:67]\n"
    "ds_read_b64_tr_b16 a[186:187], v10 offset:64000\n"
    "v_mul_f32 v122, v122, v47\n"
    "v_mfma_f32_16x16x32_bf16 v[72:75], a[104:107], v[216:219], v[72:75]\n"
    "ds_read_b64_tr_b16 a[188:189], v11 offset:61952\n"
    "v_mul_f32 v123, v123, v47\n"
    "v_mfma_f32_16x16x32_bf16 v[76:79], a[108:111], v[216:219], v[76:79]\n"
    "ds_read_b64_tr_b16 a[190:191], v11 offset:64000\n"
    "v_mul_f32 v124, v124, v47\n"
    "v_mfma_f32_16x16x32_bf16 v[68:71], a[108:111], v[208:211], v[68:71]\n"
    "ds_read_b64_tr_b16 a[64:65], v12 offset:16384\n"
    "v_mul_f32 v125, v125, v47\n"
    "v_mfma_f32_16x16x32_bf16 v[64:67], a[112:115], v[224:227], v[64:67]\n"
    "ds_read_b64_tr_b16 a[66:67], v12 offset:18432\n"
    "v_mul_f32 v126, v126, v47\n"
    "v_mfma_f32_16x16x32_bf16 v[72:75], a[112:115], v[232:235], v[72:75]\n"
    "ds_read_b64_tr_b16 a[68:69], v13 offset:16384\n"
    "v_mul_f32 v127, v127, v47\n"
    "v_mfma_f32_16x16x32_bf16 v[76:79], a[116:119], v[232:235], v[76:79]\n"
    "ds_read_b64_tr_b16 a[70:71], v13 offset:18432\n"
    "v_cvt_pk_bf16_f32 v120, v120, v121\n"
    "v_mfma_f32_16x16x32_bf16 v[68:71], a[116:119], v[224:227], v[68:71]\n"
    "ds_read_b64_tr_b16 a[72:73], v12 offset:20480\n"
    "v_cvt_pk_bf16_f32 v121, v122, v123\n"
    "v_mfma_f32_16x16x32_bf16 v[64:67], a[120:123], v[240:243], v[64:67]\n"
    "ds_read_b64_tr_b16 a[74:75], v12 offset:22528\n"
    "v_cvt_pk_bf16_f32 v122, v124, v125\n"
    "v_mfma_f32_16x16x32_bf16 v[72:75], a[120:123], v[248:251], v[72:75]\n"
    "ds_read_b64_tr_b16 a[76:77], v13 offset:20480\n"
    "v_cvt_pk_bf16_f32 v123, v126, v127\n"
    "v_mfma_f32_16x16x32_bf16 v[76:79], a[124:127], v[248:251], v[76:79]\n"
    "buffer_store_dwordx4 v[120:123], v45, s[56:59], s68 offen offset:3072 sc1\n"
    "v_mfma_f32_16x16x32_bf16 v[68:71], a[124:127], v[240:243], v[68:71]\n"
    "ds_read_b64_tr_b16 a[78:79], v13 offset:22528\n"
    "s_waitcnt lgkmcnt(8)\n"
    "s_waitcnt vmcnt(9)\n"
    "buffer_store_dword v63, v31, s[20:23], 0 offen sc1\n"
    "v_mfma_f32_16x16x32_bf16 v[80:83], a[128:131], v[128:131], 0\n"
    "ds_read_b64_tr_b16 a[80:81], v12 offset:24576\n"
    "s_cmp_eq_u32 s72, 2\n"
    "s_cselect_b64 s[74:75], -1, 0\n"
    "v_mfma_f32_16x16x32_bf16 v[88:91], a[128:131], v[136:139], 0\n"
    "ds_read_b64_tr_b16 a[82:83], v12 offset:26624\n"
    "v_cndmask_b32_e64 v14, v45, v15, s[74:75]\n"
    "v_mfma_f32_16x16x32_bf16 v[92:95], a[132:135], v[136:139], 0\n"
    "ds_read_b64_tr_b16 a[84:85], v13 offset:24576\n"
    "ds_read_b64_tr_b16 a[86:87], v13 offset:26624\n"
    "v_mfma_f32_16x16x32_bf16 v[84:87], a[132:135], v[128:131], 0\n"
    "ds_read_b64_tr_b16 a[88:89], v12 offset:28672\n"
    "ds_read_b64_tr_b16 a[90:91], v12 offset:30720\n"
    "v_mfma_f32_16x16x32_bf16 v[80:83], a[136:139], v[144:147], v[80:83]\n"
    "ds_read_b64_tr_b16 a[92:93], v13 offset:28672\n"
    "v_mul_f32 v64, v64, v46\n"
    "v_mfma_f32_16x16x32_bf16 v[88:91], a[136:139], v[152:155], v[88:91]\n"
    "ds_read_b64_tr_b16 a[94:95], v13 offset:30720\n"
    "v_mul_f32 v65, v65, v46\n"
    "v_mfma_f32_16x16x32_bf16 v[92:95], a[140:143], v[152:155], v[92:95]\n"
    "ds_read_b64_tr_b16 a[96:97], v12 offset:0\n"
    "v_mul_f32 v66, v66, v46\n"
    "v_mfma_f32_16x16x32_bf16 v[84:87], a[140:143], v[144:147], v[84:87]\n"
    "ds_read_b64_tr_b16 a[98:99], v12 offset:2048\n"
    "v_mul_f32 v67, v67, v46\n"
    "v_mfma_f32_16x16x32_bf16 v[80:83], a[144:147], v[160:163], v[80:83]\n"
    "ds_read_b64_tr_b16 a[100:101], v13 offset:0\n"
    "ds_read_b64_tr_b16 a[102:103], v13 offset:2048\n"
    "v_mul_f32 v68, v68, v46\n"
    "v_mfma_f32_16x16x32_bf16 v[88:91], a[144:147], v[168:171], v[88:91]\n"
    "ds_read_b64_tr_b16 a[104:105], v12 offset:4096\n"
    "v_mul_f32 v69, v69, v46\n"
    "v_mfma_f32_16x16x32_bf16 v[92:95], a[148:151], v[168:171], v[92:95]\n"
    "ds_read_b64_tr_b16 a[106:107], v12 offset:6144\n"
    "v_mul_f32 v70, v70, v46\n"
    "v_mfma_f32_16x16x32_bf16 v[84:87], a[148:151], v[160:163], v[84:87]\n"
    "ds_read_b64_tr_b16 a[108:109], v13 offset:4096\n"
    "v_mul_f32 v71, v71, v46\n"
    "v_mfma_f32_16x16x32_bf16 v[80:83], a[152:155], v[176:179], v[80:83]\n"
    "ds_read_b64_tr_b16 a[110:111], v13 offset:6144\n"
    "v_cvt_pk_bf16_f32 v64, v64, v65\n"
    "v_mfma_f32_16x16x32_bf16 v[88:91], a[152:155], v[184:187], v[88:91]\n"
    "ds_read_b64_tr_b16 a[112:113], v12 offset:8192\n"
    "v_cvt_pk_bf16_f32 v65, v66, v67\n"
    "v_mfma_f32_16x16x32_bf16 v[92:95], a[156:159], v[184:187], v[92:95]\n"
    "ds_read_b64_tr_b16 a[114:115], v12 offset:10240\n"
    "v_cvt_pk_bf16_f32 v66, v68, v69\n"
    "v_mfma_f32_16x16x32_bf16 v[84:87], a[156:159], v[176:179], v[84:87]\n"
    "ds_read_b64_tr_b16 a[116:117], v13 offset:8192\n"
    "v_cvt_pk_bf16_f32 v67, v70, v71\n"
    "v_mfma_f32_16x16x32_bf16 v[80:83], a[160:163], v[192:195], v[80:83]\n"
    "ds_read_b64_tr_b16 a[118:119], v13 offset:10240\n"
    "ds_read_b64_tr_b16 a[120:121], v12 offset:12288\n"
    "s_mov_b32 s68, 0x10000\n"
    "v_mfma_f32_16x16x32_bf16 v[88:91], a[160:163], v[200:203], v[88:91]\n"
    "ds_read_b64_tr_b16 a[122:123], v12 offset:14336\n"
    "v_cndmask_b32_e64 v34, v34, v64, s[74:75]\n"
    "v_mfma_f32_16x16x32_bf16 v[92:95], a[164:167], v[200:203], v[92:95]\n"
    "ds_read_b64_tr_b16 a[124:125], v13 offset:12288\n"
    "v_cndmask_b32_e64 v35, v35, v65, s[74:75]\n"
    "v_mfma_f32_16x16x32_bf16 v[84:87], a[164:167], v[192:195], v[84:87]\n"
    "ds_read_b64_tr_b16 a[126:127], v13 offset:14336\n"
    "v_cndmask_b32_e64 v36, v36, v66, s[74:75]\n"
    "v_mfma_f32_16x16x32_bf16 v[80:83], a[168:171], v[208:211], v[80:83]\n"
    "ds_read_b64_tr_b16 a[128:129], v12 offset:16896\n"
    "v_cndmask_b32_e64 v37, v37, v67, s[74:75]\n"
    "v_mfma_f32_16x16x32_bf16 v[88:91], a[168:171], v[216:219], v[88:91]\n"
    "buffer_store_dwordx4 v[64:67], v14, s[56:59], s68 offen sc1\n"
    "v_mfma_f32_16x16x32_bf16 v[92:95], a[172:175], v[216:219], v[92:95]\n"
    "ds_read_b64_tr_b16 a[130:131], v12 offset:18944\n"
    "v_mul_f32 v72, v72, v47\n"
    "v_mfma_f32_16x16x32_bf16 v[84:87], a[172:175], v[208:211], v[84:87]\n"
    "ds_read_b64_tr_b16 a[132:133], v13 offset:16896\n"
    "v_mul_f32 v73, v73, v47\n"
    "v_mfma_f32_16x16x32_bf16 v[80:83], a[176:179], v[224:227], v[80:83]\n"
    "ds_read_b64_tr_b16 a[134:135], v13 offset:18944\n"
    "ds_read_b64_tr_b16 a[136:137], v12 offset:20992\n"
    "v_mul_f32 v74, v74, v47\n"
    "v_mfma_f32_16x16x32_bf16 v[88:91], a[176:179], v[232:235], v[88:91]\n"
    "ds_read_b64_tr_b16 a[138:139], v12 offset:23040\n"
    "v_mul_f32 v75, v75, v47\n"
    "v_mfma_f32_16x16x32_bf16 v[92:95], a[180:183], v[232:235], v[92:95]\n"
    "ds_read_b64_tr_b16 a[140:141], v13 offset:20992\n"
    "v_mul_f32 v76, v76, v47\n"
    "v_mfma_f32_16x16x32_bf16 v[84:87], a[180:183], v[224:227], v[84:87]\n"
    "ds_read_b64_tr_b16 a[142:143], v13 offset:23040\n"
    "v_mul_f32 v77, v77, v47\n"
    "v_mfma_f32_16x16x32_bf16 v[80:83], a[184:187], v[240:243], v[80:83]\n"
    "v_mul_f32 v78, v78, v47\n"
    "v_mul_f32 v79, v79, v47\n"
    "v_mfma_f32_16x16x32_bf16 v[88:91], a[184:187], v[248:251], v[88:91]\n"
    "v_cvt_pk_bf16_f32 v72, v72, v73\n"
    "v_cvt_pk_bf16_f32 v73, v74, v75\n"
    "v_mfma_f32_16x16x32_bf16 v[92:95], a[188:191], v[248:251], v[92:95]\n"
    "v_cvt_pk_bf16_f32 v74, v76, v77\n"
    "v_cvt_pk_bf16_f32 v75, v78, v79\n"
    "v_mfma_f32_16x16x32_bf16 v[84:87], a[188:191], v[240:243], v[84:87]\n"
    "buffer_store_dwordx4 v[72:75], v45, s[56:59], s68 offen offset:1024 sc1\n"
    "s_waitcnt vmcnt(13) lgkmcnt(8)\n"
    "s_barrier\n"
    "v_mfma_f32_16x16x32_bf16 v[96:99], a[64:67], v[128:131], 0\n"
    "ds_read_b64_tr_b16 a[144:145], v12 offset:25088\n"
    "v_mfma_f32_16x16x32_bf16 v[104:107], a[64:67], v[136:139], 0\n"
    "ds_read_b64_tr_b16 a[146:147], v12 offset:27136\n"
    "ds_read_b64_tr_b16 a[148:149], v13 offset:25088\n"
    "v_mfma_f32_16x16x32_bf16 v[108:111], a[68:71], v[136:139], 0\n"
    "ds_read_b64_tr_b16 a[150:151], v13 offset:27136\n"
    "v_mfma_f32_16x16x32_bf16 v[100:103], a[68:71], v[128:131], 0\n"
    "ds_read_b64_tr_b16 a[152:153], v12 offset:29184\n"
    "ds_read_b64_tr_b16 a[154:155], v12 offset:31232\n"
    "v_mfma_f32_16x16x32_bf16 v[96:99], a[72:75], v[144:147], v[96:99]\n"
    "ds_read_b64_tr_b16 a[156:157], v13 offset:29184\n"
    "v_mul_f32 v80, v80, v46\n"
    "v_mfma_f32_16x16x32_bf16 v[104:107], a[72:75], v[152:155], v[104:107]\n"
    "ds_read_b64_tr_b16 a[158:159], v13 offset:31232\n"
    "v_mul_f32 v81, v81, v46\n"
    "v_mfma_f32_16x16x32_bf16 v[108:111], a[76:79], v[152:155], v[108:111]\n"
    "ds_read_b64_tr_b16 a[160:161], v12 offset:512\n"
    "v_mul_f32 v82, v82, v46\n"
    "v_mfma_f32_16x16x32_bf16 v[100:103], a[76:79], v[144:147], v[100:103]\n"
    "ds_read_b64_tr_b16 a[162:163], v12 offset:2560\n"
    "v_mul_f32 v83, v83, v46\n"
    "v_mfma_f32_16x16x32_bf16 v[96:99], a[80:83], v[160:163], v[96:99]\n"
    "ds_read_b64_tr_b16 a[164:165], v13 offset:512\n"
    "v_mul_f32 v84, v84, v46\n"
    "v_mfma_f32_16x16x32_bf16 v[104:107], a[80:83], v[168:171], v[104:107]\n"
    "ds_read_b64_tr_b16 a[166:167], v13 offset:2560\n"
    "v_mul_f32 v85, v85, v46\n"
    "v_mfma_f32_16x16x32_bf16 v[108:111], a[84:87], v[168:171], v[108:111]\n"
    "ds_read_b64_tr_b16 a[168:169], v12 offset:4608\n"
    "v_mul_f32 v86, v86, v46\n"
    "v_mfma_f32_16x16x32_bf16 v[100:103], a[84:87], v[160:163], v[100:103]\n"
    "ds_read_b64_tr_b16 a[170:171], v12 offset:6656\n"
    "v_mul_f32 v87, v87, v46\n"
    "v_mfma_f32_16x16x32_bf16 v[96:99], a[88:91], v[176:179], v[96:99]\n"
    "ds_read_b64_tr_b16 a[172:173], v13 offset:4608\n"
    "v_cvt_pk_bf16_f32 v80, v80, v81\n"
    "v_mfma_f32_16x16x32_bf16 v[104:107], a[88:91], v[184:187], v[104:107]\n"
    "ds_read_b64_tr_b16 a[174:175], v13 offset:6656\n"
    "v_cvt_pk_bf16_f32 v81, v82, v83\n"
    "v_mfma_f32_16x16x32_bf16 v[108:111], a[92:95], v[184:187], v[108:111]\n"
    "ds_read_b64_tr_b16 a[176:177], v12 offset:8704\n"
    "v_cvt_pk_bf16_f32 v82, v84, v85\n"
    "v_mfma_f32_16x16x32_bf16 v[100:103], a[92:95], v[176:179], v[100:103]\n"
    "ds_read_b64_tr_b16 a[178:179], v12 offset:10752\n"
    "v_cvt_pk_bf16_f32 v83, v86, v87\n"
    "v_mfma_f32_16x16x32_bf16 v[96:99], a[96:99], v[192:195], v[96:99]\n"
    "ds_read_b64_tr_b16 a[180:181], v13 offset:8704\n"
    "s_mov_b32 s68, 0x10000\n"
    "v_mfma_f32_16x16x32_bf16 v[104:107], a[96:99], v[200:203], v[104:107]\n"
    "buffer_store_dwordx4 v[80:83], v45, s[56:59], s68 offen offset:2048 sc1\n"
    "v_mfma_f32_16x16x32_bf16 v[108:111], a[100:103], v[200:203], v[108:111]\n"
    "ds_read_b64_tr_b16 a[182:183], v13 offset:10752\n"
    "v_mul_f32 v88, v88, v47\n"
    "v_mfma_f32_16x16x32_bf16 v[100:103], a[100:103], v[192:195], v[100:103]\n"
    "ds_read_b64_tr_b16 a[184:185], v12 offset:12800\n"
    "v_mul_f32 v89, v89, v47\n"
    "v_mfma_f32_16x16x32_bf16 v[96:99], a[104:107], v[208:211], v[96:99]\n"
    "ds_read_b64_tr_b16 a[186:187], v12 offset:14848\n"
    "v_mul_f32 v90, v90, v47\n"
    "v_mfma_f32_16x16x32_bf16 v[104:107], a[104:107], v[216:219], v[104:107]\n"
    "ds_read_b64_tr_b16 a[188:189], v13 offset:12800\n"
    "v_mul_f32 v91, v91, v47\n"
    "v_mfma_f32_16x16x32_bf16 v[108:111], a[108:111], v[216:219], v[108:111]\n"
    "ds_read_b64_tr_b16 a[190:191], v13 offset:14848\n"
    "v_mul_f32 v92, v92, v47\n"
    "v_mfma_f32_16x16x32_bf16 v[100:103], a[108:111], v[208:211], v[100:103]\n"
    "ds_read_b64_tr_b16 a[64:65], v8 offset:0\n"
    "v_mul_f32 v93, v93, v47\n"
    "v_mfma_f32_16x16x32_bf16 v[96:99], a[112:115], v[224:227], v[96:99]\n"
    "ds_read_b64_tr_b16 a[66:67], v8 offset:2048\n"
    "v_mul_f32 v94, v94, v47\n"
    "v_mfma_f32_16x16x32_bf16 v[104:107], a[112:115], v[232:235], v[104:107]\n"
    "ds_read_b64_tr_b16 a[68:69], v9 offset:0\n"
    "v_mul_f32 v95, v95, v47\n"
    "v_mfma_f32_16x16x32_bf16 v[108:111], a[116:119], v[232:235], v[108:111]\n"
    "ds_read_b64_tr_b16 a[70:71], v9 offset:2048\n"
    "v_cvt_pk_bf16_f32 v88, v88, v89\n"
    "v_mfma_f32_16x16x32_bf16 v[100:103], a[116:119], v[224:227], v[100:103]\n"
    "ds_read_b64_tr_b16 a[72:73], v8 offset:4096\n"
    "v_cvt_pk_bf16_f32 v89, v90, v91\n"
    "v_mfma_f32_16x16x32_bf16 v[96:99], a[120:123], v[240:243], v[96:99]\n"
    "ds_read_b64_tr_b16 a[74:75], v8 offset:6144\n"
    "v_cvt_pk_bf16_f32 v90, v92, v93\n"
    "v_mfma_f32_16x16x32_bf16 v[104:107], a[120:123], v[248:251], v[104:107]\n"
    "ds_read_b64_tr_b16 a[76:77], v9 offset:4096\n"
    "v_cvt_pk_bf16_f32 v91, v94, v95\n"
    "v_mfma_f32_16x16x32_bf16 v[108:111], a[124:127], v[248:251], v[108:111]\n"
    "buffer_store_dwordx4 v[88:91], v45, s[56:59], s68 offen offset:3072 sc1\n"
    "v_mfma_f32_16x16x32_bf16 v[100:103], a[124:127], v[240:243], v[100:103]\n"
    "ds_read_b64_tr_b16 a[78:79], v9 offset:6144\n"
    "s_waitcnt lgkmcnt(8)\n"
    "s_waitcnt vmcnt(10)\n"
    "buffer_store_dword v63, v32, s[20:23], 0 offen sc1\n"
    "v_mfma_f32_16x16x32_bf16 v[112:115], a[128:131], v[128:131], 0\n"
    "ds_read_b64_tr_b16 a[80:81], v8 offset:8192\n"
    "s_cmp_eq_u32 s72, 1\n"
    "s_cselect_b64 s[74:75], -1, 0\n"
    "v_mfma_f32_16x16x32_bf16 v[120:123], a[128:131], v[136:139], 0\n"
    "ds_read_b64_tr_b16 a[82:83], v8 offset:10240\n"
    "v_cndmask_b32_e64 v14, v45, v15, s[74:75]\n"
    "v_mfma_f32_16x16x32_bf16 v[124:127], a[132:135], v[136:139], 0\n"
    "ds_read_b64_tr_b16 a[84:85], v9 offset:8192\n"
    "ds_read_b64_tr_b16 a[86:87], v9 offset:10240\n"
    "v_mfma_f32_16x16x32_bf16 v[116:119], a[132:135], v[128:131], 0\n"
    "ds_read_b64_tr_b16 a[88:89], v8 offset:12288\n"
    "ds_read_b64_tr_b16 a[90:91], v8 offset:14336\n"
    "v_mfma_f32_16x16x32_bf16 v[112:115], a[136:139], v[144:147], v[112:115]\n"
    "ds_read_b64_tr_b16 a[92:93], v9 offset:12288\n"
    "v_mul_f32 v96, v96, v46\n"
    "v_mfma_f32_16x16x32_bf16 v[120:123], a[136:139], v[152:155], v[120:123]\n"
    "ds_read_b64_tr_b16 a[94:95], v9 offset:14336\n"
    "v_mul_f32 v97, v97, v46\n"
    "v_mfma_f32_16x16x32_bf16 v[124:127], a[140:143], v[152:155], v[124:127]\n"
    "ds_read_b64_tr_b16 a[96:97], v10 offset:0\n"
    "v_mul_f32 v98, v98, v46\n"
    "v_mfma_f32_16x16x32_bf16 v[116:119], a[140:143], v[144:147], v[116:119]\n"
    "ds_read_b64_tr_b16 a[98:99], v10 offset:2048\n"
    "v_mul_f32 v99, v99, v46\n"
    "v_mfma_f32_16x16x32_bf16 v[112:115], a[144:147], v[160:163], v[112:115]\n"
    "ds_read_b64_tr_b16 a[100:101], v11 offset:0\n"
    "ds_read_b64_tr_b16 a[102:103], v11 offset:2048\n"
    "v_mul_f32 v100, v100, v46\n"
    "v_mfma_f32_16x16x32_bf16 v[120:123], a[144:147], v[168:171], v[120:123]\n"
    "ds_read_b64_tr_b16 a[104:105], v10 offset:4096\n"
    "v_mul_f32 v101, v101, v46\n"
    "v_mfma_f32_16x16x32_bf16 v[124:127], a[148:151], v[168:171], v[124:127]\n"
    "ds_read_b64_tr_b16 a[106:107], v10 offset:6144\n"
    "v_mul_f32 v102, v102, v46\n"
    "v_mfma_f32_16x16x32_bf16 v[116:119], a[148:151], v[160:163], v[116:119]\n"
    "ds_read_b64_tr_b16 a[108:109], v11 offset:4096\n"
    "v_mul_f32 v103, v103, v46\n"
    "v_mfma_f32_16x16x32_bf16 v[112:115], a[152:155], v[176:179], v[112:115]\n"
    "ds_read_b64_tr_b16 a[110:111], v11 offset:6144\n"
    "v_cvt_pk_bf16_f32 v96, v96, v97\n"
    "v_mfma_f32_16x16x32_bf16 v[120:123], a[152:155], v[184:187], v[120:123]\n"
    "ds_read_b64_tr_b16 a[112:113], v10 offset:8192\n"
    "v_cvt_pk_bf16_f32 v97, v98, v99\n"
    "v_mfma_f32_16x16x32_bf16 v[124:127], a[156:159], v[184:187], v[124:127]\n"
    "ds_read_b64_tr_b16 a[114:115], v10 offset:10240\n"
    "v_cvt_pk_bf16_f32 v98, v100, v101\n"
    "v_mfma_f32_16x16x32_bf16 v[116:119], a[156:159], v[176:179], v[116:119]\n"
    "ds_read_b64_tr_b16 a[116:117], v11 offset:8192\n"
    "v_cvt_pk_bf16_f32 v99, v102, v103\n"
    "v_mfma_f32_16x16x32_bf16 v[112:115], a[160:163], v[192:195], v[112:115]\n"
    "ds_read_b64_tr_b16 a[118:119], v11 offset:10240\n"
    "ds_read_b64_tr_b16 a[120:121], v10 offset:12288\n"
    "s_mov_b32 s68, 0x14000\n"
    "v_mfma_f32_16x16x32_bf16 v[120:123], a[160:163], v[200:203], v[120:123]\n"
    "ds_read_b64_tr_b16 a[122:123], v10 offset:14336\n"
    "v_cndmask_b32_e64 v34, v34, v96, s[74:75]\n"
    "v_mfma_f32_16x16x32_bf16 v[124:127], a[164:167], v[200:203], v[124:127]\n"
    "ds_read_b64_tr_b16 a[124:125], v11 offset:12288\n"
    "v_cndmask_b32_e64 v35, v35, v97, s[74:75]\n"
    "v_mfma_f32_16x16x32_bf16 v[116:119], a[164:167], v[192:195], v[116:119]\n"
    "ds_read_b64_tr_b16 a[126:127], v11 offset:14336\n"
    "v_cndmask_b32_e64 v36, v36, v98, s[74:75]\n"
    "v_mfma_f32_16x16x32_bf16 v[112:115], a[168:171], v[208:211], v[112:115]\n"
    "ds_read_b64_tr_b16 a[128:129], v8 offset:512\n"
    "v_cndmask_b32_e64 v37, v37, v99, s[74:75]\n"
    "v_mfma_f32_16x16x32_bf16 v[120:123], a[168:171], v[216:219], v[120:123]\n"
    "buffer_store_dwordx4 v[96:99], v14, s[56:59], s68 offen sc1\n"
    "v_mfma_f32_16x16x32_bf16 v[124:127], a[172:175], v[216:219], v[124:127]\n"
    "ds_read_b64_tr_b16 a[130:131], v8 offset:2560\n"
    "v_mul_f32 v104, v104, v47\n"
    "v_mfma_f32_16x16x32_bf16 v[116:119], a[172:175], v[208:211], v[116:119]\n"
    "ds_read_b64_tr_b16 a[132:133], v9 offset:512\n"
    "v_mul_f32 v105, v105, v47\n"
    "v_mfma_f32_16x16x32_bf16 v[112:115], a[176:179], v[224:227], v[112:115]\n"
    "ds_read_b64_tr_b16 a[134:135], v9 offset:2560\n"
    "ds_read_b64_tr_b16 a[136:137], v8 offset:4608\n"
    "v_mul_f32 v106, v106, v47\n"
    "v_mfma_f32_16x16x32_bf16 v[120:123], a[176:179], v[232:235], v[120:123]\n"
    "ds_read_b64_tr_b16 a[138:139], v8 offset:6656\n"
    "v_mul_f32 v107, v107, v47\n"
    "v_mfma_f32_16x16x32_bf16 v[124:127], a[180:183], v[232:235], v[124:127]\n"
    "ds_read_b64_tr_b16 a[140:141], v9 offset:4608\n"
    "v_mul_f32 v108, v108, v47\n"
    "v_mfma_f32_16x16x32_bf16 v[116:119], a[180:183], v[224:227], v[116:119]\n"
    "ds_read_b64_tr_b16 a[142:143], v9 offset:6656\n"
    "v_mul_f32 v109, v109, v47\n"
    "v_mfma_f32_16x16x32_bf16 v[112:115], a[184:187], v[240:243], v[112:115]\n"
    "v_mul_f32 v110, v110, v47\n"
    "v_mul_f32 v111, v111, v47\n"
    "v_mfma_f32_16x16x32_bf16 v[120:123], a[184:187], v[248:251], v[120:123]\n"
    "v_cvt_pk_bf16_f32 v104, v104, v105\n"
    "v_cvt_pk_bf16_f32 v105, v106, v107\n"
    "v_mfma_f32_16x16x32_bf16 v[124:127], a[188:191], v[248:251], v[124:127]\n"
    "v_cvt_pk_bf16_f32 v106, v108, v109\n"
    "v_cvt_pk_bf16_f32 v107, v110, v111\n"
    "v_mfma_f32_16x16x32_bf16 v[116:119], a[188:191], v[240:243], v[116:119]\n"
    "buffer_store_dwordx4 v[104:107], v45, s[56:59], s68 offen offset:1024 sc1\n"
    "s_waitcnt lgkmcnt(8)\n"
    "v_mfma_f32_16x16x32_bf16 v[64:67], a[64:67], v[128:131], 0\n"
    "ds_read_b64_tr_b16 a[144:145], v8 offset:8704\n"
    "v_mfma_f32_16x16x32_bf16 v[72:75], a[64:67], v[136:139], 0\n"
    "ds_read_b64_tr_b16 a[146:147], v8 offset:10752\n"
    "v_mfma_f32_16x16x32_bf16 v[76:79], a[68:71], v[136:139], 0\n"
    "ds_read_b64_tr_b16 a[148:149], v9 offset:8704\n"
    "v_mfma_f32_16x16x32_bf16 v[68:71], a[68:71], v[128:131], 0\n"
    "ds_read_b64_tr_b16 a[150:151], v9 offset:10752\n"
    "v_mfma_f32_16x16x32_bf16 v[64:67], a[72:75], v[144:147], v[64:67]\n"
    "ds_read_b64_tr_b16 a[152:153], v8 offset:12800\n"
    "v_mul_f32 v112, v112, v46\n"
    "v_mfma_f32_16x16x32_bf16 v[72:75], a[72:75], v[152:155], v[72:75]\n"
    "ds_read_b64_tr_b16 a[154:155], v8 offset:14848\n"
    "v_mul_f32 v113, v113, v46\n"
    "v_mfma_f32_16x16x32_bf16 v[76:79], a[76:79], v[152:155], v[76:79]\n"
    "ds_read_b64_tr_b16 a[156:157], v9 offset:12800\n"
    "v_mul_f32 v114, v114, v46\n"
    "v_mfma_f32_16x16x32_bf16 v[68:71], a[76:79], v[144:147], v[68:71]\n"
    "ds_read_b64_tr_b16 a[158:159], v9 offset:14848\n"
    "v_mul_f32 v115, v115, v46\n"
    "v_mfma_f32_16x16x32_bf16 v[64:67], a[80:83], v[160:163], v[64:67]\n"
    "ds_read_b64_tr_b16 a[160:161], v10 offset:512\n"
    "v_mul_f32 v116, v116, v46\n"
    "v_mfma_f32_16x16x32_bf16 v[72:75], a[80:83], v[168:171], v[72:75]\n"
    "ds_read_b64_tr_b16 a[162:163], v10 offset:2560\n"
    "v_mul_f32 v117, v117, v46\n"
    "v_mfma_f32_16x16x32_bf16 v[76:79], a[84:87], v[168:171], v[76:79]\n"
    "ds_read_b64_tr_b16 a[164:165], v11 offset:512\n"
    "v_mul_f32 v118, v118, v46\n"
    "v_mfma_f32_16x16x32_bf16 v[68:71], a[84:87], v[160:163], v[68:71]\n"
    "ds_read_b64_tr_b16 a[166:167], v11 offset:2560\n"
    "v_mul_f32 v119, v119, v46\n"
    "v_mfma_f32_16x16x32_bf16 v[64:67], a[88:91], v[176:179], v[64:67]\n"
    "ds_read_b64_tr_b16 a[168:169], v10 offset:4608\n"
    "v_cvt_pk_bf16_f32 v112, v112, v113\n"
    "v_mfma_f32_16x16x32_bf16 v[72:75], a[88:91], v[184:187], v[72:75]\n"
    "ds_read_b64_tr_b16 a[170:171], v10 offset:6656\n"
    "v_cvt_pk_bf16_f32 v113, v114, v115\n"
    "v_mfma_f32_16x16x32_bf16 v[76:79], a[92:95], v[184:187], v[76:79]\n"
    "ds_read_b64_tr_b16 a[172:173], v11 offset:4608\n"
    "v_cvt_pk_bf16_f32 v114, v116, v117\n"
    "v_mfma_f32_16x16x32_bf16 v[68:71], a[92:95], v[176:179], v[68:71]\n"
    "ds_read_b64_tr_b16 a[174:175], v11 offset:6656\n"
    "v_cvt_pk_bf16_f32 v115, v118, v119\n"
    "v_mfma_f32_16x16x32_bf16 v[64:67], a[96:99], v[192:195], v[64:67]\n"
    "ds_read_b64_tr_b16 a[176:177], v10 offset:8704\n"
    "s_mov_b32 s68, 0x14000\n"
    "v_mfma_f32_16x16x32_bf16 v[72:75], a[96:99], v[200:203], v[72:75]\n"
    "ds_read_b64_tr_b16 a[178:179], v10 offset:10752\n"
    "v_mfma_f32_16x16x32_bf16 v[76:79], a[100:103], v[200:203], v[76:79]\n"
    "ds_read_b64_tr_b16 a[180:181], v11 offset:8704\n"
    "v_mfma_f32_16x16x32_bf16 v[68:71], a[100:103], v[192:195], v[68:71]\n"
    "ds_read_b64_tr_b16 a[182:183], v11 offset:10752\n"
    "v_mfma_f32_16x16x32_bf16 v[64:67], a[104:107], v[208:211], v[64:67]\n"
    "ds_read_b64_tr_b16 a[184:185], v10 offset:12800\n"
    "v_mfma_f32_16x16x32_bf16 v[72:75], a[104:107], v[216:219], v[72:75]\n"
    "ds_read_b64_tr_b16 a[186:187], v10 offset:14848\n"
    "v_mfma_f32_16x16x32_bf16 v[76:79], a[108:111], v[216:219], v[76:79]\n"
    "ds_read_b64_tr_b16 a[188:189], v11 offset:12800\n"
    "v_mfma_f32_16x16x32_bf16 v[68:71], a[108:111], v[208:211], v[68:71]\n"
    "ds_read_b64_tr_b16 a[190:191], v11 offset:14848\n"
    "v_mfma_f32_16x16x32_bf16 v[64:67], a[112:115], v[224:227], v[64:67]\n"
    "buffer_store_dwordx4 v[112:115], v45, s[56:59], s68 offen offset:2048 sc1\n"
    "v_mfma_f32_16x16x32_bf16 v[72:75], a[112:115], v[232:235], v[72:75]\n"
    "v_mul_f32 v120, v120, v47\n"
    "v_mul_f32 v121, v121, v47\n"
    "v_mfma_f32_16x16x32_bf16 v[76:79], a[116:119], v[232:235], v[76:79]\n"
    "v_mul_f32 v122, v122, v47\n"
    "v_mul_f32 v123, v123, v47\n"
    "v_mfma_f32_16x16x32_bf16 v[68:71], a[116:119], v[224:227], v[68:71]\n"
    "v_mul_f32 v124, v124, v47\n"
    "v_mul_f32 v125, v125, v47\n"
    "v_mfma_f32_16x16x32_bf16 v[64:67], a[120:123], v[240:243], v[64:67]\n"
    "v_mul_f32 v126, v126, v47\n"
    "v_mul_f32 v127, v127, v47\n"
    "v_mfma_f32_16x16x32_bf16 v[72:75], a[120:123], v[248:251], v[72:75]\n"
    "v_cvt_pk_bf16_f32 v120, v120, v121\n"
    "v_cvt_pk_bf16_f32 v121, v122, v123\n"
    "v_mfma_f32_16x16x32_bf16 v[76:79], a[124:127], v[248:251], v[76:79]\n"
    "v_cvt_pk_bf16_f32 v122, v124, v125\n"
    "v_cvt_pk_bf16_f32 v123, v126, v127\n"
    "v_mfma_f32_16x16x32_bf16 v[68:71], a[124:127], v[240:243], v[68:71]\n"
    "buffer_store_dwordx4 v[120:123], v45, s[56:59], s68 offen offset:3072 sc1\n"
    "s_waitcnt lgkmcnt(0)\n"
    "s_waitcnt vmcnt(10)\n"
    "buffer_store_dword v63, v33, s[20:23], 0 offen offset:64 sc1\n"
    "v_mfma_f32_16x16x32_bf16 v[80:83], a[128:131], v[128:131], 0\n"
    "s_cmp_eq_u32 s72, 0\n"
    "s_cselect_b64 s[74:75], -1, 0\n"
    "v_mfma_f32_16x16x32_bf16 v[84:87], a[132:135], v[128:131], 0\n"
    "v_cndmask_b32_e64 v14, v45, v15, s[74:75]\n"
    "v_mfma_f32_16x16x32_bf16 v[80:83], a[136:139], v[144:147], v[80:83]\n"
    "buffer_load_dword v2, v26, s[20:23], 0 offen sc1\n"
    "v_mfma_f32_16x16x32_bf16 v[84:87], a[140:143], v[144:147], v[84:87]\n"
    "buffer_load_dword v3, v26, s[20:23], 0 offen offset:64 sc1\n"
    "v_mfma_f32_16x16x32_bf16 v[80:83], a[144:147], v[160:163], v[80:83]\n"
    "v_mul_f32 v64, v64, v46\n"
    "v_mul_f32 v65, v65, v46\n"
    "v_mfma_f32_16x16x32_bf16 v[84:87], a[148:151], v[160:163], v[84:87]\n"
    "v_mul_f32 v66, v66, v46\n"
    "v_mul_f32 v67, v67, v46\n"
    "v_mfma_f32_16x16x32_bf16 v[80:83], a[152:155], v[176:179], v[80:83]\n"
    "v_mul_f32 v68, v68, v46\n"
    "v_mul_f32 v69, v69, v46\n"
    "v_mfma_f32_16x16x32_bf16 v[84:87], a[156:159], v[176:179], v[84:87]\n"
    "v_mul_f32 v70, v70, v46\n"
    "v_mul_f32 v71, v71, v46\n"
    "v_mfma_f32_16x16x32_bf16 v[80:83], a[160:163], v[192:195], v[80:83]\n"
    "v_cvt_pk_bf16_f32 v64, v64, v65\n"
    "v_cvt_pk_bf16_f32 v65, v66, v67\n"
    "v_mfma_f32_16x16x32_bf16 v[84:87], a[164:167], v[192:195], v[84:87]\n"
    "v_cvt_pk_bf16_f32 v66, v68, v69\n"
    "v_cvt_pk_bf16_f32 v67, v70, v71\n"
    "v_mfma_f32_16x16x32_bf16 v[80:83], a[168:171], v[208:211], v[80:83]\n"
    "s_mov_b32 s68, 0x18000\n"
    "v_cndmask_b32_e64 v34, v34, v64, s[74:75]\n"
    "v_mfma_f32_16x16x32_bf16 v[84:87], a[172:175], v[208:211], v[84:87]\n"
    "v_cndmask_b32_e64 v35, v35, v65, s[74:75]\n"
    "v_cndmask_b32_e64 v36, v36, v66, s[74:75]\n"
    "L_iw7:\n"
    "s_cbranch_execz L_iwback\n"
    "v_mfma_f32_16x16x32_bf16 v[80:83], a[176:179], v[224:227], v[80:83]\n"
    "v_cndmask_b32_e64 v37, v37, v67, s[74:75]\n"
    "v_mfma_f32_16x16x32_bf16 v[84:87], a[180:183], v[224:227], v[84:87]\n"
    "buffer_store_dwordx4 v[64:67], v14, s[56:59], s68 offen sc1\n"
    "v_mfma_f32_16x16x32_bf16 v[80:83], a[184:187], v[240:243], v[80:83]\n"
    "v_mul_f32 v72, v72, v47\n"
    "v_mul_f32 v73, v73, v47\n"
    "v_mfma_f32_16x16x32_bf16 v[84:87], a[188:191], v[240:243], v[84:87]\n"
    "v_mul_f32 v74, v74, v47\n"
    "v_mul_f32 v75, v75, v47\n"
    "v_mfma_f32_16x16x32_bf16 v[88:91], a[128:131], v[136:139], 0\n"
    "v_mul_f32 v76, v76, v47\n"
    "v_mul_f32 v77, v77, v47\n"
    "v_mfma_f32_16x16x32_bf16 v[92:95], a[132:135], v[136:139], 0\n"
    "v_mul_f32 v78, v78, v47\n"
    "v_mul_f32 v79, v79, v47\n"
    "v_mfma_f32_16x16x32_bf16 v[88:91], a[136:139], v[152:155], v[88:91]\n"
    "v_cvt_pk_bf16_f32 v72, v72, v73\n"
    "v_cvt_pk_bf16_f32 v73, v74, v75\n"
    "v_mfma_f32_16x16x32_bf16 v[92:95], a[140:143], v[152:155], v[92:95]\n"
    "v_cvt_pk_bf16_f32 v74, v76, v77\n"
    "v_cvt_pk_bf16_f32 v75, v78, v79\n"
    "v_mfma_f32_16x16x32_bf16 v[88:91], a[144:147], v[168:171], v[88:91]\n"
    "buffer_store_dwordx4 v[72:75], v45, s[56:59], s68 offen offset:1024 sc1\n"
    "v_mfma_f32_16x16x32_bf16 v[92:95], a[148:151], v[168:171], v[92:95]\n"
    "v_mul_f32 v80, v80, v46\n"
    "v_mul_f32 v81, v81, v46\n"
    "v_mfma_f32_16x16x32_bf16 v[88:91], a[152:155], v[184:187], v[88:91]\n"
    "v_mul_f32 v82, v82, v46\n"
    "v_mul_f32 v83, v83, v46\n"
    "v_mfma_f32_16x16x32_bf16 v[92:95], a[156:159], v[184:187], v[92:95]\n"
    "v_mul_f32 v84, v84, v46\n"
    "v_mul_f32 v85, v85, v46\n"
    "v_mfma_f32_16x16x32_bf16 v[88:91], a[160:163], v[200:203], v[88:91]\n"
    "v_mul_f32 v86, v86, v46\n"
    "v_mul_f32 v87, v87, v46\n"
    "v_mfma_f32_16x16x32_bf16 v[92:95], a[164:167], v[200:203], v[92:95]\n"
    "v_cvt_pk_bf16_f32 v80, v80, v81\n"
    "v_cvt_pk_bf16_f32 v81, v82, v83\n"
    "v_mfma_f32_16x16x32_bf16 v[88:91], a[168:171], v[216:219], v[88:91]\n"
    "v_cvt_pk_bf16_f32 v82, v84, v85\n"
    "v_cvt_pk_bf16_f32 v83, v86, v87\n"
    "v_mfma_f32_16x16x32_bf16 v[92:95], a[172:175], v[216:219], v[92:95]\n"
    "buffer_store_dwordx4 v[80:83], v45, s[56:59], s68 offen offset:2048 sc1\n"
    "v_mfma_f32_16x16x32_bf16 v[88:91], a[176:179], v[232:235], v[88:91]\n"
    "v_mfma_f32_16x16x32_bf16 v[92:95], a[180:183], v[232:235], v[92:95]\n"
    "v_mfma_f32_16x16x32_bf16 v[88:91], a[184:187], v[248:251], v[88:91]\n"
    "v_mfma_f32_16x16x32_bf16 v[92:95], a[188:191], v[248:251], v[92:95]\n"
    "L_oe:\n"
    "s_nop 3\n"
    "s_nop 0\n"
    "s_nop 0\n"
    "s_nop 0\n"
    "v_mul_f32 v88, v88, v47\n"
    "v_mul_f32 v89, v89, v47\n"
    "v_mul_f32 v90, v90, v47\n"
    "v_mul_f32 v91, v91, v47\n"
    "v_mul_f32 v92, v92, v47\n"
    "v_mul_f32 v93, v93, v47\n"
    "v_mul_f32 v94, v94, v47\n"
    "v_mul_f32 v95, v95, v47\n"
    "v_cvt_pk_bf16_f32 v88, v88, v89\n"
    "v_cvt_pk_bf16_f32 v89, v90, v91\n"
    "v_cvt_pk_bf16_f32 v90, v92, v93\n"
    "v_cvt_pk_bf16_f32 v91, v94, v95\n"
    "buffer_store_dwordx4 v[88:91], v45, s[56:59], s68 offen offset:3072 sc1\n"
    "s_waitcnt vmcnt(4)\n"
    "buffer_store_dword v63, v32, s[20:23], 0 offen offset:64 sc1\n"
    "buffer_store_dword v63, v31, s[20:23], 0 offen offset:64 sc1\n"
    "s_movk_i32 s18, 0x380\n"
    "v_and_b32 v56, 15, v0\n"
    "v_lshrrev_b32 v57, 4, v0\n"
    "v_lshlrev_b32 v57, 4, v57\n"
    "v_mad_u32_u24 v6, v56, s18, v57\n"
    "s_mul_i32 s18, s69, 0x7000\n"
    "v_add_u32 v6, s18, v6\n"
    "s_add_u32 s18, s77, 1\n"
    "v_add_u32 v56, s18, v0\n"
    "v_and_b32 v56, 15, v56\n"
    "v_mul_u32_u24 v57, 0x1c000, v56\n"
    "v_xor_b32 v56, s77, v56\n"
    "v_and_b32 v56, 3, v56\n"
    "v_lshl_add_u32 v58, v56, 10, v57\n"
    "s_lshl_b32 s40, s72, 14\n"
    "v_add_u32 v16, s40, v45\n"
    "s_sub_u32 s40, 0x18000, s40\n"
    "v_add_u32 v60, s40, v45\n"
    "s_lshl_b32 s36, s72, 7\n"
    "s_sub_u32 s37, 0x300, s36\n"
    "s_add_u32 s36, s36, s19\n"
    "s_sub_u32 s40, s36, 0x380\n"
    "s_cmp_ge_u32 s36, 0x380\n"
    "s_cselect_b32 s36, s40, s36\n"
    "s_add_u32 s37, s37, s19\n"
    "s_sub_u32 s40, s37, 0x380\n"
    "s_cmp_ge_u32 s37, 0x380\n"
    "s_cselect_b32 s37, s40, s37\n"
    "v_readlane_b32 s4, v58, 0\n"
    "v_readlane_b32 s5, v58, 1\n"
    "v_readlane_b32 s6, v58, 2\n"
    "v_readlane_b32 s7, v58, 3\n"
    "v_readlane_b32 s8, v58, 4\n"
    "v_readlane_b32 s9, v58, 5\n"
    "v_readlane_b32 s10, v58, 6\n"
    "v_readlane_b32 s44, v58, 7\n"
    "v_readlane_b32 s45, v58, 8\n"
    "v_readlane_b32 s46, v58, 9\n"
    "v_readlane_b32 s47, v58, 10\n"
    "v_readlane_b32 s48, v58, 11\n"
    "v_readlane_b32 s49, v58, 12\n"
    "v_readlane_b32 s50, v58, 13\n"
    "v_readlane_b32 s51, v58, 14\n"
    "v_and_b32 v56, 1, v0\n"
    "v_lshlrev_b32 v56, 4, v56\n"
    "v_mov_b32 v57, 0x3f80\n"
    "v_lshlrev_b32 v56, v56, v57\n"
    "v_bfe_u32 v57, v0, 3, 1\n"
    "v_lshlrev_b32 v57, 1, v57\n"
    "v_bfe_u32 v58, v0, 1, 1\n"
    "v_add_u32 v57, v57, v58\n"
    "v_lshrrev_b32 v58, 4, v0\n"
    "v_lshl_add_u32 v57, v58, 2, v57\n"
    "v_bfe_u32 v58, v0, 2, 1\n"
    "v_lshlrev_b32 v58, 3, v58\n"
    "v_sub_u32 v57, v57, v58\n"
    "v_cmp_eq_u32 vcc, 0, v57\n"
    "s_nop 1\n"
    "v_cndmask_b32 v48, 0, v56, vcc\n"
    "v_cmp_eq_u32 vcc, 1, v57\n"
    "s_nop 1\n"
    "v_cndmask_b32 v49, 0, v56, vcc\n"
    "v_cmp_eq_u32 vcc, 2, v57\n"
    "s_nop 1\n"
    "v_cndmask_b32 v50, 0, v56, vcc\n"
    "v_cmp_eq_u32 vcc, 3, v57\n"
    "s_nop 1\n"
    "v_cndmask_b32 v51, 0, v56, vcc\n"
    "v_cmp_eq_u32 vcc, 4, v57\n"
    "s_nop 1\n"
    "v_cndmask_b32 v52, 0, v56, vcc\n"
    "v_cmp_eq_u32 vcc, 5, v57\n"
    "s_nop 1\n"
    "v_cndmask_b32 v53, 0, v56, vcc\n"
    "v_cmp_eq_u32 vcc, 6, v57\n"
    "s_nop 1\n"
    "v_cndmask_b32 v54, 0, v56, vcc\n"
    "v_cmp_eq_u32 vcc, 7, v57\n"
    "s_nop 1\n"
    "v_cndmask_b32 v55, 0, v56, vcc\n"
    "s_waitcnt vmcnt(2)\n"
    "buffer_store_dword v63, v30, s[20:23], 0 offen offset:64 sc1\n"
    "s_cmp_eq_u32 s72, 3\n"
    "s_cbranch_scc1 L_r1\n"
    "s_mov_b32 s18, 1\n"
    "L_sp1:\n"
    "v_cmp_ne_u32 vcc, v62, v2\n"
    "s_cbranch_vccz L_ok1\n"
    "s_and_b32 s39, s18, 7\n"
    "s_cbranch_scc1 L_sq1\n"
    "buffer_load_dword v63, v29, s[20:23], 0 offen sc1\n"
    "s_waitcnt vmcnt(0)\n"
    "buffer_store_dword v63, v30, s[20:23], 0 offen sc1\n"
    "buffer_store_dword v63, v31, s[20:23], 0 offen sc1\n"
    "buffer_store_dword v63, v32, s[20:23], 0 offen sc1\n"
    "buffer_store_dword v63, v33, s[20:23], 0 offen offset:64 sc1\n"
    "buffer_store_dword v63, v32, s[20:23], 0 offen offset:64 sc1\n"
    "buffer_store_dword v63, v31, s[20:23], 0 offen offset:64 sc1\n"
    "buffer_store_dword v63, v30, s[20:23], 0 offen offset:64 sc1\n"
    "L_sq1:\n"
    "buffer_load_dword v2, v26, s[20:23], 0 offen sc1\n"
    "s_waitcnt vmcnt(0)\n"
    "s_add_u32 s18, s18, 1\n"
    "s_cmp_lt_u32 s18, 0x10000\n"
    "s_cbranch_scc1 L_sp1\n"
    "L_ok1:\n"
    "buffer_load_dwordx4 a[0:3], v16, s[24:27], s4 offen sc1\n"
    "buffer_load_dwordx4 a[4:7], v16, s[24:27], s5 offen sc1\n"
    "buffer_load_dwordx4 a[8:11], v16, s[24:27], s6 offen sc1\n"
    "buffer_load_dwordx4 a[12:15], v16, s[24:27], s7 offen sc1\n"
    "buffer_load_dwordx4 a[16:19], v16, s[24:27], s8 offen sc1\n"
    "buffer_load_dwordx4 a[20:23], v16, s[24:27], s9 offen sc1\n"
    "buffer_load_dwordx4 a[24:27], v16, s[24:27], s10 offen sc1\n"
    "buffer_load_dwordx4 a[28:31], v16, s[24:27], s44 offen sc1\n"
    "buffer_load_dwordx4 a[32:35], v16, s[24:27], s45 offen sc1\n"
    "buffer_load_dwordx4 a[36:39], v16, s[24:27], s46 offen sc1\n"
    "buffer_load_dwordx4 a[40:43], v16, s[24:27], s47 offen sc1\n"
    "buffer_load_dwordx4 a[44:47], v16, s[24:27], s48 offen sc1\n"
    "buffer_load_dwordx4 a[48:51], v16, s[24:27], s49 offen sc1\n"
    "buffer_load_dwordx4 a[52:55], v16, s[24:27], s50 offen sc1\n"
    "buffer_load_dwordx4 a[56:59], v16, s[24:27], s51 offen sc1\n"
    "buffer_load_dword v3, v26, s[20:23], 0 offen offset:64 sc1\n"
    "v_mfma_f32_16x16x32_bf16 v[96:99], v[48:51], v[22:25], 0\n"
    "v_mfma_f32_16x16x32_bf16 v[100:103], v[52:55], v[22:25], 0\n"
    "s_waitcnt vmcnt(14)\n"
    "v_mfma_f32_16x16x32_bf16 v[96:99], v[48:51], a[0:3], v[96:99]\n"
    "v_mfma_f32_16x16x32_bf16 v[100:103], v[52:55], a[0:3], v[100:103]\n"
    "v_mfma_f32_16x16x32_bf16 v[96:99], v[48:51], a[4:7], v[96:99]\n"
    "v_mfma_f32_16x16x32_bf16 v[100:103], v[52:55], a[4:7], v[100:103]\n"
    "s_waitcnt vmcnt(12)\n"
    "v_mfma_f32_16x16x32_bf16 v[96:99], v[48:51], a[8:11], v[96:99]\n"
    "v_mfma_f32_16x16x32_bf16 v[100:103], v[52:55], a[8:11], v[100:103]\n"
    "v_mfma_f32_16x16x32_bf16 v[96:99], v[48:51], a[12:15], v[96:99]\n"
    "v_mfma_f32_16x16x32_bf16 v[100:103], v[52:55], a[12:15], v[100:103]\n"
    "s_waitcnt vmcnt(10)\n"
    "v_mfma_f32_16x16x32_bf16 v[96:99], v[48:51], a[16:19], v[96:99]\n"
    "v_mfma_f32_16x16x32_bf16 v[100:103], v[52:55], a[16:19], v[100:103]\n"
    "v_mfma_f32_16x16x32_bf16 v[96:99], v[48:51], a[20:23], v[96:99]\n"
    "v_mfma_f32_16x16x32_bf16 v[100:103], v[52:55], a[20:23], v[100:103]\n"
    "s_waitcnt vmcnt(8)\n"
    "v_mfma_f32_16x16x32_bf16 v[96:99], v[48:51], a[24:27], v[96:99]\n"
    "v_mfma_f32_16x16x32_bf16 v[100:103], v[52:55], a[24:27], v[100:103]\n"
    "v_mfma_f32_16x16x32_bf16 v[96:99], v[48:51], a[28:31], v[96:99]\n"
    "v_mfma_f32_16x16x32_bf16 v[100:103], v[52:55], a[28:31], v[100:103]\n"
    "s_waitcnt vmcnt(6)\n"
    "v_mfma_f32_16x16x32_bf16 v[96:99], v[48:51], a[32:35], v[96:99]\n"
    "v_mfma_f32_16x16x32_bf16 v[100:103], v[52:55], a[32:35], v[100:103]\n"
    "v_mfma_f32_16x16x32_bf16 v[96:99], v[48:51], a[36:39], v[96:99]\n"
    "v_mfma_f32_16x16x32_bf16 v[100:103], v[52:55], a[36:39], v[100:103]\n"
    "s_waitcnt vmcnt(4)\n"
    "v_mfma_f32_16x16x32_bf16 v[96:99], v[48:51], a[40:43], v[96:99]\n"
    "v_mfma_f32_16x16x32_bf16 v[100:103], v[52:55], a[40:43], v[100:103]\n"
    "v_mfma_f32_16x16x32_bf16 v[96:99], v[48:51], a[44:47], v[96:99]\n"
    "v_mfma_f32_16x16x32_bf16 v[100:103], v[52:55], a[44:47], v[100:103]\n"
    "s_waitcnt vmcnt(2)\n"
    "v_mfma_f32_16x16x32_bf16 v[96:99], v[48:51], a[48:51], v[96:99]\n"
    "v_mfma_f32_16x16x32_bf16 v[100:103], v[52:55], a[48:51], v[100:103]\n"
    "v_mfma_f32_16x16x32_bf16 v[96:99], v[48:51], a[52:55], v[96:99]\n"
    "v_mfma_f32_16x16x32_bf16 v[100:103], v[52:55], a[52:55], v[100:103]\n"
    "s_waitcnt vmcnt(1)\n"
    "v_mfma_f32_16x16x32_bf16 v[96:99], v[48:51], a[56:59], v[96:99]\n"
    "v_mfma_f32_16x16x32_bf16 v[100:103], v[52:55], a[56:59], v[100:103]\n"
    "s_nop 7\n"
    "v_cvt_pk_bf16_f32 v96, v96, v97\n"
    "v_cvt_pk_bf16_f32 v97, v98, v99\n"
    "v_cvt_pk_bf16_f32 v98, v100, v101\n"
    "v_cvt_pk_bf16_f32 v99, v102, v103\n"
    "buffer_store_dwordx4 v[96:99], v6, s[28:31], s36 offen\n"
    "s_waitcnt vmcnt(1)\n"
    "L_r1:\n"
    "L_iw8:\n"
    "s_cbranch_execz L_iwback\n"
    "s_mov_b32 s18, 1\n"
    "L_sp2:\n"
    "v_cmp_ne_u32 vcc, v62, v3\n"
    "s_cbranch_vccz L_ok2\n"
    "s_and_b32 s39, s18, 7\n"
    "s_cbranch_scc1 L_sq2\n"
    "buffer_load_dword v63, v29, s[20:23], 0 offen sc1\n"
    "s_waitcnt vmcnt(0)\n"
    "buffer_store_dword v63, v30, s[20:23], 0 offen sc1\n"
    "buffer_store_dword v63, v31, s[20:23], 0 offen sc1\n"
    "buffer_store_dword v63, v32, s[20:23], 0 offen sc1\n"
    "buffer_store_dword v63, v33, s[20:23], 0 offen offset:64 sc1\n"
    "buffer_store_dword v63, v32, s[20:23], 0 offen offset:64 sc1\n"
    "buffer_store_dword v63, v31, s[20:23], 0 offen offset:64 sc1\n"
    "buffer_store_dword v63, v30, s[20:23], 0 offen offset:64 sc1\n"
    "L_sq2:\n"
    "buffer_load_dword v3, v26, s[20:23], 0 offen offset:64 sc1\n"
    "s_waitcnt vmcnt(0)\n"
    "s_add_u32 s18, s18, 1\n"
    "s_cmp_lt_u32 s18, 0x10000\n"
    "s_cbranch_scc1 L_sp2\n"
    "L_ok2:\n"
    "buffer_load_dwordx4 a[192:195], v60, s[24:27], s4 offen sc1\n"
    "buffer_load_dwordx4 a[196:199], v60, s[24:27], s5 offen sc1\n"
    "buffer_load_dwordx4 a[200:203], v60, s[24:27], s6 offen sc1\n"
    "buffer_load_dwordx4 a[204:207], v60, s[24:27], s7 offen sc1\n"
    "buffer_load_dwordx4 a[208:211], v60, s[24:27], s8 offen sc1\n"
    "buffer_load_dwordx4 a[212:215], v60, s[24:27], s9 offen sc1\n"
    "buffer_load_dwordx4 a[216:219], v60, s[24:27], s10 offen sc1\n"
    "buffer_load_dwordx4 a[220:223], v60, s[24:27], s44 offen sc1\n"
    "buffer_load_dwordx4 a[224:227], v60, s[24:27], s45 offen sc1\n"
    "buffer_load_dwordx4 a[228:231], v60, s[24:27], s46 offen sc1\n"
    "buffer_load_dwordx4 a[232:235], v60, s[24:27], s47 offen sc1\n"
    "buffer_load_dwordx4 a[236:239], v60, s[24:27], s48 offen sc1\n"
    "buffer_load_dwordx4 a[240:243], v60, s[24:27], s49 offen sc1\n"
    "buffer_load_dwordx4 a[244:247], v60, s[24:27], s50 offen sc1\n"
    "buffer_load_dwordx4 a[248:251], v60, s[24:27], s51 offen sc1\n"
    "buffer_load_dword v7, v29, s[20:23], 0 offen sc1\n"
    "v_mfma_f32_16x16x32_bf16 v[104:107], v[48:51], v[34:37], 0\n"
    "v_mfma_f32_16x16x32_bf16 v[108:111], v[52:55], v[34:37], 0\n"
    "s_waitcnt vmcnt(14)\n"
    "v_mfma_f32_16x16x32_bf16 v[104:107], v[48:51], a[192:195], v[104:107]\n"
    "v_mfma_f32_16x16x32_bf16 v[108:111], v[52:55], a[192:195], v[108:111]\n"
    "v_mfma_f32_16x16x32_bf16 v[104:107], v[48:51], a[196:199], v[104:107]\n"
    "v_mfma_f32_16x16x32_bf16 v[108:111], v[52:55], a[196:199], v[108:111]\n"
    "s_waitcnt vmcnt(12)\n"
    "v_mfma_f32_16x16x32_bf16 v[104:107], v[48:51], a[200:203], v[104:107]\n"
    "v_mfma_f32_16x16x32_bf16 v[108:111], v[52:55], a[200:203], v[108:111]\n"
    "v_mfma_f32_16x16x32_bf16 v[104:107], v[48:51], a[204:207], v[104:107]\n"
    "v_mfma_f32_16x16x32_bf16 v[108:111], v[52:55], a[204:207], v[108:111]\n"
    "s_waitcnt vmcnt(10)\n"
    "v_mfma_f32_16x16x32_bf16 v[104:107], v[48:51], a[208:211], v[104:107]\n"
    "v_mfma_f32_16x16x32_bf16 v[108:111], v[52:55], a[208:211], v[108:111]\n"
    "v_mfma_f32_16x16x32_bf16 v[104:107], v[48:51], a[212:215], v[104:107]\n"
    "v_mfma_f32_16x16x32_bf16 v[108:111], v[52:55], a[212:215], v[108:111]\n"
    "s_waitcnt vmcnt(8)\n"
    "v_mfma_f32_16x16x32_bf16 v[104:107], v[48:51], a[216:219], v[104:107]\n"
    "v_mfma_f32_16x16x32_bf16 v[108:111], v[52:55], a[216:219], v[108:111]\n"
    "v_mfma_f32_16x16x32_bf16 v[104:107], v[48:51], a[220:223], v[104:107]\n"
    "v_mfma_f32_16x16x32_bf16 v[108:111], v[52:55], a[220:223], v[108:111]\n"
    "s_waitcnt vmcnt(6)\n"
    "v_mfma_f32_16x16x32_bf16 v[104:107], v[48:51], a[224:227], v[104:107]\n"
    "v_mfma_f32_16x16x32_bf16 v[108:111], v[52:55], a[224:227], v[108:111]\n"
    "v_mfma_f32_16x16x32_bf16 v[104:107], v[48:51], a[228:231], v[104:107]\n"
    "v_mfma_f32_16x16x32_bf16 v[108:111], v[52:55], a[228:231], v[108:111]\n"
    "s_waitcnt vmcnt(4)\n"
    "v_mfma_f32_16x16x32_bf16 v[104:107], v[48:51], a[232:235], v[104:107]\n"
    "v_mfma_f32_16x16x32_bf16 v[108:111], v[52:55], a[232:235], v[108:111]\n"
    "v_mfma_f32_16x16x32_bf16 v[104:107], v[48:51], a[236:239], v[104:107]\n"
    "v_mfma_f32_16x16x32_bf16 v[108:111], v[52:55], a[236:239], v[108:111]\n"
    "s_waitcnt vmcnt(2)\n"
    "v_mfma_f32_16x16x32_bf16 v[104:107], v[48:51], a[240:243], v[104:107]\n"
    "v_mfma_f32_16x16x32_bf16 v[108:111], v[52:55], a[240:243], v[108:111]\n"
    "v_mfma_f32_16x16x32_bf16 v[104:107], v[48:51], a[244:247], v[104:107]\n"
    "v_mfma_f32_16x16x32_bf16 v[108:111], v[52:55], a[244:247], v[108:111]\n"
    "s_waitcnt vmcnt(1)\n"
    "v_mfma_f32_16x16x32_bf16 v[104:107], v[48:51], a[248:251], v[104:107]\n"
    "v_mfma_f32_16x16x32_bf16 v[108:111], v[52:55], a[248:251], v[108:111]\n"
    "s_nop 7\n"
    "v_cvt_pk_bf16_f32 v104, v104, v105\n"
    "v_cvt_pk_bf16_f32 v105, v106, v107\n"
    "v_cvt_pk_bf16_f32 v106, v108, v109\n"
    "v_cvt_pk_bf16_f32 v107, v110, v111\n"
    "buffer_store_dwordx4 v[104:107], v6, s[28:31], s37 offen\n"
    "s_waitcnt vmcnt(1)\n"
    "v_cmp_ne_u32 vcc, v63, v7\n"
    "s_cbranch_vccnz L_resend\n"
    "L_done:\n"
    "s_endpgm\n"
    "L_stub0:\n"
    "s_call_b64 s[60:61], L_rare1\n"
    "s_branch L_ret0\n"
    "L_stub1:\n"
    "s_call_b64 s[60:61], L_rare0\n"
    "s_branch L_ret1\n"
    "L_stub2:\n"
    "s_call_b64 s[60:61], L_rare1\n"
    "s_branch L_ret2\n"
    "L_stub3:\n"
    "s_call_b64 s[60:61], L_rare0\n"
    "s_branch L_ret3\n"
    "L_stub4:\n"
    "s_call_b64 s[60:61], L_rare1\n"
    "s_branch L_ret4\n"
    "L_stub5:\n"
    "s_call_b64 s[60:61], L_rare0\n"
    "s_branch L_ret5\n"
    "L_stub6:\n"
    "s_call_b64 s[60:61], L_rare1\n"
    "s_branch L_ret6\n"
    "L_stub7:\n"
    "s_call_b64 s[60:61], L_rare0\n"
    "s_branch L_ret7\n"
    "L_stub8:\n"
    "s_call_b64 s[60:61], L_rare1\n"
    "s_branch L_ret8\n"
    "L_stub9:\n"
    "s_call_b64 s[60:61], L_rare0\n"
    "s_branch L_ret9\n"
    "L_stub10:\n"
    "s_call_b64 s[60:61], L_rare1\n"
    "s_branch L_ret10\n"
    "L_slow1:\n"
    "L_resend:\n"
    "v_mov_b32 v63, v7\n"
    "s_nop 1\n"
    "buffer_store_dword v63, v30, s[20:23], 0 offen sc1\n"
    "buffer_store_dword v63, v31, s[20:23], 0 offen sc1\n"
    "buffer_store_dword v63, v32, s[20:23], 0 offen sc1\n"
    "buffer_store_dword v63, v33, s[20:23], 0 offen offset:64 sc1\n"
    "buffer_store_dword v63, v32, s[20:23], 0 offen offset:64 sc1\n"
    "buffer_store_dword v63, v31, s[20:23], 0 offen offset:64 sc1\n"
    "buffer_store_dword v63, v30, s[20:23], 0 offen offset:64 sc1\n"
    "s_branch L_done\n"
    "L_rare0:\n"
    "s_nop 15\n"
    "v_max3_f32 v56, v64, v65, v66\n"
    "v_max3_f32 v57, v67, v68, v69\n"
    "v_max3_f32 v56, v56, v70, v71\n"
    "v_max_f32 v56, v56, v57\n"
    "v_mov_b32 v57, v56\n"
    "s_nop 1\n"
    "v_permlane32_swap_b32 v56, v57\n"
    "v_max_f32 v56, v56, v57\n"
    "v_mov_b32 v57, v56\n"
    "s_nop 1\n"
    "v_permlane16_swap_b32 v56, v57\n"
    "v_max_f32 v56, v56, v57\n"
    "v_max_f32 v56, 0, v56\n"
    "v_sub_f32 v58, 0, v56\n"
    "v_exp_f32 v58, v58\n"
    "v_sub_f32 v120, v120, v56\n"
    "v_sub_f32 v121, v121, v56\n"
    "v_sub_f32 v122, v122, v56\n"
    "v_sub_f32 v123, v123, v56\n"
    "v_sub_f32 v64, v64, v56\n"
    "v_sub_f32 v65, v65, v56\n"
    "v_sub_f32 v66, v66, v56\n"
    "v_sub_f32 v67, v67, v56\n"
    "v_sub_f32 v68, v68, v56\n"
    "v_sub_f32 v69, v69, v56\n"
    "v_sub_f32 v70, v70, v56\n"
    "v_sub_f32 v71, v71, v56\n"
    "v_sub_f32 v80, v80, v56\n"
    "v_sub_f32 v81, v81, v56\n"
    "v_sub_f32 v82, v82, v56\n"
    "v_sub_f32 v83, v83, v56\n"
    "v_sub_f32 v84, v84, v56\n"
    "v_sub_f32 v85, v85, v56\n"
    "v_sub_f32 v86, v86, v56\n"
    "v_sub_f32 v87, v87, v56\n"
    "v_mul_f32 v40, v40, v58\n"
    "v_mul_f32 v128, v128, v58\n"
    "v_mul_f32 v129, v129, v58\n"
    "v_mul_f32 v130, v130, v58\n"
    "v_mul_f32 v131, v131, v58\n"
    "v_mul_f32 v132, v132, v58\n"
    "v_mul_f32 v133, v133, v58\n"
    "v_mul_f32 v134, v134, v58\n"
    "v_mul_f32 v135, v135, v58\n"
    "v_mul_f32 v144, v144, v58\n"
    "v_mul_f32 v145, v145, v58\n"
    "v_mul_f32 v146, v146, v58\n"
    "v_mul_f32 v147, v147, v58\n"
    "v_mul_f32 v148, v148, v58\n"
    "v_mul_f32 v149, v149, v58\n"
    "v_mul_f32 v150, v150, v58\n"
    "v_mul_f32 v151, v151, v58\n"
    "v_mul_f32 v160, v160, v58\n"
    "v_mul_f32 v161, v161, v58\n"
    "v_mul_f32 v162, v162, v58\n"
    "v_mul_f32 v163, v163, v58\n"
    "v_mul_f32 v164, v164, v58\n"
    "v_mul_f32 v165, v165, v58\n"
    "v_mul_f32 v166, v166, v58\n"
    "v_mul_f32 v167, v167, v58\n"
    "v_mul_f32 v176, v176, v58\n"
    "v_mul_f32 v177, v177, v58\n"
    "v_mul_f32 v178, v178, v58\n"
    "v_mul_f32 v179, v179, v58\n"
    "v_mul_f32 v180, v180, v58\n"
    "v_mul_f32 v181, v181, v58\n"
    "v_mul_f32 v182, v182, v58\n"
    "v_mul_f32 v183, v183, v58\n"
    "v_mul_f32 v192, v192, v58\n"
    "v_mul_f32 v193, v193, v58\n"
    "v_mul_f32 v194, v194, v58\n"
    "v_mul_f32 v195, v195, v58\n"
    "v_mul_f32 v196, v196, v58\n"
    "v_mul_f32 v197, v197, v58\n"
    "v_mul_f32 v198, v198, v58\n"
    "v_mul_f32 v199, v199, v58\n"
    "v_mul_f32 v208, v208, v58\n"
    "v_mul_f32 v209, v209, v58\n"
    "v_mul_f32 v210, v210, v58\n"
    "v_mul_f32 v211, v211, v58\n"
    "v_mul_f32 v212, v212, v58\n"
    "v_mul_f32 v213, v213, v58\n"
    "v_mul_f32 v214, v214, v58\n"
    "v_mul_f32 v215, v215, v58\n"
    "v_mul_f32 v224, v224, v58\n"
    "v_mul_f32 v225, v225, v58\n"
    "v_mul_f32 v226, v226, v58\n"
    "v_mul_f32 v227, v227, v58\n"
    "v_mul_f32 v228, v228, v58\n"
    "v_mul_f32 v229, v229, v58\n"
    "v_mul_f32 v230, v230, v58\n"
    "v_mul_f32 v231, v231, v58\n"
    "v_mul_f32 v240, v240, v58\n"
    "v_mul_f32 v241, v241, v58\n"
    "v_mul_f32 v242, v242, v58\n"
    "v_mul_f32 v243, v243, v58\n"
    "v_mul_f32 v244, v244, v58\n"
    "v_mul_f32 v245, v245, v58\n"
    "v_mul_f32 v246, v246, v58\n"
    "v_mul_f32 v247, v247, v58\n"
    "v_exp_f32 v48, v64\n"
    "v_exp_f32 v49, v65\n"
    "v_exp_f32 v50, v66\n"
    "v_exp_f32 v51, v67\n"
    "v_exp_f32 v52, v68\n"
    "v_exp_f32 v53, v69\n"
    "v_exp_f32 v54, v70\n"
    "v_exp_f32 v55, v71\n"
    "s_nop 0\n"
    "v_add_f32 v42, v48, v49\n"
    "v_add_f32 v42, v42, v50\n"
    "v_add_f32 v42, v42, v51\n"
    "v_add_f32 v42, v42, v52\n"
    "v_add_f32 v42, v42, v53\n"
    "v_add_f32 v42, v42, v54\n"
    "v_add_f32 v42, v42, v55\n"
    "v_cvt_pk_bf16_f32 v96, v48, v49\n"
    "v_cvt_pk_bf16_f32 v97, v50, v51\n"
    "v_cvt_pk_bf16_f32 v98, v52, v53\n"
    "v_cvt_pk_bf16_f32 v99, v54, v55\n"
    "v_max3_f32 v56, v72, v73, v74\n"
    "v_max3_f32 v57, v75, v76, v77\n"
    "v_max3_f32 v56, v56, v78, v79\n"
    "v_max_f32 v56, v56, v57\n"
    "v_mov_b32 v57, v56\n"
    "s_nop 1\n"
    "v_permlane32_swap_b32 v56, v57\n"
    "v_max_f32 v56, v56, v57\n"
    "v_mov_b32 v57, v56\n"
    "s_nop 1\n"
    "v_permlane16_swap_b32 v56, v57\n"
    "v_max_f32 v56, v56, v57\n"
    "v_max_f32 v56, 0, v56\n"
    "v_sub_f32 v58, 0, v56\n"
    "v_exp_f32 v58, v58\n"
    "v_sub_f32 v124, v124, v56\n"
    "v_sub_f32 v125, v125, v56\n"
    "v_sub_f32 v126, v126, v56\n"
    "v_sub_f32 v127, v127, v56\n"
    "v_sub_f32 v72, v72, v56\n"
    "v_sub_f32 v73, v73, v56\n"
    "v_sub_f32 v74, v74, v56\n"
    "v_sub_f32 v75, v75, v56\n"
    "v_sub_f32 v76, v76, v56\n"
    "v_sub_f32 v77, v77, v56\n"
    "v_sub_f32 v78, v78, v56\n"
    "v_sub_f32 v79, v79, v56\n"
    "v_sub_f32 v88, v88, v56\n"
    "v_sub_f32 v89, v89, v56\n"
    "v_sub_f32 v90, v90, v56\n"
    "v_sub_f32 v91, v91, v56\n"
    "v_sub_f32 v92, v92, v56\n"
    "v_sub_f32 v93, v93, v56\n"
    "v_sub_f32 v94, v94, v56\n"
    "v_sub_f32 v95, v95, v56\n"
    "v_mul_f32 v41, v41, v58\n"
    "v_mul_f32 v136, v136, v58\n"
    "v_mul_f32 v137, v137, v58\n"
    "v_mul_f32 v138, v138, v58\n"
    "v_mul_f32 v139, v139, v58\n"
    "v_mul_f32 v140, v140, v58\n"
    "v_mul_f32 v141, v141, v58\n"
    "v_mul_f32 v142, v142, v58\n"
    "v_mul_f32 v143, v143, v58\n"
    "v_mul_f32 v152, v152, v58\n"
    "v_mul_f32 v153, v153, v58\n"
    "v_mul_f32 v154, v154, v58\n"
    "v_mul_f32 v155, v155, v58\n"
    "v_mul_f32 v156, v156, v58\n"
    "v_mul_f32 v157, v157, v58\n"
    "v_mul_f32 v158, v158, v58\n"
    "v_mul_f32 v159, v159, v58\n"
    "v_mul_f32 v168, v168, v58\n"
    "v_mul_f32 v169, v169, v58\n"
    "v_mul_f32 v170, v170, v58\n"
    "v_mul_f32 v171, v171, v58\n"
    "v_mul_f32 v172, v172, v58\n"
    "v_mul_f32 v173, v173, v58\n"
    "v_mul_f32 v174, v174, v58\n"
    "v_mul_f32 v175, v175, v58\n"
    "v_mul_f32 v184, v184, v58\n"
    "v_mul_f32 v185, v185, v58\n"
    "v_mul_f32 v186, v186, v58\n"
    "v_mul_f32 v187, v187, v58\n"
    "v_mul_f32 v188, v188, v58\n"
    "v_mul_f32 v189, v189, v58\n"
    "v_mul_f32 v190, v190, v58\n"
    "v_mul_f32 v191, v191, v58\n"
    "v_mul_f32 v200, v200, v58\n"
    "v_mul_f32 v201, v201, v58\n"
    "v_mul_f32 v202, v202, v58\n"
    "v_mul_f32 v203, v203, v58\n"
    "v_mul_f32 v204, v204, v58\n"
    "v_mul_f32 v205, v205, v58\n"
    "v_mul_f32 v206, v206, v58\n"
    "v_mul_f32 v207, v207, v58\n"
    "v_mul_f32 v216, v216, v58\n"
    "v_mul_f32 v217, v217, v58\n"
    "v_mul_f32 v218, v218, v58\n"
    "v_mul_f32 v219, v219, v58\n"
    "v_mul_f32 v220, v220, v58\n"
    "v_mul_f32 v221, v221, v58\n"
    "v_mul_f32 v222, v222, v58\n"
    "v_mul_f32 v223, v223, v58\n"
    "v_mul_f32 v232, v232, v58\n"
    "v_mul_f32 v233, v233, v58\n"
    "v_mul_f32 v234, v234, v58\n"
    "v_mul_f32 v235, v235, v58\n"
    "v_mul_f32 v236, v236, v58\n"
    "v_mul_f32 v237, v237, v58\n"
    "v_mul_f32 v238, v238, v58\n"
    "v_mul_f32 v239, v239, v58\n"
    "v_mul_f32 v248, v248, v58\n"
    "v_mul_f32 v249, v249, v58\n"
    "v_mul_f32 v250, v250, v58\n"
    "v_mul_f32 v251, v251, v58\n"
    "v_mul_f32 v252, v252, v58\n"
    "v_mul_f32 v253, v253, v58\n"
    "v_mul_f32 v254, v254, v58\n"
    "v_mul_f32 v255, v255, v58\n"
    "v_exp_f32 v48, v72\n"
    "v_exp_f32 v49, v73\n"
    "v_exp_f32 v50, v74\n"
    "v_exp_f32 v51, v75\n"
    "v_exp_f32 v52, v76\n"
    "v_exp_f32 v53, v77\n"
    "v_exp_f32 v54, v78\n"
    "v_exp_f32 v55, v79\n"
    "s_nop 0\n"
    "v_add_f32 v43, v48, v49\n"
    "v_add_f32 v43, v43, v50\n"
    "v_add_f32 v43, v43, v51\n"
    "v_add_f32 v43, v43, v52\n"
    "v_add_f32 v43, v43, v53\n"
    "v_add_f32 v43, v43, v54\n"
    "v_add_f32 v43, v43, v55\n"
    "v_cvt_pk_bf16_f32 v100, v48, v49\n"
    "v_cvt_pk_bf16_f32 v101, v50, v51\n"
    "v_cvt_pk_bf16_f32 v102, v52, v53\n"
    "v_cvt_pk_bf16_f32 v103, v54, v55\n"
    "s_nop 3\n"
    "s_setpc_b64 s[60:61]\n"
    "L_rare1:\n"
    "s_nop 15\n"
    "v_max3_f32 v56, v80, v81, v82\n"
    "v_max3_f32 v57, v83, v84, v85\n"
    "v_max3_f32 v56, v56, v86, v87\n"
    "v_max_f32 v56, v56, v57\n"
    "v_mov_b32 v57, v56\n"
    "s_nop 1\n"
    "v_permlane32_swap_b32 v56, v57\n"
    "v_max_f32 v56, v56, v57\n"
    "v_mov_b32 v57, v56\n"
    "s_nop 1\n"
    "v_permlane16_swap_b32 v56, v57\n"
    "v_max_f32 v56, v56, v57\n"
    "v_max_f32 v56, 0, v56\n"
    "v_sub_f32 v58, 0, v56\n"
    "v_exp_f32 v58, v58\n"
    "v_sub_f32 v120, v120, v56\n"
    "v_sub_f32 v121, v121, v56\n"
    "v_sub_f32 v122, v122, v56\n"
    "v_sub_f32 v123, v123, v56\n"
    "v_sub_f32 v64, v64, v56\n"
    "v_sub_f32 v65, v65, v56\n"
    "v_sub_f32 v66, v66, v56\n"
    "v_sub_f32 v67, v67, v56\n"
    "v_sub_f32 v68, v68, v56\n"
    "v_sub_f32 v69, v69, v56\n"
    "v_sub_f32 v70, v70, v56\n"
    "v_sub_f32 v71, v71, v56\n"
    "v_sub_f32 v80, v80, v56\n"
    "v_sub_f32 v81, v81, v56\n"
    "v_sub_f32 v82, v82, v56\n"
    "v_sub_f32 v83, v83, v56\n"
    "v_sub_f32 v84, v84, v56\n"
    "v_sub_f32 v85, v85, v56\n"
    "v_sub_f32 v86, v86, v56\n"
    "v_sub_f32 v87, v87, v56\n"
    "v_mul_f32 v40, v40, v58\n"
    "v_mul_f32 v128, v128, v58\n"
    "v_mul_f32 v129, v129, v58\n"
    "v_mul_f32 v130, v130, v58\n"
    "v_mul_f32 v131, v131, v58\n"
    "v_mul_f32 v132, v132, v58\n"
    "v_mul_f32 v133, v133, v58\n"
    "v_mul_f32 v134, v134, v58\n"
    "v_mul_f32 v135, v135, v58\n"
    "v_mul_f32 v144, v144, v58\n"
    "v_mul_f32 v145, v145, v58\n"
    "v_mul_f32 v146, v146, v58\n"
    "v_mul_f32 v147, v147, v58\n"
    "v_mul_f32 v148, v148, v58\n"
    "v_mul_f32 v149, v149, v58\n"
    "v_mul_f32 v150, v150, v58\n"
    "v_mul_f32 v151, v151, v58\n"
    "v_mul_f32 v160, v160, v58\n"
    "v_mul_f32 v161, v161, v58\n"
    "v_mul_f32 v162, v162, v58\n"
    "v_mul_f32 v163, v163, v58\n"
    "v_mul_f32 v164, v164, v58\n"
    "v_mul_f32 v165, v165, v58\n"
    "v_mul_f32 v166, v166, v58\n"
    "v_mul_f32 v167, v167, v58\n"
    "v_mul_f32 v176, v176, v58\n"
    "v_mul_f32 v177, v177, v58\n"
    "v_mul_f32 v178, v178, v58\n"
    "v_mul_f32 v179, v179, v58\n"
    "v_mul_f32 v180, v180, v58\n"
    "v_mul_f32 v181, v181, v58\n"
    "v_mul_f32 v182, v182, v58\n"
    "v_mul_f32 v183, v183, v58\n"
    "v_mul_f32 v192, v192, v58\n"
    "v_mul_f32 v193, v193, v58\n"
    "v_mul_f32 v194, v194, v58\n"
    "v_mul_f32 v195, v195, v58\n"
    "v_mul_f32 v196, v196, v58\n"
    "v_mul_f32 v197, v197, v58\n"
    "v_mul_f32 v198, v198, v58\n"
    "v_mul_f32 v199, v199, v58\n"
    "v_mul_f32 v208, v208, v58\n"
    "v_mul_f32 v209, v209, v58\n"
    "v_mul_f32 v210, v210, v58\n"
    "v_mul_f32 v211, v211, v58\n"
    "v_mul_f32 v212, v212, v58\n"
    "v_mul_f32 v213, v213, v58\n"
    "v_mul_f32 v214, v214, v58\n"
    "v_mul_f32 v215, v215, v58\n"
    "v_mul_f32 v224, v224, v58\n"
    "v_mul_f32 v225, v225, v58\n"
    "v_mul_f32 v226, v226, v58\n"
    "v_mul_f32 v227, v227, v58\n"
    "v_mul_f32 v228, v228, v58\n"
    "v_mul_f32 v229, v229, v58\n"
    "v_mul_f32 v230, v230, v58\n"
    "v_mul_f32 v231, v231, v58\n"
    "v_mul_f32 v240, v240, v58\n"
    "v_mul_f32 v241, v241, v58\n"
    "v_mul_f32 v242, v242, v58\n"
    "v_mul_f32 v243, v243, v58\n"
    "v_mul_f32 v244, v244, v58\n"
    "v_mul_f32 v245, v245, v58\n"
    "v_mul_f32 v246, v246, v58\n"
    "v_mul_f32 v247, v247, v58\n"
    "v_exp_f32 v48, v80\n"
    "v_exp_f32 v49, v81\n"
    "v_exp_f32 v50, v82\n"
    "v_exp_f32 v51, v83\n"
    "v_exp_f32 v52, v84\n"
    "v_exp_f32 v53, v85\n"
    "v_exp_f32 v54, v86\n"
    "v_exp_f32 v55, v87\n"
    "s_nop 0\n"
    "v_add_f32 v42, v48, v49\n"
    "v_add_f32 v42, v42, v50\n"
    "v_add_f32 v42, v42, v51\n"
    "v_add_f32 v42, v42, v52\n"
    "v_add_f32 v42, v42, v53\n"
    "v_add_f32 v42, v42, v54\n"
    "v_add_f32 v42, v42, v55\n"
    "v_cvt_pk_bf16_f32 v104, v48, v49\n"
    "v_cvt_pk_bf16_f32 v105, v50, v51\n"
    "v_cvt_pk_bf16_f32 v106, v52, v53\n"
    "v_cvt_pk_bf16_f32 v107, v54, v55\n"
    "v_max3_f32 v56, v88, v89, v90\n"
    "v_max3_f32 v57, v91, v92, v93\n"
    "v_max3_f32 v56, v56, v94, v95\n"
    "v_max_f32 v56, v56, v57\n"
    "v_mov_b32 v57, v56\n"
    "s_nop 1\n"
    "v_permlane32_swap_b32 v56, v57\n"
    "v_max_f32 v56, v56, v57\n"
    "v_mov_b32 v57, v56\n"
    "s_nop 1\n"
    "v_permlane16_swap_b32 v56, v57\n"
    "v_max_f32 v56, v56, v57\n"
    "v_max_f32 v56, 0, v56\n"
    "v_sub_f32 v58, 0, v56\n"
    "v_exp_f32 v58, v58\n"
    "v_sub_f32 v124, v124, v56\n"
    "v_sub_f32 v125, v125, v56\n"
    "v_sub_f32 v126, v126, v56\n"
    "v_sub_f32 v127, v127, v56\n"
    "v_sub_f32 v72, v72, v56\n"
    "v_sub_f32 v73, v73, v56\n"
    "v_sub_f32 v74, v74, v56\n"
    "v_sub_f32 v75, v75, v56\n"
    "v_sub_f32 v76, v76, v56\n"
    "v_sub_f32 v77, v77, v56\n"
    "v_sub_f32 v78, v78, v56\n"
    "v_sub_f32 v79, v79, v56\n"
    "v_sub_f32 v88, v88, v56\n"
    "v_sub_f32 v89, v89, v56\n"
    "v_sub_f32 v90, v90, v56\n"
    "v_sub_f32 v91, v91, v56\n"
    "v_sub_f32 v92, v92, v56\n"
    "v_sub_f32 v93, v93, v56\n"
    "v_sub_f32 v94, v94, v56\n"
    "v_sub_f32 v95, v95, v56\n"
    "v_mul_f32 v41, v41, v58\n"
    "v_mul_f32 v136, v136, v58\n"
    "v_mul_f32 v137, v137, v58\n"
    "v_mul_f32 v138, v138, v58\n"
    "v_mul_f32 v139, v139, v58\n"
    "v_mul_f32 v140, v140, v58\n"
    "v_mul_f32 v141, v141, v58\n"
    "v_mul_f32 v142, v142, v58\n"
    "v_mul_f32 v143, v143, v58\n"
    "v_mul_f32 v152, v152, v58\n"
    "v_mul_f32 v153, v153, v58\n"
    "v_mul_f32 v154, v154, v58\n"
    "v_mul_f32 v155, v155, v58\n"
    "v_mul_f32 v156, v156, v58\n"
    "v_mul_f32 v157, v157, v58\n"
    "v_mul_f32 v158, v158, v58\n"
    "v_mul_f32 v159, v159, v58\n"
    "v_mul_f32 v168, v168, v58\n"
    "v_mul_f32 v169, v169, v58\n"
    "v_mul_f32 v170, v170, v58\n"
    "v_mul_f32 v171, v171, v58\n"
    "v_mul_f32 v172, v172, v58\n"
    "v_mul_f32 v173, v173, v58\n"
    "v_mul_f32 v174, v174, v58\n"
    "v_mul_f32 v175, v175, v58\n"
    "v_mul_f32 v184, v184, v58\n"
    "v_mul_f32 v185, v185, v58\n"
    "v_mul_f32 v186, v186, v58\n"
    "v_mul_f32 v187, v187, v58\n"
    "v_mul_f32 v188, v188, v58\n"
    "v_mul_f32 v189, v189, v58\n"
    "v_mul_f32 v190, v190, v58\n"
    "v_mul_f32 v191, v191, v58\n"
    "v_mul_f32 v200, v200, v58\n"
    "v_mul_f32 v201, v201, v58\n"
    "v_mul_f32 v202, v202, v58\n"
    "v_mul_f32 v203, v203, v58\n"
    "v_mul_f32 v204, v204, v58\n"
    "v_mul_f32 v205, v205, v58\n"
    "v_mul_f32 v206, v206, v58\n"
    "v_mul_f32 v207, v207, v58\n"
    "v_mul_f32 v216, v216, v58\n"
    "v_mul_f32 v217, v217, v58\n"
    "v_mul_f32 v218, v218, v58\n"
    "v_mul_f32 v219, v219, v58\n"
    "v_mul_f32 v220, v220, v58\n"
    "v_mul_f32 v221, v221, v58\n"
    "v_mul_f32 v222, v222, v58\n"
    "v_mul_f32 v223, v223, v58\n"
    "v_mul_f32 v232, v232, v58\n"
    "v_mul_f32 v233, v233, v58\n"
    "v_mul_f32 v234, v234, v58\n"
    "v_mul_f32 v235, v235, v58\n"
    "v_mul_f32 v236, v236, v58\n"
    "v_mul_f32 v237, v237, v58\n"
    "v_mul_f32 v238, v238, v58\n"
    "v_mul_f32 v239, v239, v58\n"
    "v_mul_f32 v248, v248, v58\n"
    "v_mul_f32 v249, v249, v58\n"
    "v_mul_f32 v250, v250, v58\n"
    "v_mul_f32 v251, v251, v58\n"
    "v_mul_f32 v252, v252, v58\n"
    "v_mul_f32 v253, v253, v58\n"
    "v_mul_f32 v254, v254, v58\n"
    "v_mul_f32 v255, v255, v58\n"
    "v_exp_f32 v48, v88\n"
    "v_exp_f32 v49, v89\n"
    "v_exp_f32 v50, v90\n"
    "v_exp_f32 v51, v91\n"
    "v_exp_f32 v52, v92\n"
    "v_exp_f32 v53, v93\n"
    "v_exp_f32 v54, v94\n"
    "v_exp_f32 v55, v95\n"
    "s_nop 0\n"
    "v_add_f32 v43, v48, v49\n"
    "v_add_f32 v43, v43, v50\n"
    "v_add_f32 v43, v43, v51\n"
    "v_add_f32 v43, v43, v52\n"
    "v_add_f32 v43, v43, v53\n"
    "v_add_f32 v43, v43, v54\n"
    "v_add_f32 v43, v43, v55\n"
    "v_cvt_pk_bf16_f32 v108, v48, v49\n"
    "v_cvt_pk_bf16_f32 v109, v50, v51\n"
    "v_cvt_pk_bf16_f32 v110, v52, v53\n"
    "v_cvt_pk_bf16_f32 v111, v54, v55\n"
    "s_nop 3\n"
    "s_setpc_b64 s[60:61]\n"
    "L_first:\n"
    "s_nop 15\n"
    "v_max3_f32 v56, v64, v65, v66\n"
    "v_max3_f32 v57, v67, v68, v69\n"
    "v_max3_f32 v56, v56, v70, v71\n"
    "v_max_f32 v56, v56, v57\n"
    "v_mov_b32 v57, v56\n"
    "s_nop 1\n"
    "v_permlane32_swap_b32 v56, v57\n"
    "v_max_f32 v56, v56, v57\n"
    "v_mov_b32 v57, v56\n"
    "s_nop 1\n"
    "v_permlane16_swap_b32 v56, v57\n"
    "v_max_f32 v56, v56, v57\n"
    "v_sub_f32 v120, v120, v56\n"
    "v_sub_f32 v121, v121, v56\n"
    "v_sub_f32 v122, v122, v56\n"
    "v_sub_f32 v123, v123, v56\n"
    "v_sub_f32 v64, v64, v56\n"
    "v_sub_f32 v65, v65, v56\n"
    "v_sub_f32 v66, v66, v56\n"
    "v_sub_f32 v67, v67, v56\n"
    "v_sub_f32 v68, v68, v56\n"
    "v_sub_f32 v69, v69, v56\n"
    "v_sub_f32 v70, v70, v56\n"
    "v_sub_f32 v71, v71, v56\n"
    "v_sub_f32 v80, v80, v56\n"
    "v_sub_f32 v81, v81, v56\n"
    "v_sub_f32 v82, v82, v56\n"
    "v_sub_f32 v83, v83, v56\n"
    "v_sub_f32 v84, v84, v56\n"
    "v_sub_f32 v85, v85, v56\n"
    "v_sub_f32 v86, v86, v56\n"
    "v_sub_f32 v87, v87, v56\n"
    "v_exp_f32 v48, v64\n"
    "v_exp_f32 v49, v65\n"
    "v_exp_f32 v50, v66\n"
    "v_exp_f32 v51, v67\n"
    "v_exp_f32 v52, v68\n"
    "v_exp_f32 v53, v69\n"
    "v_exp_f32 v54, v70\n"
    "v_exp_f32 v55, v71\n"
    "s_nop 0\n"
    "v_add_f32 v42, v48, v49\n"
    "v_add_f32 v42, v42, v50\n"
    "v_add_f32 v42, v42, v51\n"
    "v_add_f32 v42, v42, v52\n"
    "v_add_f32 v42, v42, v53\n"
    "v_add_f32 v42, v42, v54\n"
    "v_add_f32 v42, v42, v55\n"
    "v_cvt_pk_bf16_f32 v96, v48, v49\n"
    "v_cvt_pk_bf16_f32 v97, v50, v51\n"
    "v_cvt_pk_bf16_f32 v98, v52, v53\n"
    "v_cvt_pk_bf16_f32 v99, v54, v55\n"
    "v_max3_f32 v56, v72, v73, v74\n"
    "v_max3_f32 v57, v75, v76, v77\n"
    "v_max3_f32 v56, v56, v78, v79\n"
    "v_max_f32 v56, v56, v57\n"
    "v_mov_b32 v57, v56\n"
    "s_nop 1\n"
    "v_permlane32_swap_b32 v56, v57\n"
    "v_max_f32 v56, v56, v57\n"
    "v_mov_b32 v57, v56\n"
    "s_nop 1\n"
    "v_permlane16_swap_b32 v56, v57\n"
    "v_max_f32 v56, v56, v57\n"
    "v_sub_f32 v124, v124, v56\n"
    "v_sub_f32 v125, v125, v56\n"
    "v_sub_f32 v126, v126, v56\n"
    "v_sub_f32 v127, v127, v56\n"
    "v_sub_f32 v72, v72, v56\n"
    "v_sub_f32 v73, v73, v56\n"
    "v_sub_f32 v74, v74, v56\n"
    "v_sub_f32 v75, v75, v56\n"
    "v_sub_f32 v76, v76, v56\n"
    "v_sub_f32 v77, v77, v56\n"
    "v_sub_f32 v78, v78, v56\n"
    "v_sub_f32 v79, v79, v56\n"
    "v_sub_f32 v88, v88, v56\n"
    "v_sub_f32 v89, v89, v56\n"
    "v_sub_f32 v90, v90, v56\n"
    "v_sub_f32 v91, v91, v56\n"
    "v_sub_f32 v92, v92, v56\n"
    "v_sub_f32 v93, v93, v56\n"
    "v_sub_f32 v94, v94, v56\n"
    "v_sub_f32 v95, v95, v56\n"
    "v_exp_f32 v48, v72\n"
    "v_exp_f32 v49, v73\n"
    "v_exp_f32 v50, v74\n"
    "v_exp_f32 v51, v75\n"
    "v_exp_f32 v52, v76\n"
    "v_exp_f32 v53, v77\n"
    "v_exp_f32 v54, v78\n"
    "v_exp_f32 v55, v79\n"
    "s_nop 0\n"
    "v_add_f32 v43, v48, v49\n"
    "v_add_f32 v43, v43, v50\n"
    "v_add_f32 v43, v43, v51\n"
    "v_add_f32 v43, v43, v52\n"
    "v_add_f32 v43, v43, v53\n"
    "v_add_f32 v43, v43, v54\n"
    "v_add_f32 v43, v43, v55\n"
    "v_cvt_pk_bf16_f32 v100, v48, v49\n"
    "v_cvt_pk_bf16_f32 v101, v50, v51\n"
    "v_cvt_pk_bf16_f32 v102, v52, v53\n"
    "v_cvt_pk_bf16_f32 v103, v54, v55\n"
    "s_nop 3\n"
    "s_branch L_firstret\n"
    "L_end:\n"
    ".p2align 12\n"
    :
    : [ka] "s"(__builtin_amdgcn_kernarg_segment_ptr()), [blk] "s"(blockIdx.x), [tid] "v"(threadIdx.x), [lds] "s"((unsigned)(size_t)lds)
    : "v1", "v2", "v3", "v4", "v5", "v6", "v7", "v8", "v9", "v10", "v11", "v12", "v13", "v14", "v15", "v16", "v17", "v18", "v19", "v20", "v21", "v22", "v23", "v24", "v25", "v26", "v27", "v28", "v29", "v30", "v31", "v32", "v33", "v34", "v35", "v36", "v37", "v38", "v39", "v40", "v41", "v42", "v43", "v44", "v45", "v46", "v47", "v48", "v49", "v50", "v51", "v52", "v53", "v54", "v55", "v56", "v57", "v58", "v59", "v60", "v61", "v62", "v63", "v64", "v65", "v66", "v67", "v68", "v69", "v70", "v71", "v72", "v73", "v74", "v75", "v76", "v77", "v78", "v79", "v80", "v81", "v82", "v83", "v84", "v85", "v86", "v87", "v88", "v89", "v90", "v91", "v92", "v93", "v94", "v95", "v96", "v97", "v98", "v99", "v100", "v101", "v102", "v103", "v104", "v105", "v106", "v107", "v108", "v109", "v110", "v111", "v112", "v113", "v114", "v115", "v116", "v117", "v118", "v119", "v120", "v121", "v122", "v123", "v124", "v125", "v126", "v127", "v128", "v129", "v130", "v131", "v132", "v133", "v134", "v135", "v136", "v137", "v138", "v139", "v140", "v141", "v142", "v143", "v144", "v145", "v146", "v147", "v148", "v149", "v150", "v151", "v152", "v153", "v154", "v155", "v156", "v157", "v158", "v159", "v160", "v161", "v162", "v163", "v164", "v165", "v166", "v167", "v168", "v169", "v170", "v171", "v172", "v173", "v174", "v175", "v176", "v177", "v178", "v179", "v180", "v181", "v182", "v183", "v184", "v185", "v186", "v187", "v188", "v189", "v190", "v191", "v192", "v193", "v194", "v195", "v196", "v197", "v198", "v199", "v200", "v201", "v202", "v203", "v204", "v205", "v206", "v207", "v208", "v209", "v210", "v211", "v212", "v213", "v214", "v215", "v216", "v217", "v218", "v219", "v220", "v221", "v222", "v223", "v224", "v225", "v226", "v227", "v228", "v229", "v230", "v231", "v232", "v233", "v234", "v235", "v236", "v237", "v238", "v239", "v240", "v241", "v242", "v243", "v244", "v245", "v246", "v247", "v248", "v249", "v250", "v251", "v252", "v253", "v254", "v255", "a0", "a1", "a2", "a3", "a4", "a5", "a6", "a7", "a8", "a9", "a10", "a11", "a12", "a13", "a14", "a15", "a16", "a17", "a18", "a19", "a20", "a21", "a22", "a23", "a24", "a25", "a26", "a27", "a28", "a29", "a30", "a31", "a32", "a33", "a34", "a35", "a36", "a37", "a38", "a39", "a40", "a41", "a42", "a43", "a44", "a45", "a46", "a47", "a48", "a49", "a50", "a51", "a52", "a53", "a54", "a55", "a56", "a57", "a58", "a59", "a60", "a61", "a62", "a63", "a64", "a65", "a66", "a67", "a68", "a69", "a70", "a71", "a72", "a73", "a74", "a75", "a76", "a77", "a78", "a79", "a80", "a81", "a82", "a83", "a84", "a85", "a86", "a87", "a88", "a89", "a90", "a91", "a92", "a93", "a94", "a95", "a96", "a97", "a98", "a99", "a100", "a101", "a102", "a103", "a104", "a105", "a106", "a107", "a108", "a109", "a110", "a111", "a112", "a113", "a114", "a115", "a116", "a117", "a118", "a119", "a120", "a121", "a122", "a123", "a124", "a125", "a126", "a127", "a128", "a129", "a130", "a131", "a132", "a133", "a134", "a135", "a136", "a137", "a138", "a139", "a140", "a141", "a142", "a143", "a144", "a145", "a146", "a147", "a148", "a149", "a150", "a151", "a152", "a153", "a154", "a155", "a156", "a157", "a158", "a159", "a160", "a161", "a162", "a163", "a164", "a165", "a166", "a167", "a168", "a169", "a170", "a171", "a172", "a173", "a174", "a175", "a176", "a177", "a178", "a179", "a180", "a181", "a182", "a183", "a184", "a185", "a186", "a187", "a188", "a189", "a190", "a191", "a192", "a193", "a194", "a195", "a196", "a197", "a198", "a199", "a200", "a201", "a202", "a203", "a204", "a205", "a206", "a207", "a208", "a209", "a210", "a211", "a212", "a213", "a214", "a215", "a216", "a217", "a218", "a219", "a220", "a221", "a222", "a223", "a224", "a225", "a226", "a227", "a228", "a229", "a230", "a231", "a232", "a233", "a234", "a235", "a236", "a237", "a238", "a239", "a240", "a241", "a242", "a243", "a244", "a245", "a246", "a247", "a248", "a249", "a250", "a251", "a252", "a253", "a254", "a255", "s4", "s5", "s6", "s7", "s8", "s9", "s10", "s11", "s12", "s13", "s14", "s15", "s16", "s17", "s18", "s19", "s20", "s21", "s22", "s23", "s24", "s25", "s26", "s27", "s28", "s29", "s30", "s31", "s36", "s37", "s38", "s39", "s40", "s41", "s42", "s43", "s44", "s45", "s46", "s47", "s48", "s49", "s50", "s51", "s52", "s53", "s54", "s55", "s56", "s57", "s58", "s59", "s60", "s61", "s62", "s63", "s64", "s65", "s66", "s67", "s68", "s69", "s70", "s71", "s72", "s73", "s74", "s75", "s76", "s77", "s78", "s79", "s80", "s81", "s82", "s83", "s84", "s85", "s86", "s87", "s88", "s89", "s90", "s91", "s92", "s93", "s94", "s95", "s96", "s97", "s98", "s99", "vcc", "scc", "memory");
}

extern "C" void kernel_launch(void* const* d_in, const int* in_sizes, int n_in, void* d_out, int out_size, void* d_ws, size_t ws_size,
                              hipStream_t stream) {
  mha_fused<<<256, 256, 0, stream>>>((const char*)d_in[0], (const char*)d_in[1], (const char*)d_in[2], (const char*)d_in[3],
                                     (const char*)d_in[4], (char*)d_ws, (char*)d_out, (char*)d_ws);
}
```
